```python
import torch

def _softmax(x, axis=-1):
    return torch.softmax(x, dim=axis)

def _gelu(x):
    return torch.nn.functional.gelu(x, approximate='tanh')

def _silu(x):
    return torch.nn.functional.silu(x)

def _sigmoid(x):
    return torch.sigmoid(x)

def _layernorm(x, axis=-1, eps=1e-05):
    m = x.mean(dim=axis, keepdim=True)
    v = x.var(dim=axis, keepdim=True, unbiased=False)
    return (x - m) / torch.sqrt(v + eps)

def _scatter_add(tgt, idx, src, axis=0):
    return tgt.clone().index_add_(axis, idx, src)

def _segment_sum(src, seg, n):
    out = torch.zeros((n,) + src.shape[1:], dtype=src.dtype)
    return out.index_add_(0, seg, src)

def _pad(x, pw):
    flat = tuple((v for pair in reversed(pw) for v in pair))
    return torch.nn.functional.pad(x, flat)
kernel_name = 'adaLN_cond__compressed_kv_cla__mixer_channel_mlp__decode'

def setup_inputs():

    _w = lambda a: (lambda t: t.to(torch.bfloat16) if t.is_floating_point() else t)(torch.as_tensor(a))

    def _A_setup():
        B, H, Sq, Sk, D, M = (1, 32, 1, 4096, 128, 512)
        Dc = 4 * D
        return {'x': _w((lambda *s: torch.randn(*s, dtype=torch.bfloat16) * 0.1)(B, Sq, M)), 'c_kv': _w((lambda *s: torch.randn(*s, dtype=torch.bfloat16) * 0.8)(B, Sk, Dc)), 'w_q': _w((lambda *s: torch.randn(*s, dtype=torch.bfloat16) * 0.8)(M, H, D) * M ** (-0.5)), 'w_uk': _w((lambda *s: torch.randn(*s, dtype=torch.bfloat16) * 0.8)(Dc, H, D) * Dc ** (-0.5)), 'w_uv': _w((lambda *s: torch.randn(*s, dtype=torch.bfloat16) * 0.8)(Dc, H, D) * Dc ** (-0.5)), 'w_o': _w((lambda *s: torch.randn(*s, dtype=torch.bfloat16) * 0.8)(H, D, M) * (H * D) ** (-0.5))}
    _A_in = _A_setup()

    def _B_setup():
        B, S, D, F = (1, 1, 512, 1376)
        return {'x': _w((lambda *s: torch.randn(*s, dtype=torch.bfloat16) * 0.1)(B, S, D)), 'w1': _w((lambda *s: torch.randn(*s, dtype=torch.bfloat16) * 3.2)(D, F) * D ** (-0.5)), 'b1': _w((lambda *s: torch.randn(*s, dtype=torch.bfloat16) * 3.2)(F)), 'w2': _w((lambda *s: torch.randn(*s, dtype=torch.bfloat16) * 3.2)(F, D) * F ** (-0.5)), 'b2': _w((lambda *s: torch.randn(*s, dtype=torch.bfloat16) * 3.2)(D))}
    _B_in = _B_setup()
    return {'A_x': _A_in['x'], 'A_c_kv': _A_in['c_kv'], 'A_w_q': _A_in['w_q'], 'A_w_uk': _A_in['w_uk'], 'A_w_uv': _A_in['w_uv'], 'A_w_o': _A_in['w_o'], 'B_x': _B_in['x'], 'B_w1': _B_in['w1'], 'B_b1': _B_in['b1'], 'B_w2': _B_in['w2'], 'B_b2': _B_in['b2'], 'c': _w((lambda *s: torch.randn(*s, dtype=torch.bfloat16) * 0.4)(1, 512) * 8.0), 'w_mod': _w((lambda *s: torch.randn(*s, dtype=torch.bfloat16) * 0.4)(512, 6 * 512) * 512 ** (-0.5) * 8.0)}

def reference(A_x, A_c_kv, A_w_q, A_w_uk, A_w_uv, A_w_o, B_x, B_w1, B_b1, B_w2, B_b2, c, w_mod):

    def _A_ref(x, c_kv, w_q, w_uk, w_uv, w_o):
        B, Sq, M = (x.shape[0], x.shape[1], x.shape[2])
        Sk = c_kv.shape[1]
        H, D = (w_q.shape[1], w_q.shape[2])
        q = torch.einsum('bqm,mhd->bhqd', x, w_q)
        k = torch.einsum('bkc,chd->bhkd', c_kv, w_uk)
        v = torch.einsum('bkc,chd->bhkd', c_kv, w_uv)
        scores = torch.einsum('bhqd,bhkd->bhqk', q, k) * D ** (-0.5)
        off = Sk - Sq
        qi = torch.arange(Sq)[:, None]
        kj = torch.arange(Sk)[None, :]
        cmask = kj <= qi + off
        scores = torch.where(cmask[None, None], scores, -1000000000.0)
        p = _softmax(scores, axis=-1)
        ctx = torch.einsum('bhqk,bhkd->bhqd', p, v)
        return torch.einsum('bhqd,hdm->bqm', ctx, w_o)

    def _B_ref(x, w1, b1, w2, b2):
        y = _layernorm(x, axis=-1)
        h = _gelu(torch.einsum('bsd,df->bsf', y, w1) + b1)
        return torch.einsum('bsf,fd->bsd', h, w2) + b2
    x = A_x
    mod = (_silu(c) @ w_mod).reshape(c.shape[0], 6, 1, -1)
    sa, ca, ga, sb, cb, gb = (mod[:, 0].clone(), mod[:, 1].clone(), mod[:, 2].clone(), mod[:, 3].clone(), mod[:, 4].clone(), mod[:, 5].clone())
    h = _layernorm(x, axis=-1) * (1 + ca) + sa
    x = x + ga * _A_ref(h, A_c_kv, A_w_q, A_w_uk, A_w_uv, A_w_o)
    h = _layernorm(x, axis=-1) * (1 + cb) + sb
    x = x + gb * _B_ref(h, B_w1, B_b1, B_w2, B_b2)
    return x
```

```cpp
#include <hip/hip_runtime.h>
#include <hip/hip_bf16.h>
typedef unsigned short u16;
__device__ __forceinline__ float bf2f(u16 b){ return __uint_as_float(((unsigned)b)<<16); }
__device__ __forceinline__ u16 f2bf(float f){ unsigned u=__float_as_uint(f); return (u16)((u+0x7fffu+((u>>16)&1u))>>16); }
__device__ __forceinline__ float rbf(float f){ return bf2f(f2bf(f)); }

__device__ float block_sum(float v, float* sm){
  for(int o=32;o>0;o>>=1) v+=__shfl_xor(v,o);
  int w=threadIdx.x>>6, nw=blockDim.x>>6;
  __syncthreads();
  if((threadIdx.x&63)==0) sm[w]=v;
  __syncthreads();
  float s=0; for(int i=0;i<nw;i++) s+=sm[i];
  return s;
}
__global__ void k_mod(const u16* c,const u16* w_mod,float* mod){
  int j=blockIdx.x*64+(threadIdx.x&63); int part=threadIdx.x>>6;
  __shared__ float sm[256];
  float a=0;
  for(int k=part*128;k<part*128+128;k++){ float cv=bf2f(c[k]); float s=rbf(cv/(1.f+__expf(-cv))); a+=s*bf2f(w_mod[k*3072+j]); }
  sm[threadIdx.x]=a; __syncthreads();
  if(threadIdx.x<64) mod[j]=rbf(sm[threadIdx.x]+sm[threadIdx.x+64]+sm[threadIdx.x+128]+sm[threadIdx.x+192]);
}
__global__ void k_q(const u16* x,const float* mod,const u16* w_q,float* q){
  __shared__ float hs[512]; __shared__ float sm[16]; __shared__ float sp[512];
  int t=threadIdx.x;
  float xv=bf2f(x[t]);
  float m=block_sum(xv,sm)/512.f; float d=xv-m; float v=block_sum(d*d,sm)/512.f;
  float ln=rbf(d*rsqrtf(v+1e-5f));
  hs[t]=rbf(rbf(ln*rbf(1.f+mod[512+t]))+mod[t]);
  __syncthreads();
  int j=blockIdx.x*64+(t&63); int part=t>>6; float a=0;
  for(int k=part*64;k<part*64+64;k++) a+=hs[k]*bf2f(w_q[k*4096+j]);
  sp[t]=a; __syncthreads();
  if(t<64){ float s=0; for(int p=0;p<8;p++) s+=sp[p*64+t]; q[j]=rbf(s);}
}
__global__ void k_qabs(const float* q,const u16* w_uk,float* qabs){
  int h=blockIdx.x>>3, c0=(blockIdx.x&7)*64; int lane=threadIdx.x&63, w=threadIdx.x>>6;
  for(int c=c0+w;c<c0+64;c+=4){
    const u16* p=w_uk+(size_t)c*4096+h*128;
    float a=q[h*128+lane*2]*bf2f(p[lane*2])+q[h*128+lane*2+1]*bf2f(p[lane*2+1]);
    for(int o=32;o>0;o>>=1) a+=__shfl_xor(a,o);
    if(lane==0) qabs[h*512+c]=a;
  }
}
__global__ void __launch_bounds__(256) k_attn(const float* qabs,const u16* ckv,float* pctx,float* pml){
  int h=blockIdx.x>>5, sp=blockIdx.x&31; int t=threadIdx.x, lane=t&63,w=t>>6;
  __shared__ float sc[128]; __shared__ float qs[512]; __shared__ float sm[16];
  qs[t]=qabs[h*512+t]; qs[t+256]=qabs[h*512+t+256]; __syncthreads();
  for(int k=w;k<128;k+=4){
    const u16* p=ckv+(size_t)(sp*128+k)*512; float a=0;
    for(int i=0;i<8;i++) a+=qs[lane*8+i]*bf2f(p[lane*8+i]);
    for(int o=32;o>0;o>>=1) a+=__shfl_xor(a,o);
    if(lane==0) sc[k]=a*0.08838834764831845f;
  }
  __syncthreads();
  float m=-1e30f; for(int k=0;k<128;k++) m=fmaxf(m,sc[k]);
  __syncthreads();
  if(t<128) sc[t]=__expf(sc[t]-m);
  __syncthreads();
  float l=0; for(int k=0;k<128;k++) l+=sc[k];
  float a0=0,a1=0;
  for(int k=0;k<128;k++){ const u16* p=ckv+(size_t)(sp*128+k)*512; a0+=sc[k]*bf2f(p[t]); a1+=sc[k]*bf2f(p[t+256]); }
  pctx[(size_t)blockIdx.x*512+t]=a0; pctx[(size_t)blockIdx.x*512+t+256]=a1;
  if(t==0){ pml[blockIdx.x*2]=m; pml[blockIdx.x*2+1]=l; }
}
__global__ void k_ctx(const float* pctx,const float* pml,const u16* w_uv,float* ctx){
  int h=blockIdx.x,t=threadIdx.x; __shared__ float cs[512]; __shared__ float sp[512];
  float m=-1e30f; for(int s=0;s<32;s++) m=fmaxf(m,pml[(h*32+s)*2]);
  float l=0,a=0; for(int s=0;s<32;s++){ float e=__expf(pml[(h*32+s)*2]-m); l+=e*pml[(h*32+s)*2+1]; a+=e*pctx[(size_t)(h*32+s)*512+t]; }
  cs[t]=a/l; __syncthreads();
  int d=t&127, part=t>>7; float r=0;
  for(int c=part*128;c<part*128+128;c++) r+=cs[c]*bf2f(w_uv[(size_t)c*4096+h*128+d]);
  sp[t]=r; __syncthreads();
  if(t<128) ctx[h*128+t]=rbf(sp[t]+sp[t+128]+sp[t+256]+sp[t+384]);
}
__global__ void k_o(const float* ctx,const u16* w_o,const u16* x,const float* mod,float* xn){
  int t=threadIdx.x; int j=blockIdx.x*64+(t&63), part=t>>6; __shared__ float sp[1024]; float a=0;
  for(int k=part*256;k<part*256+256;k++) a+=ctx[k]*bf2f(w_o[(size_t)k*512+j]);
  sp[t]=a; __syncthreads();
  if(t<64){ float s=0; for(int p=0;p<16;p++) s+=sp[p*64+t]; s=rbf(s); xn[j]=rbf(bf2f(x[j])+rbf(mod[1024+j]*s)); }
}
__global__ void k_h1(const float* xn,const float* mod,const u16* w1,const u16* b1,float* h1){
  __shared__ float ys[512]; __shared__ float sm[16]; __shared__ float sp[512];
  int t=threadIdx.x; float xv=xn[t];
  float m=block_sum(xv,sm)/512.f; float d=xv-m; float v=block_sum(d*d,sm)/512.f;
  float ln=rbf(d*rsqrtf(v+1e-5f));
  float hv=rbf(rbf(ln*rbf(1.f+mod[2048+t]))+mod[1536+t]);
  m=block_sum(hv,sm)/512.f; d=hv-m; v=block_sum(d*d,sm)/512.f;
  ys[t]=rbf(d*rsqrtf(v+1e-5f)); __syncthreads();
  int j=blockIdx.x*32+(t&31), part=t>>5; float a=0;
  for(int k=part*32;k<part*32+32;k++) a+=ys[k]*bf2f(w1[(size_t)k*1376+j]);
  sp[t]=a; __syncthreads();
  if(t<32){ float s=0; for(int p=0;p<16;p++) s+=sp[p*32+t]; s=rbf(rbf(s)+bf2f(b1[j]));
    float g=0.5f*s*(1.f+tanhf(0.7978845608028654f*(s+0.044715f*s*s*s))); h1[j]=rbf(g);}
}
__global__ void k_out(const float* h1,const u16* w2,const u16* b2,const float* xn,const float* mod,u16* out){
  int t=threadIdx.x; int j=blockIdx.x*64+(t&63), part=t>>6; __shared__ float sp[1024]; float a=0;
  for(int k=part*86;k<part*86+86;k++) a+=h1[k]*bf2f(w2[(size_t)k*512+j]);
  sp[t]=a; __syncthreads();
  if(t<64){ float s=0; for(int p=0;p<16;p++) s+=sp[p*64+t]; s=rbf(rbf(s)+bf2f(b2[j])); out[j]=f2bf(xn[j]+rbf(mod[2560+j]*s)); }
}
extern "C" void kernel_launch(void* const* d_in, const int* in_sizes, int n_in, void* d_out, int out_size, void* d_ws, size_t ws_size, hipStream_t stream) {
  const u16* x=(const u16*)d_in[0]; const u16* ckv=(const u16*)d_in[1]; const u16* w_q=(const u16*)d_in[2]; const u16* w_uk=(const u16*)d_in[3];
  const u16* w_uv=(const u16*)d_in[4]; const u16* w_o=(const u16*)d_in[5]; const u16* w1=(const u16*)d_in[7]; const u16* b1=(const u16*)d_in[8];
  const u16* w2=(const u16*)d_in[9]; const u16* b2=(const u16*)d_in[10]; const u16* c=(const u16*)d_in[11]; const u16* w_mod=(const u16*)d_in[12];
  float* ws=(float*)d_ws; float* mod=ws; float* q=ws+4096; float* qabs=ws+8192; float* pctx=ws+32768; float* pml=pctx+1024*512; float* ctx=pml+4096; float* xn=ctx+4096; float* h1=xn+512;
  k_mod<<<48,256,0,stream>>>(c,w_mod,mod);
  k_q<<<64,512,0,stream>>>(x,mod,w_q,q);
  k_qabs<<<256,256,0,stream>>>(q,w_uk,qabs);
  k_attn<<<1024,256,0,stream>>>(qabs,ckv,pctx,pml);
  k_ctx<<<32,512,0,stream>>>(pctx,pml,w_uv,ctx);
  k_o<<<8,1024,0,stream>>>(ctx,w_o,x,mod,xn);
  k_h1<<<43,512,0,stream>>>(xn,mod,w1,b1,h1);
  k_out<<<8,1024,0,stream>>>(h1,w2,b2,xn,mod,(u16*)d_out);
}
```

```cpp
#include <hip/hip_runtime.h>
#include <stdint.h>

typedef unsigned short u16;
typedef unsigned v4u __attribute__((ext_vector_type(4)));
typedef unsigned v2u __attribute__((ext_vector_type(2)));
typedef float f32x4 __attribute__((ext_vector_type(4)));
typedef float f32x2 __attribute__((ext_vector_type(2)));
typedef short bf16x8 __attribute__((ext_vector_type(8)));
typedef short s16x4 __attribute__((ext_vector_type(4)));
typedef __bf16 bf2 __attribute__((ext_vector_type(2)));
#define LAS __attribute__((address_space(3)))

constexpr unsigned SENT = 0xFFFFFFFFu;
constexpr int NT = 512;
constexpr int SPIN_CAP = 1 << 20;
constexpr int S1_OFF = 0, S1_BYTES = 4096;
constexpr int CLR_OFF = 65536;
constexpr int S2_OFF = CLR_OFF;
constexpr int S5_OFF = S2_OFF + 32768;
constexpr int SL_OFF = S5_OFF + 32768;
constexpr int S6_OFF = SL_OFF + 4096;
constexpr int ST_OFF = S6_OFF + 4096;
constexpr int S3_OFF = ST_OFF + 8192;
constexpr int CLR_END = S3_OFF + (1 << 20);
constexpr int CLR_PIECES = (CLR_END - CLR_OFF) / 16 / 64;
constexpr int WS_USED = CLR_END;
#define STAMP(i) do {} while (0)

constexpr int L_CKV = 0;
constexpr int L_SM = 131072;
constexpr int L_TOTAL = 131072 + 20480;

__device__ __forceinline__ float bflo(unsigned w) { return __uint_as_float(w << 16); }
__device__ __forceinline__ float bfhi(unsigned w) { return __uint_as_float(w & 0xffff0000u); }
__device__ __forceinline__ unsigned pk2(float a, float b) { bf2 p; p.x = (__bf16)a; p.y = (__bf16)b; return __builtin_bit_cast(unsigned, p); }
__device__ __forceinline__ float rbf(float a) { return (float)(__bf16)a; }
__device__ __forceinline__ unsigned canon(unsigned w) { return w == SENT ? 0x7FC07FC0u : w; }
__device__ __forceinline__ float dot2(unsigned a, unsigned b, float c) { return __builtin_amdgcn_fdot2_f32_bf16(__builtin_bit_cast(bf2, a), __builtin_bit_cast(bf2, b), c, false); }
__device__ __forceinline__ void bar() { asm volatile("s_waitcnt lgkmcnt(0)\n\ts_barrier" ::: "memory"); }
template <int CTRL> __device__ __forceinline__ float dppf(float v) { return __int_as_float(__builtin_amdgcn_update_dpp(0, __float_as_int(v), CTRL, 0xf, 0xf, false)); }
template <int CTRL> __device__ __forceinline__ unsigned dppu(unsigned v) { return (unsigned)__builtin_amdgcn_update_dpp(0, (int)v, CTRL, 0xf, 0xf, false); }
__device__ __forceinline__ float red32(float a, float b) {
  auto r = __builtin_amdgcn_permlane32_swap(__float_as_uint(a), __float_as_uint(b), false, false);
  return __uint_as_float(r[0]) + __uint_as_float(r[1]);
}
__device__ __forceinline__ float red16(float a, float b) {
  auto r = __builtin_amdgcn_permlane16_swap(__float_as_uint(a), __float_as_uint(b), false, false);
  return __uint_as_float(r[0]) + __uint_as_float(r[1]);
}
__device__ __forceinline__ float max32(float a) { auto r = __builtin_amdgcn_permlane32_swap(__float_as_uint(a), __float_as_uint(a), false, false); return fmaxf(__uint_as_float(r[0]), __uint_as_float(r[1])); }
__device__ __forceinline__ float max16(float a) { auto r = __builtin_amdgcn_permlane16_swap(__float_as_uint(a), __float_as_uint(a), false, false); return fmaxf(__uint_as_float(r[0]), __uint_as_float(r[1])); }
__device__ __forceinline__ float wave_sum(float v) {
  v += dppf<0x128>(v); v += dppf<0x124>(v); v += dppf<0x122>(v); v += dppf<0x121>(v);
  v = red16(v, v); v = red32(v, v); return v;
}
__device__ __forceinline__ float wave_max(float v) {
  v = fmaxf(v, dppf<0x128>(v)); v = fmaxf(v, dppf<0x124>(v)); v = fmaxf(v, dppf<0x122>(v)); v = fmaxf(v, dppf<0x121>(v));
  v = max16(v); v = max32(v); return v;
}
__device__ __forceinline__ unsigned ilo(unsigned a, unsigned b) { return __builtin_amdgcn_perm(b, a, 0x05040100u); }
__device__ __forceinline__ unsigned ihi(unsigned a, unsigned b) { return __builtin_amdgcn_perm(b, a, 0x07060302u); }
__device__ __forceinline__ void interleave(v4u& a, v4u& b) {
  v4u x, y;
  x.x = ilo(a.x, b.x); x.y = ihi(a.x, b.x); x.z = ilo(a.y, b.y); x.w = ihi(a.y, b.y);
  y.x = ilo(a.z, b.z); y.y = ihi(a.z, b.z); y.z = ilo(a.w, b.w); y.w = ihi(a.w, b.w);
  a = x; b = y;
}
__device__ __forceinline__ bool valid4(v4u v) { return (v.x != SENT) & (v.y != SENT) & (v.z != SENT) & (v.w != SENT); }
typedef __amdgpu_buffer_rsrc_t rsrc_t;
__device__ __forceinline__ v4u ld16(rsrc_t r, int off) { return __builtin_amdgcn_raw_buffer_load_b128(r, off, 0, 16); }
__device__ __forceinline__ v2u ld8(rsrc_t r, int off) { return __builtin_amdgcn_raw_buffer_load_b64(r, off, 0, 16); }
__device__ __forceinline__ void st16(rsrc_t r, int off, v4u v) { __builtin_amdgcn_raw_buffer_store_b128(v, r, off, 0, 16); }
__device__ __forceinline__ void st8(rsrc_t r, int off, v2u v) { __builtin_amdgcn_raw_buffer_store_b64(v, r, off, 0, 16); }
__device__ __forceinline__ void glds16(const void* g, unsigned lds_base) {
  unsigned sv; asm volatile("s_mov_b32 %0, m0\n\ts_mov_b32 m0, %2\n\ts_nop 0\n\tglobal_load_lds_dwordx4 %1, off\n\ts_mov_b32 m0, %0" : "=&s"(sv) : "v"(g), "s"(lds_base) : "memory");
}
#define CBAR() asm volatile("" ::: "memory")

__device__ __forceinline__ void gemv_part(const v4u (&w)[16], const LAS unsigned* vp, LAS float* part, int wave, int lane) {
  const int rsub = lane >> 4, dgrp = lane & 15;
  const LAS v4u* hp = (const LAS v4u*)(vp + wave * 32 + rsub * 8);
  v4u h0 = hp[0], h1 = hp[1];
  unsigned hv[8] = {h0.x, h0.y, h0.z, h0.w, h1.x, h1.y, h1.z, h1.w};
  float a[8] = {0, 0, 0, 0, 0, 0, 0, 0};
#pragma unroll
  for (int i = 0; i < 8; i++) {
    a[0] = dot2(w[2 * i].x, hv[i], a[0]); a[1] = dot2(w[2 * i].y, hv[i], a[1]); a[2] = dot2(w[2 * i].z, hv[i], a[2]); a[3] = dot2(w[2 * i].w, hv[i], a[3]);
    a[4] = dot2(w[2 * i + 1].x, hv[i], a[4]); a[5] = dot2(w[2 * i + 1].y, hv[i], a[5]); a[6] = dot2(w[2 * i + 1].z, hv[i], a[6]); a[7] = dot2(w[2 * i + 1].w, hv[i], a[7]);
  }
  float b0 = red32(a[0], a[4]), b1 = red32(a[1], a[5]), b2 = red32(a[2], a[6]), b3 = red32(a[3], a[7]);
  float c0 = red16(b0, b2), c1 = red16(b1, b3);
  const int d = 8 * dgrp + 4 * (lane >> 5) + 2 * ((lane >> 4) & 1);
  f32x2 o = {c0, c1};
  *(LAS f32x2*)(part + wave * 128 + d) = o;
}

__global__ void __launch_bounds__(NT, 2) fused_kernel(
    const u16* __restrict__ x, const u16* __restrict__ ckv, const u16* __restrict__ w_q, const u16* __restrict__ w_uk,
    const u16* __restrict__ w_uv, const u16* __restrict__ w_o, const u16* __restrict__ w1, const u16* __restrict__ b1,
    const u16* __restrict__ w2, const u16* __restrict__ b2, const u16* __restrict__ cvec, const u16* __restrict__ w_mod,
    u16* __restrict__ out, unsigned char* ws) {
  extern __shared__ __attribute__((aligned(16))) unsigned char lds_raw[];
  LAS unsigned char* L = (LAS unsigned char*)lds_raw;
  const int t = threadIdx.x, lane = t & 63, wave = __builtin_amdgcn_readfirstlane(t >> 6);
  const int b = blockIdx.x, xc = b & 7, r = b >> 3;
  const rsrc_t rs = __builtin_amdgcn_make_buffer_rsrc((void*)ws, 0, WS_USED, 0x00020000);
  STAMP(0);

  if (r < 16) {
    const int head = 4 * xc + (r >> 2), cs = r & 3;
    const int split = 4 * xc + (r & 3), hg = r >> 2;
    const int s1off = S1_OFF + xc * S1_BYTES + t * 16;
    v4u snap = {0, 0, 0, 0};
    if (t < 256) snap = ld16(rs, s1off);
    v4u xv = *(const v4u*)(x + lane * 8);
    unsigned xw = 0; if (t < 256) xw = ((const unsigned*)x)[t];
    v4u wq[16];
    {
      const u16* p = w_q + (size_t)(wave * 64 + (lane >> 4) * 16) * 4096 + head * 128 + (lane & 15) * 8;
#pragma unroll
      for (int i = 0; i < 16; i++) wq[i] = *(const v4u*)(p + (size_t)i * 4096);
    }
    v4u wk[4];
    {
      const u16* p = w_uk + (size_t)(cs * 128 + (t >> 2)) * 4096 + head * 128 + (t & 3) * 8;
#pragma unroll
      for (int i = 0; i < 4; i++) wk[i] = *(const v4u*)(p + i * 32);
    }
    CBAR();
    if (wave >= 4) {
#pragma unroll 4
      for (int i = 0; i < 32; i++) {
        const int key = (wave - 4) * 32 + i;
        const int f = (key & 3) | ((key >> 1) & 4);
        const u16* src = ckv + (size_t)(split * 128 + key) * 512 + ((lane ^ (f << 1)) * 8);
        glds16(src, (unsigned)(L_CKV + key * 1024));
      }
    }
    float xs[8] = {bflo(xv.x), bfhi(xv.x), bflo(xv.y), bfhi(xv.y), bflo(xv.z), bfhi(xv.z), bflo(xv.w), bfhi(xv.w)};
    float s = 0;
#pragma unroll
    for (int i = 0; i < 8; i++) s += xs[i];
    const float mean = wave_sum(s) * (1.f / 512.f);
    float vs = 0;
#pragma unroll
    for (int i = 0; i < 8; i++) { float d = xs[i] - mean; vs += d * d; }
    const float rstd = rsqrtf(wave_sum(vs) * (1.f / 512.f) + 1e-5f);
    const float ln0 = rbf((bflo(xw) - mean) * rstd), ln1 = rbf((bfhi(xw) - mean) * rstd);
#pragma unroll
    for (int i = 0; i < 8; i++) interleave(wq[2 * i], wq[2 * i + 1]);
    STAMP(1);
    LAS unsigned* hp = (LAS unsigned*)(L + L_SM);
    LAS float* qpart = (LAS float*)(L + L_SM + 1024);
    LAS unsigned* q16 = (LAS unsigned*)(L + L_SM + 1024 + 4096);
    LAS unsigned char* qa = L + L_SM + 5632;
    LAS unsigned char* Pm = qa + 8 * 1056;
    LAS float* wmax = (LAS float*)(Pm + 8 * 288);
    LAS float* wsum = wmax + 64;
    if (t < 256) {
      v4u v; int spins = 0;
      for (;;) {
        CBAR();
        v = ld16(rs, s1off);
        if (((v.y != snap.y) & (v.w != snap.w)) || ++spins > SPIN_CAP) break;
      }
      const bool odd = t & 1;
      const unsigned send = odd ? v.x : v.z;
      const unsigned recv = dppu<0xB1>(send);
      const unsigned sa = odd ? recv : v.x, ca = odd ? v.z : recv;
      const float h0 = rbf(ln0 * rbf(1.f + bflo(ca))) + bflo(sa), h1v = rbf(ln1 * rbf(1.f + bfhi(ca))) + bfhi(sa);
      hp[t] = pk2(h0, h1v);
    }
    bar();
    STAMP(2);
    gemv_part(wq, hp, qpart, wave, lane);
    bar();
    if (t < 64) {
      float a0 = 0, a1 = 0;
#pragma unroll
      for (int w = 0; w < 8; w++) { f32x2 v = *(LAS f32x2*)(qpart + w * 128 + 2 * t); a0 += v.x; a1 += v.y; }
      q16[t] = pk2(a0, a1);
    }
    bar();
    {
      float a = 0;
#pragma unroll
      for (int i = 0; i < 4; i++) {
        v4u qv = *(const LAS v4u*)(q16 + (t & 3) * 4 + i * 16);
        a = dot2(wk[i].x, qv.x, a); a = dot2(wk[i].y, qv.y, a); a = dot2(wk[i].z, qv.z, a); a = dot2(wk[i].w, qv.w, a);
      }
      a += dppf<0xB1>(a); a += dppf<0x4E>(a);
      a *= 0.08838834764831845f * 1.4426950408889634f;
      const float nb = dppf<0x104>(a);
      const unsigned w0 = canon(pk2(a, nb));
      const unsigned w1v = dppu<0x108>(w0);
      if ((lane & 15) == 0) { v2u o = {w0, w1v}; st8(rs, S2_OFF + head * 1024 + (cs * 128 + wave * 16 + (lane >> 4) * 4) * 2, o); }
    }
    STAMP(3);
    if (wave >= 4) asm volatile("s_waitcnt vmcnt(0)" ::: "memory");
    bar();
    STAMP(4);
    const int n = lane & 15, q = lane >> 4;
    bf16x8 ka[16];
    {
      const int key = wave * 16 + n, f2 = ((key & 3) | ((key >> 1) & 4)) << 1;
#pragma unroll
      for (int s2 = 0; s2 < 16; s2++) ka[s2] = *(const LAS bf16x8*)(L + L_CKV + key * 1024 + (((4 * s2 + q) ^ f2) * 16));
    }
    {
      const int off = S2_OFF + hg * 8192 + t * 16;
      v4u v; int spins = 0;
      for (;;) { CBAR(); v = ld16(rs, off); if (valid4(v) || ++spins > SPIN_CAP) break; }
      *(LAS v4u*)(qa + (t >> 6) * 1056 + (t & 63) * 16) = v;
    }
    bar();
    STAMP(5);
    f32x4 sc = {0, 0, 0, 0};
#pragma unroll
    for (int s2 = 0; s2 < 16; s2++) {
      bf16x8 bq = *(const LAS bf16x8*)(qa + (n & 7) * 1056 + s2 * 64 + q * 16);
      sc = __builtin_amdgcn_mfma_f32_16x16x32_bf16(ka[s2], bq, sc, 0, 0, 0);
    }
    float mx = fmaxf(fmaxf(sc[0], sc[1]), fmaxf(sc[2], sc[3]));
    mx = max16(mx); mx = max32(mx);
    if (lane < 8) wmax[lane * 8 + wave] = mx;
    bar();
    {
      f32x4 m0 = *(LAS f32x4*)(wmax + (n & 7) * 8), m1 = *(LAS f32x4*)(wmax + (n & 7) * 8 + 4);
      mx = fmaxf(fmaxf(fmaxf(m0[0], m0[1]), fmaxf(m0[2], m0[3])), fmaxf(fmaxf(m1[0], m1[1]), fmaxf(m1[2], m1[3])));
    }
    const float p0 = __builtin_amdgcn_exp2f(sc[0] - mx), p1 = __builtin_amdgcn_exp2f(sc[1] - mx), p2 = __builtin_amdgcn_exp2f(sc[2] - mx), p3 = __builtin_amdgcn_exp2f(sc[3] - mx);
    float ps = (p0 + p1) + (p2 + p3);
    ps = red16(ps, ps); ps = red32(ps, ps);
    if (lane < 8) wsum[lane * 8 + wave] = ps;
    if (n < 8) { v2u o = {pk2(p0, p1), pk2(p2, p3)}; *(LAS v2u*)(Pm + n * 288 + (wave * 16 + q * 4) * 2) = o; }
    bar();
    {
      bf16x8 bp[4];
#pragma unroll
      for (int ks = 0; ks < 4; ks++) bp[ks] = *(const LAS bf16x8*)(Pm + (n & 7) * 288 + ks * 64 + q * 16);
      f32x4 acc[4];
      const int qq = (lane & 15) >> 2, pp = lane & 3;
#pragma unroll
      for (int T = 0; T < 4; T++) {
        acc[T] = (f32x4){0, 0, 0, 0};
#pragma unroll
        for (int ks = 0; ks < 4; ks++) {
          s16x4 h2[2];
#pragma unroll
          for (int tt = 0; tt < 2; tt++) {
            const int key = 32 * ks + 8 * q + 4 * tt + qq;
            const int f2 = ((key & 3) | ((key >> 1) & 4)) << 1;
            const int ch = (wave * 8 + T * 2 + (pp >> 1)) ^ f2;
            h2[tt] = __builtin_amdgcn_ds_read_tr16_b64_v4i16((LAS s16x4*)(L + L_CKV + key * 1024 + ch * 16 + (pp & 1) * 8));
          }
          bf16x8 av = {h2[0].x, h2[0].y, h2[0].z, h2[0].w, h2[1].x, h2[1].y, h2[1].z, h2[1].w};
          acc[T] = __builtin_amdgcn_mfma_f32_16x16x32_bf16(av, bp[ks], acc[T], 0, 0, 0);
        }
      }
      unsigned d0[4], d1[4];
#pragma unroll
      for (int T = 0; T < 4; T++) { d0[T] = canon(pk2(acc[T][0], acc[T][1])); d1[T] = canon(pk2(acc[T][2], acc[T][3])); }
      const bool hi = (lane & 8) != 0;
      const unsigned a0 = hi ? d0[2] : d0[0], a1 = hi ? d1[2] : d1[0], b0 = hi ? d0[3] : d0[1], b1v = hi ? d1[3] : d1[1];
      auto r0 = __builtin_amdgcn_permlane16_swap(a0, b0, false, false);
      auto r1 = __builtin_amdgcn_permlane16_swap(a1, b1v, false, false);
      v4u o = {r0[0], r1[0], r0[1], r1[1]};
      const int T = (hi ? 2 : 0) + (q & 1);
      const int c = wave * 64 + T * 16 + (q & 2) * 4;
      st16(rs, S3_OFF + ((hg * 8 + (n & 7)) * 32 + split) * 1024 + c * 2, o);
      if (t < 8) {
        f32x4 s0 = *(LAS f32x4*)(wsum + t * 8), s1 = *(LAS f32x4*)(wsum + t * 8 + 4);
        const float l = ((s0[0] + s0[1]) + (s0[2] + s0[3])) + ((s1[0] + s1[1]) + (s1[2] + s1[3]));
        v2u o2 = {canon(__float_as_uint(mx)), canon(__float_as_uint(l))};
        st8(rs, ST_OFF + ((hg * 8 + t) * 32 + split) * 8, o2);
      }
    }
    STAMP(6);
  } else if (r < 24) {
    const int e = r - 16, head = 4 * xc + (e >> 1), part = e & 1, la = xc * 8 + e;
    const int s1off = S1_OFF + (8 + xc) * S1_BYTES + t * 16;
    v4u snap = {0, 0, 0, 0};
    if (t < 256) snap = ld16(rs, s1off);
    v4u wm[4]; unsigned short cv[4];
    {
      const u16* p = w_mod + (size_t)(t >> 2) * 3072 + 1024 + la * 32 + (t & 3) * 8;
#pragma unroll
      for (int i = 0; i < 4; i++) { wm[i] = *(const v4u*)(p + (size_t)i * 128 * 3072); cv[i] = cvec[(t >> 2) + 128 * i]; }
    }
    v4u wv[16];
    {
      const u16* p = w_uv + (size_t)(wave * 64 + (lane >> 4) * 16) * 4096 + head * 128 + (lane & 15) * 8;
#pragma unroll
      for (int i = 0; i < 16; i++) wv[i] = *(const v4u*)(p + (size_t)i * 4096);
    }
    v4u wo[8];
    {
      const u16* p = w_o + (size_t)(head * 128 + (t >> 5) * 8) * 512 + part * 256 + (t & 31) * 8;
#pragma unroll
      for (int i = 0; i < 8; i++) wo[i] = *(const v4u*)(p + (size_t)i * 512);
    }
    CBAR();
    LAS float* big = (LAS float*)(L);
    LAS unsigned* cc16 = (LAS unsigned*)(L + 16384);
    LAS float* epart = (LAS float*)(L + 17408);
    LAS unsigned* c16 = (LAS unsigned*)(L + 21504);
    LAS float* apart = (LAS float*)(L + 21760);
    {
      float sv[4];
#pragma unroll
      for (int i = 0; i < 4; i++) { float c = bflo(cv[i]); sv[i] = c / (1.f + __expf(-c)); }
      const unsigned s01 = pk2(sv[0], sv[1]), s23 = pk2(sv[2], sv[3]);
      interleave(wm[0], wm[1]); interleave(wm[2], wm[3]);
      float a[8];
      a[0] = dot2(wm[2].x, s23, dot2(wm[0].x, s01, 0.f)); a[1] = dot2(wm[2].y, s23, dot2(wm[0].y, s01, 0.f));
      a[2] = dot2(wm[2].z, s23, dot2(wm[0].z, s01, 0.f)); a[3] = dot2(wm[2].w, s23, dot2(wm[0].w, s01, 0.f));
      a[4] = dot2(wm[3].x, s23, dot2(wm[1].x, s01, 0.f)); a[5] = dot2(wm[3].y, s23, dot2(wm[1].y, s01, 0.f));
      a[6] = dot2(wm[3].z, s23, dot2(wm[1].z, s01, 0.f)); a[7] = dot2(wm[3].w, s23, dot2(wm[1].w, s01, 0.f));
      float b0 = red32(a[0], a[4]), b1v = red32(a[1], a[5]), b2v = red32(a[2], a[6]), b3 = red32(a[3], a[7]);
      float c0 = red16(b0, b2v), c1 = red16(b1v, b3);
      c0 += dppf<0x128>(c0); c1 += dppf<0x128>(c1); c0 += dppf<0x124>(c0); c1 += dppf<0x124>(c1);
      if ((lane & 12) == 0) { f32x2 o = {c0, c1}; *(LAS f32x2*)(apart + wave * 32 + (lane & 3) * 8 + 4 * (lane >> 5) + 2 * ((lane >> 4) & 1)) = o; }
    }
#pragma unroll
    for (int i = 0; i < 8; i++) interleave(wv[2 * i], wv[2 * i + 1]);
#pragma unroll
    for (int i = 0; i < 4; i++) interleave(wo[2 * i], wo[2 * i + 1]);
    STAMP(1);
    if (t < 256) {
      int spins = 0;
      for (;;) { CBAR(); v4u v = ld16(rs, s1off); if (((v.y != snap.y) & (v.w != snap.w)) || ++spins > SPIN_CAP) break; __builtin_amdgcn_s_sleep(2); }
    }
    bar();
    STAMP(2);
    if (t < 4) {
      f32x4 s0 = {0, 0, 0, 0}, s1 = {0, 0, 0, 0};
#pragma unroll
      for (int w = 0; w < 8; w++) { s0 += *(LAS f32x4*)(apart + w * 32 + t * 8); s1 += *(LAS f32x4*)(apart + w * 32 + t * 8 + 4); }
      v4u o = {canon(pk2(s0[0], s0[1])), canon(pk2(s0[2], s0[3])), canon(pk2(s1[0], s1[1])), canon(pk2(s1[2], s1[3]))};
      st16(rs, SL_OFF + la * 64 + t * 16, o);
    }
    v4u pv[4]; v2u stt = {0, 0};
    {
      const int off = S3_OFF + (head * 32 + wave) * 1024 + lane * 16;
      const int soff = ST_OFF + (head * 32 + (lane & 31)) * 8;
      bool ok0 = false, ok1 = false, ok2 = false, ok3 = false, oks = false; int spins = 0;
      for (;;) {
        CBAR();
        if (!oks) stt = ld8(rs, soff);
        if (!ok0) pv[0] = ld16(rs, off);
        if (!ok1) pv[1] = ld16(rs, off + 8192);
        if (!ok2) pv[2] = ld16(rs, off + 16384);
        if (!ok3) pv[3] = ld16(rs, off + 24576);
        oks = (stt.x != SENT) & (stt.y != SENT);
        ok0 = valid4(pv[0]); ok1 = valid4(pv[1]); ok2 = valid4(pv[2]); ok3 = valid4(pv[3]);
        if ((oks & ok0 & ok1 & ok2 & ok3) || ++spins > SPIN_CAP) break;
      }
    }
    STAMP(3);
    float inv_l;
    {
      const float m = __uint_as_float(stt.x), l = __uint_as_float(stt.y);
      const float mg = wave_max(m);
      const float sc = __builtin_amdgcn_exp2f(m - mg);
      const float lt = wave_sum(sc * l) * 0.5f;
      inv_l = 1.f / lt;
      float a[8] = {0, 0, 0, 0, 0, 0, 0, 0};
#pragma unroll
      for (int k = 0; k < 4; k++) {
        const float f = __int_as_float(__builtin_amdgcn_readlane(__float_as_int(sc), wave + 8 * k));
        a[0] += f * bflo(pv[k].x); a[1] += f * bfhi(pv[k].x); a[2] += f * bflo(pv[k].y); a[3] += f * bfhi(pv[k].y);
        a[4] += f * bflo(pv[k].z); a[5] += f * bfhi(pv[k].z); a[6] += f * bflo(pv[k].w); a[7] += f * bfhi(pv[k].w);
      }
      f32x4 o0 = {a[0], a[1], a[2], a[3]}, o1 = {a[4], a[5], a[6], a[7]};
      *(LAS f32x4*)(big + wave * 512 + lane * 8) = o0; *(LAS f32x4*)(big + wave * 512 + lane * 8 + 4) = o1;
    }
    bar();
    if (t < 256) {
      float a0 = 0, a1 = 0;
#pragma unroll
      for (int w = 0; w < 8; w++) { f32x2 v = *(LAS f32x2*)(big + w * 512 + 2 * t); a0 += v.x; a1 += v.y; }
      cc16[t] = pk2(a0 * inv_l, a1 * inv_l);
    }
    bar();
    gemv_part(wv, cc16, epart, wave, lane);
    bar();
    if (t < 64) {
      float a0 = 0, a1 = 0;
#pragma unroll
      for (int w = 0; w < 8; w++) { f32x2 v = *(LAS f32x2*)(epart + w * 128 + 2 * t); a0 += v.x; a1 += v.y; }
      c16[t] = pk2(a0, a1);
    }
    bar();
    {
      v4u cvv = *(const LAS v4u*)(c16 + (t >> 5) * 4);
      unsigned cp[4] = {cvv.x, cvv.y, cvv.z, cvv.w};
      float a[8] = {0, 0, 0, 0, 0, 0, 0, 0};
#pragma unroll
      for (int i = 0; i < 4; i++) {
        a[0] = dot2(wo[2 * i].x, cp[i], a[0]); a[1] = dot2(wo[2 * i].y, cp[i], a[1]); a[2] = dot2(wo[2 * i].z, cp[i], a[2]); a[3] = dot2(wo[2 * i].w, cp[i], a[3]);
        a[4] = dot2(wo[2 * i + 1].x, cp[i], a[4]); a[5] = dot2(wo[2 * i + 1].y, cp[i], a[5]); a[6] = dot2(wo[2 * i + 1].z, cp[i], a[6]); a[7] = dot2(wo[2 * i + 1].w, cp[i], a[7]);
      }
      f32x4 o = {red32(a[0], a[4]), red32(a[1], a[5]), red32(a[2], a[6]), red32(a[3], a[7])};
      *(LAS f32x4*)(big + wave * 256 + (lane & 31) * 8 + (lane >> 5) * 4) = o;
    }
    bar();
    if (t < 128) {
      float a0 = 0, a1 = 0;
#pragma unroll
      for (int w = 0; w < 8; w++) { f32x2 v = *(LAS f32x2*)(big + w * 256 + 2 * t); a0 += v.x; a1 += v.y; }
      const unsigned d = canon(pk2(a0, a1));
      v4u o = {dppu<0x00>(d), dppu<0x55>(d), dppu<0xAA>(d), dppu<0xFF>(d)};
      if ((t & 3) == 0) st16(rs, S5_OFF + head * 1024 + part * 512 + t * 4, o);
    }
    STAMP(4);
  } else {
    const int ti = xc * 8 + (r - 24);
    const int s1off = S1_OFF + (8 + xc) * S1_BYTES + t * 16;
    v4u snap = {0, 0, 0, 0};
    if (t < 256) snap = ld16(rs, s1off);
    v4u wm[2]; unsigned short cv[2];
    {
      const u16* p = w_mod + (size_t)(t >> 1) * 3072 + ti * 16 + (t & 1) * 8;
      wm[0] = *(const v4u*)p; wm[1] = *(const v4u*)(p + (size_t)256 * 3072);
      cv[0] = cvec[t >> 1]; cv[1] = cvec[(t >> 1) + 256];
    }
    CBAR();
    {
      const v4u sv = {SENT, SENT, SENT, SENT};
      const int base = CLR_OFF + ti * CLR_PIECES * 16;
      for (int i = t; i < CLR_PIECES; i += NT) st16(rs, base + i * 16, sv);
    }
    CBAR();
    LAS float* big = (LAS float*)(L);
    LAS float* xn = (LAS float*)(L + 16384);
    LAS float* apart = (LAS float*)(L + 18432);
    LAS unsigned* pub = (LAS unsigned*)(L + 18944);
    LAS unsigned* ml = (LAS unsigned*)(L + 19456);
    LAS float* gpart = (LAS float*)(L + 23552);
    LAS unsigned* h1s = (LAS unsigned*)(L + 24576);
    LAS float* hpart = (LAS float*)(L + 28672);
    {
      const float c0 = bflo(cv[0]), c1 = bflo(cv[1]);
      const unsigned s01 = pk2(c0 / (1.f + __expf(-c0)), c1 / (1.f + __expf(-c1)));
      interleave(wm[0], wm[1]);
      float a[8] = {dot2(wm[0].x, s01, 0.f), dot2(wm[0].y, s01, 0.f), dot2(wm[0].z, s01, 0.f), dot2(wm[0].w, s01, 0.f),
                    dot2(wm[1].x, s01, 0.f), dot2(wm[1].y, s01, 0.f), dot2(wm[1].z, s01, 0.f), dot2(wm[1].w, s01, 0.f)};
      float b0 = red32(a[0], a[4]), b1v = red32(a[1], a[5]), b2v = red32(a[2], a[6]), b3 = red32(a[3], a[7]);
      float d0 = red16(b0, b2v), d1 = red16(b1v, b3);
      d0 += dppf<0x128>(d0); d1 += dppf<0x128>(d1); d0 += dppf<0x124>(d0); d1 += dppf<0x124>(d1); d0 += dppf<0x122>(d0); d1 += dppf<0x122>(d1);
      if ((lane & 14) == 0) { f32x2 o = {d0, d1}; *(LAS f32x2*)(apart + wave * 16 + (lane & 1) * 8 + 4 * (lane >> 5) + 2 * ((lane >> 4) & 1)) = o; }
    }
    asm volatile("s_waitcnt vmcnt(0)" ::: "memory");
    bar();
    STAMP(1);
    const int pidx = (ti < 32) ? 2 * (4 * ti + (lane & 3)) : 2 * (4 * (ti - 32) + (lane & 3)) + 1;
    const int puboff = S1_OFF + (lane >> 2) * S1_BYTES + pidx * 16;
    unsigned tagc = ((unsigned)wave << 24) + 1u;
    if (wave == 0) {
      f32x4 s = {0, 0, 0, 0};
#pragma unroll
      for (int w = 0; w < 8; w++) s += *(LAS f32x4*)(apart + w * 16 + (lane & 3) * 4);
      const unsigned v01 = pk2(s[0], s[1]), v23 = pk2(s[2], s[3]);
      v4u o = {v01, tagc, v23, tagc};
      st16(rs, puboff, o); tagc++;
      if (lane < 4) { v2u p2 = {v01, v23}; *(LAS v2u*)(pub + lane * 2) = p2; }
    }
    STAMP(2);
    const int st0 = (43 * ti) >> 4, nst = ((43 * (ti + 1)) >> 4) - st0;
    v4u wg[3][1];
    {
      const u16* p = w1 + (size_t)t * 1376 + st0 * 8;
      wg[0][0] = *(const v4u*)p; wg[1][0] = *(const v4u*)(p + 8);
      wg[2][0] = (nst == 3) ? *(const v4u*)(p + 16) : (v4u){0, 0, 0, 0};
    }
    v4u wh[4];
    {
      const u16* p = w2 + (size_t)(2 * t) * 512 + ti * 8;
      wh[0] = *(const v4u*)p; wh[1] = *(const v4u*)(p + 512);
      if (t < 176) { wh[2] = *(const v4u*)(p + (size_t)1024 * 512); wh[3] = *(const v4u*)(p + (size_t)1025 * 512); }
      else { wh[2] = (v4u){0, 0, 0, 0}; wh[3] = (v4u){0, 0, 0, 0}; }
    }
    const float xme = bflo(x[t]);
    float b1v = 0.f; if (t < nst * 8) b1v = bflo(b1[st0 * 8 + t]);
    float b2v = 0.f; if (t < 8) b2v = bflo(b2[ti * 8 + t]);
    bar();
#define REPUB() do { v2u p2_ = *(LAS v2u*)(pub + (lane & 3) * 2); v4u o_ = {p2_.x, tagc, p2_.y, tagc}; st16(rs, puboff, o_); tagc++; } while (0)
    if (wave < 4) {
      int spins = 0;
      for (;;) {
        CBAR(); v4u v = ld16(rs, s1off);
        const bool ok = (v.y != snap.y) & (v.w != snap.w);
        if (__all(ok) || ++spins > SPIN_CAP) break;
        if ((spins & 15) == 0) REPUB();
        __builtin_amdgcn_s_sleep(2);
      }
    }
    bar();
    interleave(wh[0], wh[1]); interleave(wh[2], wh[3]);
    STAMP(3);
    if (wave < 4) {
      v4u v; int spins = 0;
      for (;;) {
        CBAR(); v = ld16(rs, SL_OFF + t * 16);
        if (__all(valid4(v)) || ++spins > SPIN_CAP) break;
        if ((spins & 15) == 0) REPUB();
        __builtin_amdgcn_s_sleep(2);
      }
      *(LAS v4u*)(ml + t * 4) = v;
    }
    STAMP(4);
    {
      v4u pv[4];
      const int off = S5_OFF + wave * 1024 + lane * 16;
      bool ok0 = false, ok1 = false, ok2 = false, ok3 = false; int spins = 0;
      for (;;) {
        CBAR();
        if (!ok0) pv[0] = ld16(rs, off);
        if (!ok1) pv[1] = ld16(rs, off + 8192);
        if (!ok2) pv[2] = ld16(rs, off + 16384);
        if (!ok3) pv[3] = ld16(rs, off + 24576);
        ok0 = valid4(pv[0]); ok1 = valid4(pv[1]); ok2 = valid4(pv[2]); ok3 = valid4(pv[3]);
        if (__all(ok0 & ok1 & ok2 & ok3) || ++spins > SPIN_CAP) break;
        if ((spins & 15) == 0) REPUB();
      }
      float a[8] = {0, 0, 0, 0, 0, 0, 0, 0};
#pragma unroll
      for (int k = 0; k < 4; k++) {
        a[0] += bflo(pv[k].x); a[1] += bfhi(pv[k].x); a[2] += bflo(pv[k].y); a[3] += bfhi(pv[k].y);
        a[4] += bflo(pv[k].z); a[5] += bfhi(pv[k].z); a[6] += bflo(pv[k].w); a[7] += bfhi(pv[k].w);
      }
      f32x4 o0 = {a[0], a[1], a[2], a[3]}, o1 = {a[4], a[5], a[6], a[7]};
      *(LAS f32x4*)(big + wave * 512 + lane * 8) = o0; *(LAS f32x4*)(big + wave * 512 + lane * 8 + 4) = o1;
    }
    bar();
    STAMP(5);
    const LAS u16* mlh = (const LAS u16*)ml;
    float xnew;
    {
      float a = 0;
#pragma unroll
      for (int w = 0; w < 8; w++) a += big[w * 512 + t];
      xnew = rbf(xme + rbf(bflo(mlh[t]) * rbf(a)));
      xn[t] = xnew;
    }
    bar();
    float yv;
    {
      f32x4 v0 = *(LAS f32x4*)(xn + lane * 8), v1 = *(LAS f32x4*)(xn + lane * 8 + 4);
      v4u sbv = *(LAS v4u*)(ml + 256 + lane * 4), cbv = *(LAS v4u*)(ml + 512 + lane * 4);
      float xs[8] = {v0[0], v0[1], v0[2], v0[3], v1[0], v1[1], v1[2], v1[3]};
      float sb8[8] = {bflo(sbv.x), bfhi(sbv.x), bflo(sbv.y), bfhi(sbv.y), bflo(sbv.z), bfhi(sbv.z), bflo(sbv.w), bfhi(sbv.w)};
      float cb8[8] = {bflo(cbv.x), bfhi(cbv.x), bflo(cbv.y), bfhi(cbv.y), bflo(cbv.z), bfhi(cbv.z), bflo(cbv.w), bfhi(cbv.w)};
      float s = 0;
#pragma unroll
      for (int i = 0; i < 8; i++) s += xs[i];
      const float mean = wave_sum(s) * (1.f / 512.f);
      float vs = 0;
#pragma unroll
      for (int i = 0; i < 8; i++) { float d = xs[i] - mean; vs += d * d; }
      const float rstd = rsqrtf(wave_sum(vs) * (1.f / 512.f) + 1e-5f);
      float h2[8]; s = 0;
#pragma unroll
      for (int i = 0; i < 8; i++) { h2[i] = rbf(rbf(rbf((xs[i] - mean) * rstd) * rbf(1.f + cb8[i])) + sb8[i]); s += h2[i]; }
      const float mean2 = wave_sum(s) * (1.f / 512.f);
      vs = 0;
#pragma unroll
      for (int i = 0; i < 8; i++) { float d = h2[i] - mean2; vs += d * d; }
      const float rstd2 = rsqrtf(wave_sum(vs) * (1.f / 512.f) + 1e-5f);
      const float hme = rbf(rbf(rbf((xnew - mean) * rstd) * rbf(1.f + bflo(mlh[1024 + t]))) + bflo(mlh[512 + t]));
      yv = rbf((hme - mean2) * rstd2);
    }
    {
      float g[3][2];
#pragma unroll
      for (int s2 = 0; s2 < 3; s2++) {
        const v4u w = wg[s2][0];
        float a[8] = {yv * bflo(w.x), yv * bfhi(w.x), yv * bflo(w.y), yv * bfhi(w.y), yv * bflo(w.z), yv * bfhi(w.z), yv * bflo(w.w), yv * bfhi(w.w)};
        float b0 = red32(a[0], a[4]), b1x = red32(a[1], a[5]), b2x = red32(a[2], a[6]), b3 = red32(a[3], a[7]);
        float d0 = red16(b0, b2x), d1 = red16(b1x, b3);
        d0 += dppf<0x128>(d0); d1 += dppf<0x128>(d1); d0 += dppf<0x124>(d0); d1 += dppf<0x124>(d1);
        d0 += dppf<0x122>(d0); d1 += dppf<0x122>(d1); d0 += dppf<0x121>(d0); d1 += dppf<0x121>(d1);
        g[s2][0] = d0; g[s2][1] = d1;
      }
      if ((lane & 15) == 0) {
#pragma unroll
        for (int s2 = 0; s2 < 3; s2++) { f32x2 o = {g[s2][0], g[s2][1]}; *(LAS f32x2*)(gpart + wave * 24 + s2 * 8 + 4 * (lane >> 5) + 2 * ((lane >> 4) & 1)) = o; }
      }
    }
    bar();
    if (wave == 0) {
      float a = 0;
      if (lane < 24) {
#pragma unroll
        for (int w = 0; w < 8; w++) a += gpart[w * 24 + lane];
      }
      const float z = rbf(rbf(a) + b1v);
      const float u = 0.7978845608028654f * (z + 0.044715f * z * z * z);
      const float gl = z / (1.f + __expf(-2.f * u));
      const float nb = dppf<0x101>(gl);
      const unsigned d = canon(pk2(gl, nb));
      v4u o = {d, dppu<0x102>(d), dppu<0x104>(d), dppu<0x106>(d)};
      if ((lane & 7) == 0 && lane < nst * 8) st16(rs, S6_OFF + (st0 + (lane >> 3)) * 16, o);
    }
    STAMP(6);
    if (wave < 3) {
      v4u v = {0, 0, 0, 0}; int spins = 0;
      const bool act = t < 172;
      for (;;) {
        CBAR(); if (act) v = ld16(rs, S6_OFF + t * 16);
        if (__all(valid4(v)) || ++spins > SPIN_CAP) break;
        if ((spins & 15) == 0) REPUB();
      }
      *(LAS v4u*)(h1s + t * 4) = v;
    }
    bar();
    STAMP(7);
    {
      const unsigned p0 = h1s[t], p1 = (t < 176) ? h1s[512 + t] : 0u;
      float a[8];
      a[0] = dot2(wh[2].x, p1, dot2(wh[0].x, p0, 0.f)); a[1] = dot2(wh[2].y, p1, dot2(wh[0].y, p0, 0.f));
      a[2] = dot2(wh[2].z, p1, dot2(wh[0].z, p0, 0.f)); a[3] = dot2(wh[2].w, p1, dot2(wh[0].w, p0, 0.f));
      a[4] = dot2(wh[3].x, p1, dot2(wh[1].x, p0, 0.f)); a[5] = dot2(wh[3].y, p1, dot2(wh[1].y, p0, 0.f));
      a[6] = dot2(wh[3].z, p1, dot2(wh[1].z, p0, 0.f)); a[7] = dot2(wh[3].w, p1, dot2(wh[1].w, p0, 0.f));
      float b0 = red32(a[0], a[4]), b1x = red32(a[1], a[5]), b2x = red32(a[2], a[6]), b3 = red32(a[3], a[7]);
      float d0 = red16(b0, b2x), d1 = red16(b1x, b3);
      d0 += dppf<0x128>(d0); d1 += dppf<0x128>(d1); d0 += dppf<0x124>(d0); d1 += dppf<0x124>(d1);
      d0 += dppf<0x122>(d0); d1 += dppf<0x122>(d1); d0 += dppf<0x121>(d0); d1 += dppf<0x121>(d1);
      if ((lane & 15) == 0) { f32x2 o = {d0, d1}; *(LAS f32x2*)(hpart + wave * 8 + 4 * (lane >> 5) + 2 * ((lane >> 4) & 1)) = o; }
    }
    bar();
    if (t < 8) {
      float a = 0;
#pragma unroll
      for (int w = 0; w < 8; w++) a += hpart[w * 8 + t];
      const float o = rbf(rbf(a) + b2v);
      const float res = xn[ti * 8 + t] + rbf(bflo(mlh[1536 + ti * 8 + t]) * o);
      out[ti * 8 + t] = __builtin_bit_cast(u16, (__bf16)res);
    }
    STAMP(8);
  }
}

extern "C" void kernel_launch(void* const* d_in, const int* in_sizes, int n_in, void* d_out, int out_size, void* d_ws, size_t ws_size, hipStream_t stream) {
  (void)in_sizes; (void)n_in; (void)out_size; (void)ws_size;
  hipFuncSetAttribute((const void*)fused_kernel, hipFuncAttributeMaxDynamicSharedMemorySize, L_TOTAL);
  fused_kernel<<<256, NT, L_TOTAL, stream>>>((const u16*)d_in[0], (const u16*)d_in[1], (const u16*)d_in[2], (const u16*)d_in[3], (const u16*)d_in[4], (const u16*)d_in[5],
                                             (const u16*)d_in[7], (const u16*)d_in[8], (const u16*)d_in[9], (const u16*)d_in[10], (const u16*)d_in[11], (const u16*)d_in[12],
                                             (u16*)d_out, (unsigned char*)d_ws);
}
```

```cpp
#include <hip/hip_runtime.h>
#include <stdint.h>

typedef unsigned short u16;
typedef unsigned v4u __attribute__((ext_vector_type(4)));
typedef unsigned v2u __attribute__((ext_vector_type(2)));
typedef float f32x4 __attribute__((ext_vector_type(4)));
typedef float f32x2 __attribute__((ext_vector_type(2)));
typedef short bf16x8 __attribute__((ext_vector_type(8)));
typedef short s16x4 __attribute__((ext_vector_type(4)));
typedef __bf16 bf2 __attribute__((ext_vector_type(2)));
#define LAS __attribute__((address_space(3)))

constexpr unsigned SENT = 0xFFFFFFFFu;
constexpr int NT = 512;
constexpr int SPIN_CAP = 1 << 20;
constexpr int S1_OFF = 0, S1_BYTES = 4096;
constexpr int CLR_OFF = 65536;
constexpr int S2_OFF = CLR_OFF;
constexpr int S5_OFF = S2_OFF + 32768;
constexpr int SL_OFF = S5_OFF + 32768;
constexpr int S6_OFF = SL_OFF + 4096;
constexpr int DN_OFF = S6_OFF + 3072;
constexpr int ST_OFF = S6_OFF + 4096;
constexpr int S3_OFF = ST_OFF + 8192;
constexpr int CLR_END = S3_OFF + (1 << 20);
constexpr int CLR_PIECES = (CLR_END - CLR_OFF) / 16 / 64;
constexpr int WS_USED = CLR_END;
#define STAMP(i) do {} while (0)

constexpr int L_CKV = 0;
constexpr int L_SM = 131072;
constexpr int L_TOTAL = 131072 + 30720;

__device__ __forceinline__ float bflo(unsigned w) { return __uint_as_float(w << 16); }
__device__ __forceinline__ float bfhi(unsigned w) { return __uint_as_float(w & 0xffff0000u); }
__device__ __forceinline__ unsigned pk2(float a, float b) { bf2 p; p.x = (__bf16)a; p.y = (__bf16)b; return __builtin_bit_cast(unsigned, p); }
__device__ __forceinline__ float rbf(float a) { return (float)(__bf16)a; }
__device__ __forceinline__ unsigned canon(unsigned w) { return w == SENT ? 0x7FC07FC0u : w; }
__device__ __forceinline__ float dot2(unsigned a, unsigned b, float c) { return __builtin_amdgcn_fdot2_f32_bf16(__builtin_bit_cast(bf2, a), __builtin_bit_cast(bf2, b), c, false); }
__device__ __forceinline__ void bar() { asm volatile("s_waitcnt lgkmcnt(0)\n\ts_barrier" ::: "memory"); }
template <int CTRL> __device__ __forceinline__ float dppf(float v) { return __int_as_float(__builtin_amdgcn_update_dpp(0, __float_as_int(v), CTRL, 0xf, 0xf, false)); }
template <int CTRL> __device__ __forceinline__ unsigned dppu(unsigned v) { return (unsigned)__builtin_amdgcn_update_dpp(0, (int)v, CTRL, 0xf, 0xf, false); }
__device__ __forceinline__ float red32(float a, float b) {
  auto r = __builtin_amdgcn_permlane32_swap(__float_as_uint(a), __float_as_uint(b), false, false);
  return __uint_as_float(r[0]) + __uint_as_float(r[1]);
}
__device__ __forceinline__ float red16(float a, float b) {
  auto r = __builtin_amdgcn_permlane16_swap(__float_as_uint(a), __float_as_uint(b), false, false);
  return __uint_as_float(r[0]) + __uint_as_float(r[1]);
}
__device__ __forceinline__ float max32(float a) { auto r = __builtin_amdgcn_permlane32_swap(__float_as_uint(a), __float_as_uint(a), false, false); return fmaxf(__uint_as_float(r[0]), __uint_as_float(r[1])); }
__device__ __forceinline__ float max16(float a) { auto r = __builtin_amdgcn_permlane16_swap(__float_as_uint(a), __float_as_uint(a), false, false); return fmaxf(__uint_as_float(r[0]), __uint_as_float(r[1])); }
__device__ __forceinline__ float wave_sum(float v) {
  v += dppf<0x128>(v); v += dppf<0x124>(v); v += dppf<0x122>(v); v += dppf<0x121>(v);
  v = red16(v, v); v = red32(v, v); return v;
}
__device__ __forceinline__ float wave_max(float v) {
  v = fmaxf(v, dppf<0x128>(v)); v = fmaxf(v, dppf<0x124>(v)); v = fmaxf(v, dppf<0x122>(v)); v = fmaxf(v, dppf<0x121>(v));
  v = max16(v); v = max32(v); return v;
}
__device__ __forceinline__ unsigned ilo(unsigned a, unsigned b) { return __builtin_amdgcn_perm(b, a, 0x05040100u); }
__device__ __forceinline__ unsigned ihi(unsigned a, unsigned b) { return __builtin_amdgcn_perm(b, a, 0x07060302u); }
__device__ __forceinline__ void interleave(v4u& a, v4u& b) {
  v4u x, y;
  x.x = ilo(a.x, b.x); x.y = ihi(a.x, b.x); x.z = ilo(a.y, b.y); x.w = ihi(a.y, b.y);
  y.x = ilo(a.z, b.z); y.y = ihi(a.z, b.z); y.z = ilo(a.w, b.w); y.w = ihi(a.w, b.w);
  a = x; b = y;
}
__device__ __forceinline__ bool valid4(v4u v) { return (v.x != SENT) & (v.y != SENT) & (v.z != SENT) & (v.w != SENT); }
typedef __amdgpu_buffer_rsrc_t rsrc_t;
__device__ __forceinline__ v4u ld16(rsrc_t r, int off) { return __builtin_amdgcn_raw_buffer_load_b128(r, off, 0, 16); }
__device__ __forceinline__ v2u ld8(rsrc_t r, int off) { return __builtin_amdgcn_raw_buffer_load_b64(r, off, 0, 16); }
__device__ __forceinline__ void st16(rsrc_t r, int off, v4u v) { __builtin_amdgcn_raw_buffer_store_b128(v, r, off, 0, 16); }
__device__ __forceinline__ void st8(rsrc_t r, int off, v2u v) { __builtin_amdgcn_raw_buffer_store_b64(v, r, off, 0, 16); }
__device__ __forceinline__ void glds16(const void* g, unsigned lds_base) {
  unsigned sv; asm volatile("s_mov_b32 %0, m0\n\ts_mov_b32 m0, %2\n\ts_nop 0\n\tglobal_load_lds_dwordx4 %1, off\n\ts_mov_b32 m0, %0" : "=&s"(sv) : "v"(g), "s"(lds_base) : "memory");
}
#define CBAR() asm volatile("" ::: "memory")

__device__ __forceinline__ int swz(int row) { return ((row & 3) | ((row >> 1) & 4)) << 1; }
__device__ __forceinline__ void dma16(const void* g, LAS unsigned char* l) { __builtin_amdgcn_global_load_lds((const __attribute__((address_space(1))) unsigned*)g, (LAS unsigned*)l, 16, 0, 0); }
template <int RB, bool SW>
__device__ __forceinline__ bf16x8 trfrag(const LAS unsigned char* img, int k0, int ch0, int lane) {
  const int g = lane >> 4, qq = (lane & 15) >> 2, pp = lane & 3;
  s16x4 h[2];
#pragma unroll
  for (int tt = 0; tt < 2; tt++) {
    const int row = k0 + 8 * g + 4 * tt + qq;
    const int ch = SW ? ((ch0 + (pp >> 1)) ^ swz(row)) : (ch0 + (pp >> 1));
    h[tt] = __builtin_amdgcn_ds_read_tr16_b64_v4i16((LAS s16x4*)(img + row * RB + ch * 16 + (pp & 1) * 8));
  }
  return (bf16x8){h[0].x, h[0].y, h[0].z, h[0].w, h[1].x, h[1].y, h[1].z, h[1].w};
}
#define MFMA(a, b, c) __builtin_amdgcn_mfma_f32_16x16x32_bf16(a, b, c, 0, 0, 0)
#define VMWAIT0() asm volatile("s_waitcnt vmcnt(0)" ::: "memory")
#define LGWAIT0() asm volatile("s_waitcnt lgkmcnt(0)" ::: "memory")
__device__ __forceinline__ float wave_sum2(float a, float b) {
  a += dppf<0x128>(a); b += dppf<0x128>(b); a += dppf<0x124>(a); b += dppf<0x124>(b);
  a += dppf<0x122>(a); b += dppf<0x122>(b); a += dppf<0x121>(a); b += dppf<0x121>(b);
  float v = red16(a, b); return red32(v, v);
}

__global__ void __launch_bounds__(NT, 2) fused_kernel(
    const u16* __restrict__ x, const u16* __restrict__ ckv, const u16* __restrict__ w_q, const u16* __restrict__ w_uk,
    const u16* __restrict__ w_uv, const u16* __restrict__ w_o, const u16* __restrict__ w1, const u16* __restrict__ b1,
    const u16* __restrict__ w2, const u16* __restrict__ b2, const u16* __restrict__ cvec, const u16* __restrict__ w_mod,
    u16* __restrict__ out, unsigned char* ws) {
  extern __shared__ __attribute__((aligned(16))) unsigned char lds_raw[];
  LAS unsigned char* L = (LAS unsigned char*)lds_raw;
  const int t = threadIdx.x, lane = t & 63, wave = __builtin_amdgcn_readfirstlane(t >> 6);
  const int b = blockIdx.x, xc = b & 7, r = b >> 3;
  const int n = lane & 15, q = lane >> 4;
  const rsrc_t rs = __builtin_amdgcn_make_buffer_rsrc((void*)ws, 0, WS_USED, 0x00020000);
  LAS unsigned char* SM = L + L_SM;
  STAMP(0);

  if (r < 16) {
    const int head = 4 * xc + (r >> 2), cs = r & 3;
    const int s1off = S1_OFF + xc * S1_BYTES + (t & 255) * 16;
    const v4u snap = ld16(rs, s1off);
    const v4u xv = *(const v4u*)(x + lane * 8);
    unsigned xw = ((const unsigned*)x)[t & 255];
    bf16x8 wk[4];
    {
      const u16* pk = w_uk + (size_t)(cs * 128 + wave * 16 + n) * 4096 + head * 128 + q * 8;
#pragma unroll
      for (int i = 0; i < 4; i++) wk[i] = *(const bf16x8*)(pk + i * 32);
    }
    CBAR();
    {
#pragma unroll
      for (int i = 0; i < 16; i++) {
        const int rg = (8 * i + wave + 32 * cs) & 127, row = 4 * rg + q;
        dma16(w_q + (size_t)row * 4096 + head * 128 + ((n ^ swz(row)) * 8), L + rg * 1024);
      }
    }
    CBAR();
    float xs[8] = {bflo(xv.x), bfhi(xv.x), bflo(xv.y), bfhi(xv.y), bflo(xv.z), bfhi(xv.z), bflo(xv.w), bfhi(xv.w)};
    float s = 0;
#pragma unroll
    for (int i = 0; i < 8; i++) s += xs[i];
    const float mean = wave_sum(s) * (1.f / 512.f);
    float vs = 0;
#pragma unroll
    for (int i = 0; i < 8; i++) { float d = xs[i] - mean; vs += d * d; }
    const float rstd = rsqrtf(wave_sum(vs) * (1.f / 512.f) + 1e-5f);
    float ln0 = rbf((bflo(xw) - mean) * rstd), ln1 = rbf((bfhi(xw) - mean) * rstd);
    asm volatile("" : "+v"(ln0), "+v"(ln1));
    VMWAIT0();
    bar();
    STAMP(1);
    bf16x8 wqf[16];
#pragma unroll
    for (int s2 = 0; s2 < 16; s2++) wqf[s2] = trfrag<256, true>(L, 32 * s2, 2 * wave, lane);
    LAS unsigned* hp = (LAS unsigned*)(SM);
    LAS unsigned* q16 = (LAS unsigned*)(SM + 1024);
    if (wave < 4) {
      v4u v; int spins = 0;
      for (;;) {
        CBAR();
        v = ld16(rs, s1off);
        if (((v.y != snap.y) & (v.w != snap.w)) || ++spins > SPIN_CAP) break;
      }
      const bool odd = t & 1;
      const unsigned send = odd ? v.x : v.z;
      const unsigned recv = dppu<0xB1>(send);
      const unsigned sa = odd ? recv : v.x, ca = odd ? v.z : recv;
      const float h0 = rbf(ln0 * rbf(1.f + bflo(ca))) + bflo(sa), h1v = rbf(ln1 * rbf(1.f + bfhi(ca))) + bfhi(sa);
      hp[t] = pk2(h0, h1v);
    }
    bar();
    STAMP(2);
    {
      f32x4 a0 = {0, 0, 0, 0};
#pragma unroll
      for (int s2 = 0; s2 < 16; s2++) a0 = MFMA(*(const LAS bf16x8*)(hp + s2 * 16 + q * 4), wqf[s2], a0);
      const unsigned d = pk2(a0[0], dppf<0x101>(a0[0]));
      if (lane < 16 && !(lane & 1)) q16[wave * 8 + (lane >> 1)] = d;
    }
    bar();
    {
      f32x4 a = {0, 0, 0, 0};
#pragma unroll
      for (int i = 0; i < 4; i++) a = MFMA(wk[i], *(const LAS bf16x8*)(q16 + i * 16 + q * 4), a);
      const float SC = 0.08838834764831845f * 1.4426950408889634f;
      v2u o = {canon(pk2(a[0] * SC, a[1] * SC)), canon(pk2(a[2] * SC, a[3] * SC))};
      if (n == 0) st8(rs, S2_OFF + head * 1024 + (cs * 128 + wave * 16 + q * 4) * 2, o);
    }
    STAMP(3);
    if (r >= 8) return;
    const int ti = xc * 8 + r;
    const int st0 = (43 * ti) >> 4, nst = ((43 * (ti + 1)) >> 4) - st0;
    const int wst = st0 < 168 ? st0 : 168;
    LAS unsigned char* W1I = L;
    LAS unsigned char* W2I = L + 32768;
    LAS unsigned char* STG = L + 65536;
#pragma unroll
    for (int i = 0; i < 4; i++) { const int rg = 8 * i + wave; dma16(w1 + (size_t)(rg * 16 + (lane >> 2)) * 1376 + wst * 8 + (lane & 3) * 8, W1I + rg * 1024); }
#pragma unroll
    for (int i = 0; i < 3; i++) { const int rg = 8 * i + wave; if (rg < 22) { int row = rg * 64 + lane; row = row < 1376 ? row : 1375; dma16(w2 + (size_t)row * 512 + ti * 8, W2I + rg * 1024); } }
    const float xme = bflo(x[t]);
    const int gcol = (st0 - wst) * 8 + lane;
    const float b1v = bflo(b1[st0 * 8 + (lane < nst * 8 ? lane : 0)]);
    const float b2v = bflo(b2[ti * 8 + (lane & 7)]);
    LAS unsigned* ml = (LAS unsigned*)(SM + 2048);
    LAS float* xn = (LAS float*)(SM + 6144);
    LAS float* stat = (LAS float*)(SM + 8192);
    LAS u16* y16 = (LAS u16*)(SM + 8448);
    LAS float* gpart = (LAS float*)(SM + 9472);
    LAS float* hpart = (LAS float*)(SM + 10496);
    if (wave < 4) {
      v4u v; int spins = 0;
      for (;;) { CBAR(); v = ld16(rs, SL_OFF + t * 16); if (valid4(v) || ++spins > SPIN_CAP) break; __builtin_amdgcn_s_sleep(2); }
      *(LAS v4u*)(ml + t * 4) = v;
    }
    VMWAIT0();
    bar();
    bf16x8 w1f[4], w2f[6];
#pragma unroll
    for (int i = 0; i < 4; i++) w1f[i] = trfrag<64, false>(W1I, 64 * wave + 32 * (i >> 1), 2 * (i & 1), lane);
#pragma unroll
    for (int i = 0; i < 6; i++) {
      const int ks = wave + 8 * i < 43 ? wave + 8 * i : 0;
      const int g = lane >> 4, qq = (lane & 15) >> 2, pp = lane & 3;
      s16x4 h0 = __builtin_amdgcn_ds_read_tr16_b64_v4i16((LAS s16x4*)(W2I + (32 * ks + 8 * g + qq) * 16 + (pp & 1) * 8));
      s16x4 h1 = __builtin_amdgcn_ds_read_tr16_b64_v4i16((LAS s16x4*)(W2I + (32 * ks + 8 * g + 4 + qq) * 16 + (pp & 1) * 8));
      w2f[i] = (bf16x8){h0.x, h0.y, h0.z, h0.w, h1.x, h1.y, h1.z, h1.w};
    }
    const LAS u16* mlh = (const LAS u16*)ml;
    const float gam = bflo(mlh[t]), sbm = bflo(mlh[512 + t]), cb1 = rbf(1.f + bflo(mlh[1024 + t]));
    const float gbv = bflo(mlh[1536 + ti * 8 + (lane & 7)]);
    const bf16x8 ones = {0x3F80, 0x3F80, 0x3F80, 0x3F80, 0x3F80, 0x3F80, 0x3F80, 0x3F80};
    STAMP(4);
    {
      v4u pv[4];
      const int off = S5_OFF + wave * 1024 + lane * 16;
      bool ok0 = false, ok1 = false, ok2 = false, ok3 = false; int spins = 0;
      for (;;) {
        CBAR();
        if (!ok0) pv[0] = ld16(rs, off);
        if (!ok1) pv[1] = ld16(rs, off + 8192);
        if (!ok2) pv[2] = ld16(rs, off + 16384);
        if (!ok3) pv[3] = ld16(rs, off + 24576);
        ok0 = valid4(pv[0]); ok1 = valid4(pv[1]); ok2 = valid4(pv[2]); ok3 = valid4(pv[3]);
        if ((ok0 & ok1 & ok2 & ok3) || ++spins > SPIN_CAP) break;
      }
#pragma unroll
      for (int k = 0; k < 4; k++) { const int row = wave + 8 * k; *(LAS v4u*)(STG + row * 1024 + ((lane ^ swz(row)) * 16)) = pv[k]; }
    }
    bar();
    STAMP(5);
    if (ti == 0 && t < 8) { v4u o = {1, 1, 1, 1}; st16(rs, DN_OFF + t * 128, o); }
    float xnew;
    {
      f32x4 a[4];
#pragma unroll
      for (int T = 0; T < 4; T++) a[T] = MFMA(ones, (trfrag<1024, true>(STG, 0, wave * 8 + 2 * T, lane)), ((f32x4){0, 0, 0, 0}));
      const float ao = q == 0 ? a[0][0] : q == 1 ? a[1][0] : q == 2 ? a[2][0] : a[3][0];
      xnew = rbf(xme + rbf(gam * rbf(ao)));
      xn[t] = xnew;
      const float v = wave_sum2(xnew, xnew * xnew);
      if ((lane & 47) == 0) stat[wave * 2 + (lane >> 4)] = v;
    }
    bar();
    float h2;
    {
      float v = stat[n];
      v += dppf<0x128>(v); v += dppf<0x124>(v); v += dppf<0x122>(v);
      const float o = dppf<0xB1>(v);
      const float s1 = (lane & 1) ? o : v, s2 = (lane & 1) ? v : o;
      const float mean = s1 * (1.f / 512.f), var = fmaxf(s2 * (1.f / 512.f) - mean * mean, 0.f);
      const float rstd = rsqrtf(var + 1e-5f);
      h2 = rbf(rbf(rbf((xnew - mean) * rstd) * cb1) + sbm);
      const float w = wave_sum2(h2, h2 * h2);
      if ((lane & 47) == 0) stat[16 + wave * 2 + (lane >> 4)] = w;
    }
    bar();
    {
      float v = stat[16 + n];
      v += dppf<0x128>(v); v += dppf<0x124>(v); v += dppf<0x122>(v);
      const float o = dppf<0xB1>(v);
      const float s1 = (lane & 1) ? o : v, s2 = (lane & 1) ? v : o;
      const float mean = s1 * (1.f / 512.f), var = fmaxf(s2 * (1.f / 512.f) - mean * mean, 0.f);
      const float rstd = rsqrtf(var + 1e-5f);
      y16[t] = __builtin_bit_cast(u16, (__bf16)((h2 - mean) * rstd));
      LGWAIT0();
      const bf16x8 y0 = *(const LAS bf16x8*)(y16 + wave * 64 + q * 8), y1 = *(const LAS bf16x8*)(y16 + wave * 64 + 32 + q * 8);
      f32x4 z0 = MFMA(y0, w1f[0], ((f32x4){0, 0, 0, 0})), z1 = MFMA(y0, w1f[1], ((f32x4){0, 0, 0, 0}));
      z0 = MFMA(y1, w1f[2], z0); z1 = MFMA(y1, w1f[3], z1);
      if (lane < 32) gpart[wave * 32 + lane] = q == 0 ? z0[0] : z1[0];
    }
    bar();
    if (wave == 0) {
      float a = 0;
#pragma unroll
      for (int w = 0; w < 8; w++) a += gpart[w * 32 + (gcol & 31)];
      const float z = rbf(rbf(a) + b1v);
      const float u = z * (0.7978845608028654f + 0.035677408136300125f * z * z);
      const float gl = z * __builtin_amdgcn_rcpf(1.f + __builtin_amdgcn_exp2f(-2.885390081777927f * u));
      const unsigned d = canon(pk2(gl, dppf<0x101>(gl)));
      v4u o = {d, dppu<0x102>(d), dppu<0x104>(d), dppu<0x106>(d)};
      if ((lane & 7) == 0 && lane < nst * 8) st16(rs, S6_OFF + (st0 + (lane >> 3)) * 16, o);
    }
    STAMP(6);
    {
      v4u hv[6]; bool ok[6]; int spins = 0;
#pragma unroll
      for (int i = 0; i < 6; i++) { ok[i] = (i == 5) && (wave >= 3); hv[i] = (v4u){0, 0, 0, 0}; }
      for (;;) {
        CBAR();
        bool all = true;
#pragma unroll
        for (int i = 0; i < 6; i++) if (!ok[i]) hv[i] = ld16(rs, S6_OFF + (wave + 8 * i) * 64 + q * 16);
#pragma unroll
        for (int i = 0; i < 6; i++) { ok[i] = valid4(hv[i]); all &= ok[i]; }
        if (all || ++spins > SPIN_CAP) break;
      }
      STAMP(7);
      f32x4 a = {0, 0, 0, 0};
#pragma unroll
      for (int i = 0; i < 6; i++) a = MFMA(__builtin_bit_cast(bf16x8, hv[i]), w2f[i], a);
      if (lane < 8) hpart[wave * 8 + lane] = a[0];
    }
    bar();
    if (wave == 0) {
      float a = 0;
#pragma unroll
      for (int w = 0; w < 8; w++) a += hpart[w * 8 + (lane & 7)];
      const float o = rbf(rbf(a) + b2v);
      const float res = xn[ti * 8 + (lane & 7)] + rbf(gbv * o);
      if (lane < 8) out[ti * 8 + lane] = __builtin_bit_cast(u16, (__bf16)res);
    }
    STAMP(8);
  } else if (r >= 24) {
    const int e = r - 24, head = 4 * xc + (e >> 1), part = e & 1, la = xc * 8 + e;
    const int s1off = S1_OFF + (8 + xc) * S1_BYTES + (t & 255) * 16;
    const v4u snap = ld16(rs, s1off);
    v4u wo[8];
    {
      const u16* p = w_o + (size_t)(head * 128 + (t >> 5) * 8) * 512 + part * 256 + (t & 31) * 8;
#pragma unroll
      for (int i = 0; i < 8; i++) wo[i] = *(const v4u*)(p + (size_t)i * 512);
    }
    v4u wm[4]; unsigned short cv[4];
    {
      const u16* p = w_mod + (size_t)(t >> 2) * 3072 + 1024 + la * 32 + (t & 3) * 8;
#pragma unroll
      for (int i = 0; i < 4; i++) { wm[i] = *(const v4u*)(p + (size_t)i * 128 * 3072); cv[i] = cvec[(t >> 2) + 128 * i]; }
    }
    CBAR();
#pragma unroll
    for (int i = 0; i < 16; i++) {
      const int rg = (8 * i + wave + 64 * part) & 127, row = 4 * rg + q;
      dma16(w_uv + (size_t)row * 4096 + head * 128 + ((n ^ swz(row)) * 8), L + rg * 1024);
    }
    CBAR();
    LAS float* apart = (LAS float*)(SM);
    LAS u16* scl = (LAS u16*)(SM + 1024);
    LAS unsigned* cc16 = (LAS unsigned*)(SM + 2048);
    LAS unsigned* c16 = (LAS unsigned*)(SM + 3072);
    {
      float sv[4];
#pragma unroll
      for (int i = 0; i < 4; i++) { float c = bflo(cv[i]); sv[i] = c / (1.f + __expf(-c)); }
      const unsigned s01 = pk2(sv[0], sv[1]), s23 = pk2(sv[2], sv[3]);
      interleave(wm[0], wm[1]); interleave(wm[2], wm[3]);
      float a[8];
      a[0] = dot2(wm[2].x, s23, dot2(wm[0].x, s01, 0.f)); a[1] = dot2(wm[2].y, s23, dot2(wm[0].y, s01, 0.f));
      a[2] = dot2(wm[2].z, s23, dot2(wm[0].z, s01, 0.f)); a[3] = dot2(wm[2].w, s23, dot2(wm[0].w, s01, 0.f));
      a[4] = dot2(wm[3].x, s23, dot2(wm[1].x, s01, 0.f)); a[5] = dot2(wm[3].y, s23, dot2(wm[1].y, s01, 0.f));
      a[6] = dot2(wm[3].z, s23, dot2(wm[1].z, s01, 0.f)); a[7] = dot2(wm[3].w, s23, dot2(wm[1].w, s01, 0.f));
      float b0 = red32(a[0], a[4]), b1v = red32(a[1], a[5]), b2v = red32(a[2], a[6]), b3 = red32(a[3], a[7]);
      float c0 = red16(b0, b2v), c1 = red16(b1v, b3);
      c0 += dppf<0x128>(c0); c1 += dppf<0x128>(c1); c0 += dppf<0x124>(c0); c1 += dppf<0x124>(c1);
      if ((lane & 12) == 0) { f32x2 o = {c0, c1}; *(LAS f32x2*)(apart + wave * 32 + (lane & 3) * 8 + 4 * (lane >> 5) + 2 * ((lane >> 4) & 1)) = o; }
    }
    VMWAIT0();
    bar();
    STAMP(1);
    bf16x8 wvf[16];
#pragma unroll
    for (int s2 = 0; s2 < 16; s2++) wvf[s2] = trfrag<256, true>(L, 32 * s2, 2 * wave, lane);
    bar();
#pragma unroll
    for (int i = 0; i < 8; i++) { const int row = (t >> 5) * 8 + i; *(LAS v4u*)(L + row * 512 + (((t & 31) ^ swz(row)) * 16)) = wo[i]; }
    if (wave < 4) {
      int spins = 0;
      for (;;) { CBAR(); v4u v = ld16(rs, s1off); if (((v.y != snap.y) & (v.w != snap.w)) || ++spins > SPIN_CAP) break; __builtin_amdgcn_s_sleep(2); }
    }
    bar();
    STAMP(2);
    if (t < 4) {
      f32x4 s0 = {0, 0, 0, 0}, s1 = {0, 0, 0, 0};
#pragma unroll
      for (int w = 0; w < 8; w++) { s0 += *(LAS f32x4*)(apart + w * 32 + t * 8); s1 += *(LAS f32x4*)(apart + w * 32 + t * 8 + 4); }
      v4u o = {canon(pk2(s0[0], s0[1])), canon(pk2(s0[2], s0[3])), canon(pk2(s1[0], s1[1])), canon(pk2(s1[2], s1[3]))};
      st16(rs, SL_OFF + la * 64 + t * 16, o);
    }
    bf16x8 wof[8];
#pragma unroll
    for (int i = 0; i < 8; i++) wof[i] = trfrag<512, true>(L, 32 * (i >> 1), 4 * wave + 2 * (i & 1), lane);
    LAS unsigned char* STG = L + 65536;
    STAMP(3);
    float inv_l;
    {
      v4u pv[4]; v2u stt;
      const int off = S3_OFF + (head * 32 + wave) * 1024 + lane * 16;
      const int soff = ST_OFF + (head * 32 + (lane & 31)) * 8;
      bool ok0 = false, ok1 = false, ok2 = false, ok3 = false, oks = false; int spins = 0;
      for (;;) {
        CBAR();
        if (!oks) stt = ld8(rs, soff);
        if (!ok0) pv[0] = ld16(rs, off);
        if (!ok1) pv[1] = ld16(rs, off + 8192);
        if (!ok2) pv[2] = ld16(rs, off + 16384);
        if (!ok3) pv[3] = ld16(rs, off + 24576);
        oks = (stt.x != SENT) & (stt.y != SENT);
        ok0 = valid4(pv[0]); ok1 = valid4(pv[1]); ok2 = valid4(pv[2]); ok3 = valid4(pv[3]);
        if ((oks & ok0 & ok1 & ok2 & ok3) || ++spins > SPIN_CAP) break;
      }
      STAMP(4);
#pragma unroll
      for (int k = 0; k < 4; k++) { const int row = wave + 8 * k; *(LAS v4u*)(STG + row * 1024 + ((lane ^ swz(row)) * 16)) = pv[k]; }
      const float m = __uint_as_float(stt.x), l = __uint_as_float(stt.y);
      const float mg = wave_max(m);
      const __bf16 scb = (__bf16)__builtin_amdgcn_exp2f(m - mg);
      if (lane < 32) scl[wave * 32 + lane] = __builtin_bit_cast(u16, scb);
      inv_l = 2.f * __builtin_amdgcn_rcpf(wave_sum((float)scb * l));
    }
    bar();
    {
      const bf16x8 sf = *(const LAS bf16x8*)(scl + wave * 32 + q * 8);
      f32x4 a[4];
#pragma unroll
      for (int T = 0; T < 4; T++) a[T] = MFMA(sf, (trfrag<1024, true>(STG, 0, wave * 8 + 2 * T, lane)), ((f32x4){0, 0, 0, 0}));
      const float cv2 = (q == 0 ? a[0][0] : q == 1 ? a[1][0] : q == 2 ? a[2][0] : a[3][0]) * inv_l;
      const unsigned d = pk2(cv2, dppf<0x101>(cv2));
      if (!(lane & 1)) cc16[t >> 1] = d;
    }
    bar();
    {
      f32x4 a0 = {0, 0, 0, 0};
#pragma unroll
      for (int s2 = 0; s2 < 16; s2++) a0 = MFMA(*(const LAS bf16x8*)(cc16 + s2 * 16 + q * 4), wvf[s2], a0);
      const unsigned d = pk2(a0[0], dppf<0x101>(a0[0]));
      if (lane < 16 && !(lane & 1)) c16[wave * 8 + (lane >> 1)] = d;
    }
    bar();
    {
      f32x4 a0 = {0, 0, 0, 0}, a1 = {0, 0, 0, 0};
#pragma unroll
      for (int ks = 0; ks < 4; ks++) { const bf16x8 cf = *(const LAS bf16x8*)(c16 + ks * 16 + q * 4); a0 = MFMA(cf, wof[2 * ks], a0); a1 = MFMA(cf, wof[2 * ks + 1], a1); }
      const float v = q == 0 ? a0[0] : a1[0];
      const unsigned d = canon(pk2(v, dppf<0x101>(v)));
      v4u o = {d, dppu<0x102>(d), dppu<0x104>(d), dppu<0x106>(d)};
      if (lane < 32 && (lane & 7) == 0) st16(rs, S5_OFF + head * 1024 + part * 512 + wave * 64 + lane * 2, o);
    }
    STAMP(5);
  } else {
    const int e = r - 16, ti = xc * 8 + e, split = 4 * xc + (e & 3), hg = e >> 2;
    const int s1off = S1_OFF + (8 + xc) * S1_BYTES + (t & 255) * 16;
    v4u wm[2]; unsigned short cv[2];
    {
      const u16* p = w_mod + (size_t)(t >> 1) * 3072 + ti * 16 + (t & 1) * 8;
      wm[0] = *(const v4u*)p; wm[1] = *(const v4u*)(p + (size_t)256 * 3072);
      cv[0] = cvec[t >> 1]; cv[1] = cvec[(t >> 1) + 256];
    }
    const v4u snap = ld16(rs, s1off);
    CBAR();
    {
      const v4u sv = {SENT, SENT, SENT, SENT};
      const int base = CLR_OFF + ti * CLR_PIECES * 16;
      st16(rs, base + t * 16, sv); st16(rs, base + (t + 512) * 16, sv);
      if (t < CLR_PIECES - 1024) st16(rs, base + (t + 1024) * 16, sv);
    }
    CBAR();
    LAS unsigned char* qa = SM;
    LAS unsigned char* Pm = SM + 16896;
    LAS float* wmax = (LAS float*)(SM + 21504);
    LAS float* wsum = wmax + 128;
    LAS float* apart = (LAS float*)(SM + 22528);
    LAS unsigned* pub = (LAS unsigned*)(SM + 23040);
    {
      const float c0 = bflo(cv[0]), c1 = bflo(cv[1]);
      const unsigned s01 = pk2(c0 * __builtin_amdgcn_rcpf(1.f + __expf(-c0)), c1 * __builtin_amdgcn_rcpf(1.f + __expf(-c1)));
      interleave(wm[0], wm[1]);
      float a[8] = {dot2(wm[0].x, s01, 0.f), dot2(wm[0].y, s01, 0.f), dot2(wm[0].z, s01, 0.f), dot2(wm[0].w, s01, 0.f),
                    dot2(wm[1].x, s01, 0.f), dot2(wm[1].y, s01, 0.f), dot2(wm[1].z, s01, 0.f), dot2(wm[1].w, s01, 0.f)};
      float b0 = red32(a[0], a[4]), b1v = red32(a[1], a[5]), b2v = red32(a[2], a[6]), b3 = red32(a[3], a[7]);
      float d0 = red16(b0, b2v), d1 = red16(b1v, b3);
      d0 += dppf<0x128>(d0); d1 += dppf<0x128>(d1); d0 += dppf<0x124>(d0); d1 += dppf<0x124>(d1); d0 += dppf<0x122>(d0); d1 += dppf<0x122>(d1);
      if ((lane & 14) == 0) { f32x2 o = {d0, d1}; *(LAS f32x2*)(apart + wave * 16 + (lane & 1) * 8 + 4 * (lane >> 5) + 2 * ((lane >> 4) & 1)) = o; }
    }
    VMWAIT0();
    bar();
    STAMP(1);
    const int pidx = (ti < 32) ? 2 * (4 * ti + (lane & 3)) : 2 * (4 * (ti - 32) + (lane & 3)) + 1;
    const int puboff = S1_OFF + (lane >> 2) * S1_BYTES + pidx * 16;
    unsigned tagc = ((unsigned)wave << 24) + 1u;
    if (wave == 0) {
      f32x4 s = {0, 0, 0, 0};
#pragma unroll
      for (int w = 0; w < 8; w++) s += *(LAS f32x4*)(apart + w * 16 + (lane & 3) * 4);
      const unsigned v01 = pk2(s[0], s[1]), v23 = pk2(s[2], s[3]);
      v4u o = {v01, tagc, v23, tagc};
      st16(rs, puboff, o); tagc++;
      if (lane < 4) { v2u p2 = {v01, v23}; *(LAS v2u*)(pub + lane * 2) = p2; }
    }
    STAMP(2);
#pragma unroll
    for (int i = 0; i < 16; i++) {
      const int key = ((i * 8 + wave) + hg * 64) & 127;
      dma16(ckv + (size_t)(split * 128 + key) * 512 + ((lane ^ swz(key)) * 8), L + key * 1024);
    }
#define REPUB() do { v2u p2_ = *(LAS v2u*)(pub + (lane & 3) * 2); v4u o_ = {p2_.x, tagc, p2_.y, tagc}; st16(rs, puboff, o_); tagc++; } while (0)
    VMWAIT0();
    bar();
    STAMP(3);
    bf16x8 ka[16], va[16];
    {
      const int key = wave * 16 + n, f2 = swz(key);
#pragma unroll
      for (int s2 = 0; s2 < 16; s2++) ka[s2] = *(const LAS bf16x8*)(L + key * 1024 + (((4 * s2 + q) ^ f2) * 16));
#pragma unroll
      for (int i = 0; i < 16; i++) va[i] = trfrag<1024, true>(L, 32 * (i & 3), wave * 8 + 2 * (i >> 2), lane);
    }
    if (wave < 4) {
      int spins = 0;
      for (;;) {
        CBAR(); v4u v = ld16(rs, s1off);
        const bool ok = (v.y != snap.y) & (v.w != snap.w);
        if (__all(ok) || ++spins > SPIN_CAP) break;
        if ((spins & 15) == 0) REPUB();
      }
    }
    bar();
    STAMP(4);
    {
      const int off = S2_OFF + hg * 16384 + t * 16;
      v4u v0, v1; bool ok0 = false, ok1 = false; int spins = 0;
      for (;;) {
        CBAR();
        if (!ok0) v0 = ld16(rs, off);
        if (!ok1) v1 = ld16(rs, off + 8192);
        ok0 = valid4(v0); ok1 = valid4(v1);
        if (__all(ok0 & ok1) || ++spins > SPIN_CAP) break;
        if ((spins & 15) == 0) REPUB();
      }
      *(LAS v4u*)(qa + wave * 1056 + lane * 16) = v0;
      *(LAS v4u*)(qa + (8 + wave) * 1056 + lane * 16) = v1;
    }
    bar();
    STAMP(5);
    f32x4 sc = {0, 0, 0, 0};
#pragma unroll
    for (int s2 = 0; s2 < 16; s2++) sc = MFMA(ka[s2], *(const LAS bf16x8*)(qa + n * 1056 + s2 * 64 + q * 16), sc);
    float mx = fmaxf(fmaxf(sc[0], sc[1]), fmaxf(sc[2], sc[3]));
    mx = max16(mx); mx = max32(mx);
    if (lane < 16) wmax[lane * 8 + wave] = mx;
    bar();
    {
      f32x4 m0 = *(LAS f32x4*)(wmax + n * 8), m1 = *(LAS f32x4*)(wmax + n * 8 + 4);
      mx = fmaxf(fmaxf(fmaxf(m0[0], m0[1]), fmaxf(m0[2], m0[3])), fmaxf(fmaxf(m1[0], m1[1]), fmaxf(m1[2], m1[3])));
    }
    const float p0 = __builtin_amdgcn_exp2f(sc[0] - mx), p1 = __builtin_amdgcn_exp2f(sc[1] - mx), p2 = __builtin_amdgcn_exp2f(sc[2] - mx), p3 = __builtin_amdgcn_exp2f(sc[3] - mx);
    { v2u o = {pk2(p0, p1), pk2(p2, p3)}; *(LAS v2u*)(Pm + n * 288 + (wave * 16 + q * 4) * 2) = o; }
    float ps = (p0 + p1) + (p2 + p3);
    ps = red16(ps, ps); ps = red32(ps, ps);
    if (lane < 16) wsum[lane * 8 + wave] = ps;
    bar();
    {
      bf16x8 bp[4];
#pragma unroll
      for (int ks = 0; ks < 4; ks++) bp[ks] = *(const LAS bf16x8*)(Pm + n * 288 + ks * 64 + q * 16);
      f32x4 acc[4];
#pragma unroll
      for (int T = 0; T < 4; T++) {
        acc[T] = (f32x4){0, 0, 0, 0};
#pragma unroll
        for (int ks = 0; ks < 4; ks++) acc[T] = MFMA(va[T * 4 + ks], bp[ks], acc[T]);
      }
      unsigned d0[4], d1[4];
#pragma unroll
      for (int T = 0; T < 4; T++) { d0[T] = canon(pk2(acc[T][0], acc[T][1])); d1[T] = canon(pk2(acc[T][2], acc[T][3])); }
      auto r0 = __builtin_amdgcn_permlane16_swap(d0[0], d0[1], false, false);
      auto r1 = __builtin_amdgcn_permlane16_swap(d1[0], d1[1], false, false);
      auto r2 = __builtin_amdgcn_permlane16_swap(d0[2], d0[3], false, false);
      auto r3 = __builtin_amdgcn_permlane16_swap(d1[2], d1[3], false, false);
      v4u oa = {r0[0], r1[0], r0[1], r1[1]}, ob = {r2[0], r3[0], r2[1], r3[1]};
      const int c = wave * 64 + (q & 1) * 16 + (q & 2) * 4;
      const int o3 = S3_OFF + ((hg * 16 + n) * 32 + split) * 1024 + c * 2;
      st16(rs, o3, oa); st16(rs, o3 + 64, ob);
      if (wave == 7) {
        f32x4 s0 = *(LAS f32x4*)(wsum + n * 8), s1 = *(LAS f32x4*)(wsum + n * 8 + 4);
        const float l = ((s0[0] + s0[1]) + (s0[2] + s0[3])) + ((s1[0] + s1[1]) + (s1[2] + s1[3]));
        v2u o2 = {canon(__float_as_uint(mx)), canon(__float_as_uint(l))};
        if (lane < 16) st8(rs, ST_OFF + ((hg * 16 + lane) * 32 + split) * 8, o2);
      }
    }
    STAMP(6);
    if (wave == 0) {
      int spins = 0;
      for (;;) {
        CBAR(); v4u v = ld16(rs, DN_OFF + xc * 128);
        if (v.x != SENT || ++spins > SPIN_CAP) break;
        if ((spins & 3) == 0) REPUB();
        __builtin_amdgcn_s_sleep(8);
      }
    }
    STAMP(7);
  }
}

extern "C" void kernel_launch(void* const* d_in, const int* in_sizes, int n_in, void* d_out, int out_size, void* d_ws, size_t ws_size, hipStream_t stream) {
  (void)in_sizes; (void)n_in; (void)out_size; (void)ws_size;
  (void)hipFuncSetAttribute((const void*)fused_kernel, hipFuncAttributeMaxDynamicSharedMemorySize, L_TOTAL);
  fused_kernel<<<256, NT, L_TOTAL, stream>>>((const u16*)d_in[0], (const u16*)d_in[1], (const u16*)d_in[2], (const u16*)d_in[3], (const u16*)d_in[4], (const u16*)d_in[5],
                                             (const u16*)d_in[7], (const u16*)d_in[8], (const u16*)d_in[9], (const u16*)d_in[10], (const u16*)d_in[11], (const u16*)d_in[12],
                                             (u16*)d_out, (unsigned char*)d_ws);
}
```

```cpp
#include <hip/hip_runtime.h>
#include <stdint.h>

typedef unsigned short u16;
typedef unsigned v4u __attribute__((ext_vector_type(4)));
typedef unsigned v2u __attribute__((ext_vector_type(2)));
typedef float f32x4 __attribute__((ext_vector_type(4)));
typedef float f32x2 __attribute__((ext_vector_type(2)));
typedef short bf16x8 __attribute__((ext_vector_type(8)));
typedef short s16x4 __attribute__((ext_vector_type(4)));
typedef __bf16 bf2 __attribute__((ext_vector_type(2)));
#define LAS __attribute__((address_space(3)))

constexpr unsigned SENT = 0xFFFFFFFFu;
constexpr int NT = 512;
constexpr int SPIN_CAP = 1 << 20;
#define PSL 0
#define POLLSLEEP() do { if (PSL) __builtin_amdgcn_s_sleep(PSL); } while (0)
#define CLRAUX 16
#define NTAUX 2
#define NTS " nt"
#define LDNT(T, p) __builtin_nontemporal_load((const T*)(p))
constexpr int S1_OFF = 0, S1_BYTES = 4096;
constexpr int CLR_OFF = 65536;
constexpr int S2_OFF = CLR_OFF;
constexpr int S4_OFF = S2_OFF + 32768;
constexpr int S5_OFF = S4_OFF + 16384;
constexpr int SL_OFF = S5_OFF + 8192;
constexpr int S6_OFF = SL_OFF + 4096;
constexpr int DN_OFF = S6_OFF + 3072;
constexpr int ST_OFF = S6_OFF + 4096;
constexpr int S3_OFF = ST_OFF + 8192;
constexpr int CLR_END = S3_OFF + (1 << 20);
constexpr unsigned FLAG = 0x80000000u;
constexpr int WS_USED = CLR_END;
#define STAMP(i) do {} while (0)
#define STAMP_DECL do {} while (0)
#define STAMP_FLUSH() do {} while (0)

constexpr int L_CKV = 0;
constexpr int L_SM = 131072;
constexpr int L_TOTAL = 131072 + 30720;

__device__ __forceinline__ float bflo(unsigned w) { return __uint_as_float(w << 16); }
__device__ __forceinline__ float bfhi(unsigned w) { return __uint_as_float(w & 0xffff0000u); }
__device__ __forceinline__ unsigned pk2(float a, float b) { bf2 p; p.x = (__bf16)a; p.y = (__bf16)b; return __builtin_bit_cast(unsigned, p); }
__device__ __forceinline__ float rbf(float a) { return (float)(__bf16)a; }
__device__ __forceinline__ unsigned canon(unsigned w) { return w == SENT ? 0x7FC07FC0u : w; }
__device__ __forceinline__ float dot2(unsigned a, unsigned b, float c) { return __builtin_amdgcn_fdot2_f32_bf16(__builtin_bit_cast(bf2, a), __builtin_bit_cast(bf2, b), c, false); }
__device__ __forceinline__ void bar() { __builtin_amdgcn_sched_barrier(0); asm volatile("s_waitcnt lgkmcnt(0)\n\ts_barrier" ::: "memory"); __builtin_amdgcn_sched_barrier(0); }
template <int CTRL> __device__ __forceinline__ float dppf(float v) { return __int_as_float(__builtin_amdgcn_update_dpp(0, __float_as_int(v), CTRL, 0xf, 0xf, false)); }
template <int CTRL> __device__ __forceinline__ unsigned dppu(unsigned v) { return (unsigned)__builtin_amdgcn_update_dpp(0, (int)v, CTRL, 0xf, 0xf, false); }
__device__ __forceinline__ float red32(float a, float b) {
  auto r = __builtin_amdgcn_permlane32_swap(__float_as_uint(a), __float_as_uint(b), false, false);
  return __uint_as_float(r[0]) + __uint_as_float(r[1]);
}
__device__ __forceinline__ float red16(float a, float b) {
  auto r = __builtin_amdgcn_permlane16_swap(__float_as_uint(a), __float_as_uint(b), false, false);
  return __uint_as_float(r[0]) + __uint_as_float(r[1]);
}
__device__ __forceinline__ float max32(float a) { auto r = __builtin_amdgcn_permlane32_swap(__float_as_uint(a), __float_as_uint(a), false, false); return fmaxf(__uint_as_float(r[0]), __uint_as_float(r[1])); }
__device__ __forceinline__ float max16(float a) { auto r = __builtin_amdgcn_permlane16_swap(__float_as_uint(a), __float_as_uint(a), false, false); return fmaxf(__uint_as_float(r[0]), __uint_as_float(r[1])); }
__device__ __forceinline__ float wave_sum(float v) {
  v += dppf<0x128>(v); v += dppf<0x124>(v); v += dppf<0x122>(v); v += dppf<0x121>(v);
  v = red16(v, v); v = red32(v, v); return v;
}
__device__ __forceinline__ float wave_max(float v) {
  v = fmaxf(v, dppf<0x128>(v)); v = fmaxf(v, dppf<0x124>(v)); v = fmaxf(v, dppf<0x122>(v)); v = fmaxf(v, dppf<0x121>(v));
  v = max16(v); v = max32(v); return v;
}
__device__ __forceinline__ unsigned ilo(unsigned a, unsigned b) { return __builtin_amdgcn_perm(b, a, 0x05040100u); }
__device__ __forceinline__ unsigned ihi(unsigned a, unsigned b) { return __builtin_amdgcn_perm(b, a, 0x07060302u); }
__device__ __forceinline__ void interleave(v4u& a, v4u& b) {
  v4u x, y;
  x.x = ilo(a.x, b.x); x.y = ihi(a.x, b.x); x.z = ilo(a.y, b.y); x.w = ihi(a.y, b.y);
  y.x = ilo(a.z, b.z); y.y = ihi(a.z, b.z); y.z = ilo(a.w, b.w); y.w = ihi(a.w, b.w);
  a = x; b = y;
}
__device__ __forceinline__ bool valid4(v4u v) { return (v.x != SENT) & (v.y != SENT) & (v.z != SENT) & (v.w != SENT); }
typedef __amdgpu_buffer_rsrc_t rsrc_t;
__device__ __forceinline__ v4u ld16(rsrc_t r, int off) { return __builtin_amdgcn_raw_buffer_load_b128(r, off, 0, 16); }
__device__ __forceinline__ v2u ld8(rsrc_t r, int off) { return __builtin_amdgcn_raw_buffer_load_b64(r, off, 0, 16); }
__device__ __forceinline__ void st16(rsrc_t r, int off, v4u v) { __builtin_amdgcn_raw_buffer_store_b128(v, r, off, 0, 16); }
__device__ __forceinline__ void st8(rsrc_t r, int off, v2u v) { __builtin_amdgcn_raw_buffer_store_b64(v, r, off, 0, 16); }
__device__ __forceinline__ void glds16(const void* g, unsigned lds_base) {
  unsigned sv; asm volatile("s_mov_b32 %0, m0\n\ts_mov_b32 m0, %2\n\ts_nop 0\n\tglobal_load_lds_dwordx4 %1, off" NTS "\n\ts_mov_b32 m0, %0" : "=&s"(sv) : "v"(g), "s"(lds_base) : "memory");
}
#define CBAR() do { asm volatile("" ::: "memory"); __builtin_amdgcn_sched_barrier(0); } while (0)

__device__ __forceinline__ int swz(int row) { return ((row & 3) | ((row >> 1) & 4)) << 1; }
template <int AUX> __device__ __forceinline__ void dma16a(const void* g, LAS unsigned char* l) { __builtin_amdgcn_global_load_lds((const __attribute__((address_space(1))) unsigned*)g, (LAS unsigned*)l, 16, 0, AUX); }
#define dma16 dma16a<NTAUX>
#define NARROW 2
#define dma16n dma16a<NARROW>
#define LDNAR(T, p) __builtin_nontemporal_load((const T*)(p))
template <int RB, bool SW>
__device__ __forceinline__ bf16x8 trfrag(const LAS unsigned char* img, int k0, int ch0, int lane) {
  const int g = lane >> 4, qq = (lane & 15) >> 2, pp = lane & 3;
  s16x4 h[2];
#pragma unroll
  for (int tt = 0; tt < 2; tt++) {
    const int row = k0 + 8 * g + 4 * tt + qq;
    const int ch = SW ? ((ch0 + (pp >> 1)) ^ swz(row)) : (ch0 + (pp >> 1));
    h[tt] = __builtin_amdgcn_ds_read_tr16_b64_v4i16((LAS s16x4*)(img + row * RB + ch * 16 + (pp & 1) * 8));
  }
  return (bf16x8){h[0].x, h[0].y, h[0].z, h[0].w, h[1].x, h[1].y, h[1].z, h[1].w};
}
#define MFMA(a, b, c) __builtin_amdgcn_mfma_f32_16x16x32_bf16(a, b, c, 0, 0, 0)
#define VMWAIT0() do { asm volatile("" ::: "memory"); __builtin_amdgcn_s_waitcnt(0x0F70); asm volatile("" ::: "memory"); } while (0)
#define LGWAIT0() asm volatile("s_waitcnt lgkmcnt(0)" ::: "memory")
__device__ __forceinline__ float wave_sum2(float a, float b) {
  a += dppf<0x128>(a); b += dppf<0x128>(b); a += dppf<0x124>(a); b += dppf<0x124>(b);
  a += dppf<0x122>(a); b += dppf<0x122>(b); a += dppf<0x121>(a); b += dppf<0x121>(b);
  float v = red16(a, b); return red32(v, v);
}

typedef unsigned long long u64;
__device__ __forceinline__ void ldm4(rsrc_t r, int off, unsigned s1, unsigned s2, unsigned s3, v4u& a, v4u& b, v4u& c, v4u& d, u64 m0, u64 m1, u64 m2, u64 m3) {
  u64 sv;
  asm volatile("s_mov_b64 %0, exec\n\t"
               "s_mov_b64 exec, %10\n\tbuffer_load_dwordx4 %1, %5, %6, 0 offen sc1\n\t"
               "s_mov_b64 exec, %11\n\tbuffer_load_dwordx4 %2, %5, %6, %7 offen sc1\n\t"
               "s_mov_b64 exec, %12\n\tbuffer_load_dwordx4 %3, %5, %6, %8 offen sc1\n\t"
               "s_mov_b64 exec, %13\n\tbuffer_load_dwordx4 %4, %5, %6, %9 offen sc1\n\t"
               "s_mov_b64 exec, %0\n\ts_waitcnt vmcnt(0)"
               : "=&s"(sv), "+v"(a), "+v"(b), "+v"(c), "+v"(d) : "v"(off), "s"(r), "s"(s1), "s"(s2), "s"(s3), "s"(m0), "s"(m1), "s"(m2), "s"(m3) : "memory");
}
__device__ __forceinline__ void ldm2(rsrc_t r, int off, unsigned s1, v4u& a, v4u& b, u64 m0, u64 m1) {
  u64 sv;
  asm volatile("s_mov_b64 %0, exec\n\t"
               "s_mov_b64 exec, %6\n\tbuffer_load_dwordx4 %1, %3, %4, 0 offen sc1\n\t"
               "s_mov_b64 exec, %7\n\tbuffer_load_dwordx4 %2, %3, %4, %5 offen sc1\n\t"
               "s_mov_b64 exec, %0\n\ts_waitcnt vmcnt(0)"
               : "=&s"(sv), "+v"(a), "+v"(b) : "v"(off), "s"(r), "s"(s1), "s"(m0), "s"(m1) : "memory");
}
__device__ __forceinline__ void ldm6(rsrc_t r, int off, v4u (&h)[6], u64 m0, u64 m1, u64 m2, u64 m3, u64 m4, u64 m5) {
  u64 sv;
  asm volatile("s_mov_b64 %0, exec\n\t"
               "s_mov_b64 exec, %9\n\tbuffer_load_dwordx4 %1, %7, %8, 0 offen sc1\n\t"
               "s_mov_b64 exec, %10\n\tbuffer_load_dwordx4 %2, %7, %8, 0 offen offset:512 sc1\n\t"
               "s_mov_b64 exec, %11\n\tbuffer_load_dwordx4 %3, %7, %8, 0 offen offset:1024 sc1\n\t"
               "s_mov_b64 exec, %12\n\tbuffer_load_dwordx4 %4, %7, %8, 0 offen offset:1536 sc1\n\t"
               "s_mov_b64 exec, %13\n\tbuffer_load_dwordx4 %5, %7, %8, 0 offen offset:2048 sc1\n\t"
               "s_mov_b64 exec, %14\n\tbuffer_load_dwordx4 %6, %7, %8, 0 offen offset:2560 sc1\n\t"
               "s_mov_b64 exec, %0\n\ts_waitcnt vmcnt(0)"
               : "=&s"(sv), "+v"(h[0]), "+v"(h[1]), "+v"(h[2]), "+v"(h[3]), "+v"(h[4]), "+v"(h[5]) : "v"(off), "s"(r), "s"(m0), "s"(m1), "s"(m2), "s"(m3), "s"(m4), "s"(m5) : "memory");
}
__device__ __forceinline__ u64 inval(v4u v) { return __builtin_amdgcn_ballot_w64(!valid4(v)); }

__device__ __forceinline__ unsigned mx4(v4u v) { return max(max(v.x, v.y), max(v.z, v.w)); }
__device__ __forceinline__ void stclr(rsrc_t r, int off, v4u v) { __builtin_amdgcn_raw_buffer_store_b128(v, r, off, 0, CLRAUX); }

#define RGQ(i) (4 * (8 * ((i) & 3) + wave) + ((((i) >> 2) + cs) & 3))
__global__ void __launch_bounds__(NT, 2) fused_kernel(
    const u16* __restrict__ x, const u16* __restrict__ ckv, const u16* __restrict__ w_q, const u16* __restrict__ w_uk,
    const u16* __restrict__ w_uv, const u16* __restrict__ w_o, const u16* __restrict__ w1, const u16* __restrict__ b1,
    const u16* __restrict__ w2, const u16* __restrict__ b2, const u16* __restrict__ cvec, const u16* __restrict__ w_mod,
    u16* __restrict__ out, unsigned char* ws) {
  extern __shared__ __attribute__((aligned(16))) unsigned char lds_raw[];
  LAS unsigned char* L = (LAS unsigned char*)lds_raw;
  const int t = threadIdx.x, lane = t & 63, wave = __builtin_amdgcn_readfirstlane(t >> 6);
  const int b = blockIdx.x, xc = b & 7, r = b >> 3;
  const int n = lane & 15, q = lane >> 4;
  const rsrc_t rs = __builtin_amdgcn_make_buffer_rsrc((void*)ws, 0, WS_USED, 0x00020000);
  LAS unsigned char* SM = L + L_SM;
  STAMP_DECL;
  STAMP(0);

  if (r < 16) {
    const int head = 4 * xc + (r >> 2), cs = r & 3;
    const int s1off = S1_OFF + xc * S1_BYTES + (t & 255) * 16;
    const v4u snap = ld16(rs, s1off);
    const v4u xv = *(const v4u*)(x + lane * 8);
    unsigned xw = ((const unsigned*)x)[t & 255];
    CBAR();
#pragma unroll
    for (int i = 0; i < 4; i++) {
      const int rg = RGQ(i), row = 4 * rg + q;
      dma16(w_q + (size_t)row * 4096 + head * 128 + ((n ^ swz(row)) * 8), L + rg * 1024);
    }
    CBAR();
    CBAR();
#pragma unroll
    for (int i = 4; i < 16; i++) {
      const int rg = RGQ(i), row = 4 * rg + q;
      dma16(w_q + (size_t)row * 4096 + head * 128 + ((n ^ swz(row)) * 8), L + rg * 1024);
    }
    CBAR();
    v4u wkr[4];
    {
      const u16* pk = w_uk + (size_t)(cs * 128 + (t >> 4)) * 4096 + head * 128 + n * 8;
#pragma unroll
      for (int i = 0; i < 4; i++) wkr[i] = LDNT(v4u, pk + (size_t)i * 32 * 4096);
    }
    CBAR();
    float xs[8] = {bflo(xv.x), bfhi(xv.x), bflo(xv.y), bfhi(xv.y), bflo(xv.z), bfhi(xv.z), bflo(xv.w), bfhi(xv.w)};
    float s = 0;
#pragma unroll
    for (int i = 0; i < 8; i++) s += xs[i];
    const float mean = wave_sum(s) * (1.f / 512.f);
    float vs = 0;
#pragma unroll
    for (int i = 0; i < 8; i++) { float d = xs[i] - mean; vs += d * d; }
    const float rstd = rsqrtf(wave_sum(vs) * (1.f / 512.f) + 1e-5f);
    float ln0 = rbf((bflo(xw) - mean) * rstd), ln1 = rbf((bfhi(xw) - mean) * rstd);
    asm volatile("" : "+v"(ln0), "+v"(ln1));
    VMWAIT0();
    bar();
    STAMP(1);
    LAS unsigned* hp = (LAS unsigned*)(SM);
    LAS unsigned* q16 = (LAS unsigned*)(SM + 1024);
    v4u v1 = snap;
    if (wave < 4) v1 = ld16(rs, s1off);
    bf16x8 wqf[16];
#pragma unroll
    for (int s2 = 0; s2 < 16; s2++) wqf[s2] = trfrag<256, true>(L, 32 * s2, 2 * wave, lane);
    bar();
#pragma unroll
    for (int i = 0; i < 4; i++) { const int row = (t >> 4) + 32 * i; *(LAS v4u*)(L + row * 256 + ((n ^ swz(row)) * 16)) = wkr[i]; }
    if (wave < 4) {
      int spins = 0;
      while (!__all((v1.y != snap.y) & (v1.w != snap.w)) && ++spins <= SPIN_CAP) { CBAR(); v1 = ld16(rs, s1off); }
      const bool odd = t & 1;
      const unsigned send = odd ? v1.x : v1.z;
      const unsigned recv = dppu<0xB1>(send);
      const unsigned sa = odd ? recv : v1.x, ca = odd ? v1.z : recv;
      const float h0 = rbf(ln0 * rbf(1.f + bflo(ca))) + bflo(sa), h1v = rbf(ln1 * rbf(1.f + bfhi(ca))) + bfhi(sa);
      hp[t] = pk2(h0, h1v);
    }
    bar();
    STAMP(2);
    bf16x8 wk[4];
    {
      f32x4 a0 = {0, 0, 0, 0};
#pragma unroll
      for (int s2 = 0; s2 < 16; s2++) a0 = MFMA(*(const LAS bf16x8*)(hp + s2 * 16 + q * 4), wqf[s2], a0);
#pragma unroll
      for (int i = 0; i < 4; i++) wk[i] = *(const LAS bf16x8*)(L + (wave * 16 + n) * 256 + (((4 * i + q) ^ swz(n)) * 16));
      const unsigned d = pk2(a0[0], dppf<0x101>(a0[0]));
      if (lane < 16 && !(lane & 1)) q16[wave * 8 + (lane >> 1)] = d;
    }
    bar();
    {
      f32x4 a = {0, 0, 0, 0};
#pragma unroll
      for (int i = 0; i < 4; i++) a = MFMA(wk[i], *(const LAS bf16x8*)(q16 + i * 16 + q * 4), a);
      const float SC = 0.08838834764831845f * 1.4426950408889634f;
      v2u o = {canon(pk2(a[0] * SC, a[1] * SC)), canon(pk2(a[2] * SC, a[3] * SC))};
      if (n == 0) st8(rs, S2_OFF + head * 1024 + (cs * 128 + wave * 16 + q * 4) * 2, o);
    }
    STAMP(3);
    bar();
    if (r < 8) {
      const int ti = xc * 8 + r;
      v4u wm[4]; unsigned short cv[4];
      {
        const u16* p = w_mod + (size_t)(t >> 2) * 3072 + 1024 + ti * 32 + (t & 3) * 8;
#pragma unroll
        for (int i = 0; i < 4; i++) { wm[i] = LDNAR(v4u, p + (size_t)i * 128 * 3072); cv[i] = cvec[(t >> 2) + 128 * i]; }
      }
      CBAR();
#pragma unroll
      for (int i = 0; i < 8; i++) {
        const int rg = 8 * i + wave, row = 8 * rg + (lane >> 3);
        dma16(w_o + (size_t)(xc * 512 + row) * 512 + r * 64 + (((lane & 7) ^ ((row & 2) | ((row >> 1) & 4))) * 8), L + rg * 1024);
      }
      CBAR();
      LAS float* apart = (LAS float*)(SM + 2048);
      LAS float* fpart = (LAS float*)(SM + 3072);
      LAS unsigned char* cst = SM + 4096;
      {
        float sv[4];
#pragma unroll
        for (int i = 0; i < 4; i++) { float c = bflo(cv[i]); sv[i] = c * __builtin_amdgcn_rcpf(1.f + __expf(-c)); }
        const unsigned s01 = pk2(sv[0], sv[1]), s23 = pk2(sv[2], sv[3]);
        interleave(wm[0], wm[1]); interleave(wm[2], wm[3]);
        float a[8];
        a[0] = dot2(wm[2].x, s23, dot2(wm[0].x, s01, 0.f)); a[1] = dot2(wm[2].y, s23, dot2(wm[0].y, s01, 0.f));
        a[2] = dot2(wm[2].z, s23, dot2(wm[0].z, s01, 0.f)); a[3] = dot2(wm[2].w, s23, dot2(wm[0].w, s01, 0.f));
        a[4] = dot2(wm[3].x, s23, dot2(wm[1].x, s01, 0.f)); a[5] = dot2(wm[3].y, s23, dot2(wm[1].y, s01, 0.f));
        a[6] = dot2(wm[3].z, s23, dot2(wm[1].z, s01, 0.f)); a[7] = dot2(wm[3].w, s23, dot2(wm[1].w, s01, 0.f));
        float b0 = red32(a[0], a[4]), b1x = red32(a[1], a[5]), b2x = red32(a[2], a[6]), b3 = red32(a[3], a[7]);
        float c0 = red16(b0, b2x), c1 = red16(b1x, b3);
        c0 += dppf<0x128>(c0); c1 += dppf<0x128>(c1); c0 += dppf<0x124>(c0); c1 += dppf<0x124>(c1);
        if ((lane & 12) == 0) { f32x2 o = {c0, c1}; *(LAS f32x2*)(apart + wave * 32 + (lane & 3) * 8 + 4 * (lane >> 5) + 2 * ((lane >> 4) & 1)) = o; }
      }
      if (wave < 4) {
        int spins = 0;
        for (;;) { CBAR(); v4u v = ld16(rs, s1off); if (__all(((v.y & v.w) & FLAG) != 0) || ++spins > SPIN_CAP) break; __builtin_amdgcn_s_sleep(2); }
      }
      VMWAIT0();
      bar();
      if (t < 4) {
        f32x4 s0 = {0, 0, 0, 0}, s1 = {0, 0, 0, 0};
#pragma unroll
        for (int w = 0; w < 8; w++) { s0 += *(LAS f32x4*)(apart + w * 32 + t * 8); s1 += *(LAS f32x4*)(apart + w * 32 + t * 8 + 4); }
        v4u o = {canon(pk2(s0[0], s0[1])), canon(pk2(s0[2], s0[3])), canon(pk2(s1[0], s1[1])), canon(pk2(s1[2], s1[3]))};
        st16(rs, SL_OFF + ti * 64 + t * 16, o);
      }
      bf16x8 wof[8];
      {
        const int g = lane >> 4, qq = (lane & 15) >> 2, pp = lane & 3;
#pragma unroll
        for (int i = 0; i < 8; i++) {
          s16x4 h[2];
#pragma unroll
          for (int tt = 0; tt < 2; tt++) {
            const int row = 32 * ((wave >> 2) * 8 + i) + 8 * g + 4 * tt + qq;
            const int ch = (2 * (wave & 3) + (pp >> 1)) ^ ((row & 2) | ((row >> 1) & 4));
            h[tt] = __builtin_amdgcn_ds_read_tr16_b64_v4i16((LAS s16x4*)(L + row * 128 + ch * 16 + (pp & 1) * 8));
          }
          wof[i] = (bf16x8){h[0].x, h[0].y, h[0].z, h[0].w, h[1].x, h[1].y, h[1].z, h[1].w};
        }
      }
      STAMP(4);
      if (wave < 2) {
        v4u v; int spins = 0;
        for (;;) { CBAR(); v = ld16(rs, S4_OFF + xc * 2048 + t * 16); if (__all(mx4(v) != SENT) || ++spins > SPIN_CAP) break; POLLSLEEP(); }
        *(LAS v4u*)(cst + t * 16) = v;
      }
      bar();
      STAMP(10);
      {
        const LAS unsigned char* ap = cst + ((2 * (wave >> 2)) * 2 + (n & 1)) * 256 + q * 16;
        f32x4 a = {0, 0, 0, 0}, a2 = {0, 0, 0, 0};
#pragma unroll
        for (int i = 0; i < 4; i++) {
          a = MFMA(*(const LAS bf16x8*)(ap + ((2 * i) >> 2) * 512 + ((2 * i) & 3) * 64), wof[2 * i], a);
          a2 = MFMA(*(const LAS bf16x8*)(ap + ((2 * i + 1) >> 2) * 512 + ((2 * i + 1) & 3) * 64), wof[2 * i + 1], a2);
        }
        float v = (a[0] + a[1]) + (a2[0] + a2[1]);
        if (wave >= 4 && lane < 16) fpart[(wave - 4) * 16 + lane] = v;
        bar();
        if (wave < 4) {
          v += fpart[wave * 16 + n];
          const unsigned d = canon(pk2(v, dppf<0x101>(v)));
          v4u o = {d, dppu<0x102>(d), dppu<0x104>(d), dppu<0x106>(d)};
          if (lane < 16 && (lane & 7) == 0) st16(rs, S5_OFF + xc * 1024 + r * 128 + wave * 32 + lane * 2, o);
        }
      }
      STAMP(11);
    } else {
      const int ti = xc * 8 + (r - 8);
      LAS unsigned char* W2I = L;
      const float b2v = bflo(b2[ti * 8 + (lane & 7)]);
      const float x8 = bflo(x[ti * 8 + (lane & 7)]);
      CBAR();
#pragma unroll
      for (int i = 0; i < 3; i++) { const int rg = 8 * i + wave; if (rg < 22) { int row = rg * 64 + lane; row = row < 1376 ? row : 1375; dma16n(w2 + (size_t)row * 512 + ti * 8, W2I + rg * 1024); } }
      CBAR();
      LAS float* hpart = (LAS float*)(SM + 2048);
      LAS unsigned* sbuf = (LAS unsigned*)(SM + 2560);
      if (wave < 4) {
        int spins = 0;
        for (;;) { CBAR(); v4u v = ld16(rs, s1off); if (__all(((v.y & v.w) & FLAG) != 0) || ++spins > SPIN_CAP) break; __builtin_amdgcn_s_sleep(2); }
      }
      VMWAIT0();
      bar();
      STAMP(5);
      bf16x8 w2f[6];
#pragma unroll
      for (int i = 0; i < 6; i++) {
        const int ks = wave + 8 * i < 43 ? wave + 8 * i : 0;
        const int g = lane >> 4, qq = (lane & 15) >> 2, pp = lane & 3;
        s16x4 h0 = __builtin_amdgcn_ds_read_tr16_b64_v4i16((LAS s16x4*)(W2I + (32 * ks + 8 * g + qq) * 16 + (pp & 1) * 8));
        s16x4 h1 = __builtin_amdgcn_ds_read_tr16_b64_v4i16((LAS s16x4*)(W2I + (32 * ks + 8 * g + 4 + qq) * 16 + (pp & 1) * 8));
        w2f[i] = (bf16x8){h0.x, h0.y, h0.z, h0.w, h1.x, h1.y, h1.z, h1.w};
      }
      float xn8 = 0.f, gbv = 0.f;
      if (wave == 0) {
        const int off = lane < 8 ? S5_OFF + lane * 1024 + ti * 16 : (lane & 1) ? SL_OFF + 3072 + ti * 16 : SL_OFF + ti * 16;
        v4u v; int spins = 0;
        for (;;) { CBAR(); v = ld16(rs, off); if (__all(mx4(v) != SENT) || ++spins > SPIN_CAP) break; __builtin_amdgcn_s_sleep(4); }
        if (lane < 10) *(LAS v4u*)(sbuf + lane * 4) = v;
        LGWAIT0();
        const LAS u16* sh = (const LAS u16*)sbuf;
        float ao = 0;
#pragma unroll
        for (int k = 0; k < 8; k++) ao += bflo(sh[k * 8 + (lane & 7)]);
        const float ga8 = bflo(sh[64 + (lane & 7)]);
        gbv = bflo(sh[72 + (lane & 7)]);
        xn8 = rbf(x8 + rbf(ga8 * rbf(ao)));
      }
      bar();
      STAMP(6);
      {
        v4u hv[6]; int spins = 0;
        const int off = S6_OFF + wave * 64 + q * 16, off5 = wave < 3 ? off + 2560 : off;
        for (;;) {
          CBAR();
#pragma unroll
          for (int i = 0; i < 5; i++) hv[i] = ld16(rs, off + i * 512);
          hv[5] = ld16(rs, off5);
          const unsigned m = max(max(max(mx4(hv[0]), mx4(hv[1])), max(mx4(hv[2]), mx4(hv[3]))), max(mx4(hv[4]), mx4(hv[5])));
          if (__all(m != SENT) || ++spins > SPIN_CAP) break;
          POLLSLEEP();
        }
        if (wave >= 3) hv[5] = (v4u){0, 0, 0, 0};
        STAMP(8);
        f32x4 a = {0, 0, 0, 0}, a2 = {0, 0, 0, 0};
#pragma unroll
        for (int i = 0; i < 3; i++) { a = MFMA(__builtin_bit_cast(bf16x8, hv[2 * i]), w2f[2 * i], a); a2 = MFMA(__builtin_bit_cast(bf16x8, hv[2 * i + 1]), w2f[2 * i + 1], a2); }
        if (lane < 8) hpart[wave * 8 + lane] = a[0] + a2[0];
      }
      bar();
      if (wave == 0) {
        float a = hpart[lane];
        a += dppf<0x128>(a); a = red16(a, a); a = red32(a, a);
        const float o = rbf(rbf(a) + b2v);
        const float res = xn8 + rbf(gbv * o);
        if (lane < 8) out[ti * 8 + lane] = __builtin_bit_cast(u16, (__bf16)res);
      }
      STAMP(9);
    }
    STAMP_FLUSH();
  } else if (r >= 24) {
    const int e = r - 24, head = 4 * xc + (e >> 1), half = e & 1, ti = xc * 8 + e;
    const int s1off = S1_OFF + (8 + xc) * S1_BYTES + (t & 255) * 16;
    const v4u snap = ld16(rs, s1off);
    const int st0 = (43 * ti) >> 4, nst = ((43 * (ti + 1)) >> 4) - st0;
    const int wst = st0 < 168 ? st0 : 168;
    const int mcol = wave * 64 + 8 * (lane & 7) + 4 * (lane >> 5) + 2 * ((lane >> 4) & 1) + ((lane >> 3) & 1);
    const float xme = bflo(x[mcol]);
    const float b1v = bflo(b1[st0 * 8 + (lane < nst * 8 ? lane : 0)]);
#pragma unroll
    for (int i = 0; i < 8; i++) dma16(w_uk + (size_t)(e * 64 + i * 8 + wave) * 4096 + xc * 512 + lane * 8, L + 114688 + wave * 1024);
    CBAR();
    LAS unsigned char* W1I = L + 65536;
    LAS unsigned char* STG = L + 98304;
#pragma unroll
    for (int i = 0; i < 8; i++) {
      const int rg = 8 * i + wave, row = 4 * rg + q;
      dma16(w_uv + (size_t)(half * 256 + row) * 4096 + head * 128 + ((n ^ swz(row)) * 8), L + rg * 1024);
    }
#pragma unroll
    for (int i = 0; i < 4; i++) { const int rg = 8 * i + wave; dma16n(w1 + (size_t)(rg * 16 + (lane >> 2)) * 1376 + wst * 8 + (lane & 3) * 8, W1I + rg * 1024); }
    CBAR();
    LAS u16* scl = (LAS u16*)(SM);
    LAS unsigned* cc16 = (LAS unsigned*)(SM + 1024);
    LAS unsigned* ml = (LAS unsigned*)(SM + 2048);
    LAS float* stat = (LAS float*)(SM + 5120);
    LAS u16* y16 = (LAS u16*)(SM + 5376);
    LAS float* gpart = (LAS float*)(SM + 6400);
    VMWAIT0();
    bar();
    STAMP(1);
    bf16x8 wvf[8], w1f[4];
#pragma unroll
    for (int s2 = 0; s2 < 8; s2++) wvf[s2] = trfrag<256, true>(L, 32 * s2, 2 * wave, lane);
#pragma unroll
    for (int i = 0; i < 4; i++) w1f[i] = trfrag<64, false>(W1I, 64 * wave + 32 * (i >> 1), 2 * (i & 1), lane);
    if (wave < 4) {
      int spins = 0;
      for (;;) { CBAR(); v4u v = ld16(rs, s1off); if (__all((v.y != snap.y) & (v.w != snap.w) & ((v.y & v.w & FLAG) != 0)) || ++spins > SPIN_CAP) break; __builtin_amdgcn_s_sleep(2); }
    }
    bar();
    STAMP(2);
    float inv_l;
    {
      v4u p0, p1; v2u stt;
      const int off = S3_OFF + (head * 32 + 4 * wave + (lane >> 5)) * 1024 + half * 512 + (lane & 31) * 16;
      const int soff = ST_OFF + (head * 32 + (lane & 31)) * 8;
      int spins = 0;
      for (;;) {
        CBAR();
        stt = ld8(rs, soff); p0 = ld16(rs, off); p1 = ld16(rs, off + 2048);
        const unsigned m = max(max(mx4(p0), mx4(p1)), max(stt.x, stt.y));
        if (__all(m != SENT) || ++spins > SPIN_CAP) break;
        POLLSLEEP();
      }
      STAMP(3);
      { const int row = 4 * wave + (lane >> 5); *(LAS v4u*)(STG + row * 512 + (((lane & 31) ^ swz(row)) * 16)) = p0; *(LAS v4u*)(STG + (row + 2) * 512 + (((lane & 31) ^ swz(row + 2)) * 16)) = p1; }
      const float m = __uint_as_float(stt.x), l = __uint_as_float(stt.y);
      const float mg = wave_max(m);
      const __bf16 scb = (__bf16)__builtin_amdgcn_exp2f(m - mg);
      if (lane < 32) scl[wave * 32 + lane] = __builtin_bit_cast(u16, scb);
      inv_l = 2.f * __builtin_amdgcn_rcpf(wave_sum((float)scb * l));
    }
    bar();
    {
      const bf16x8 sf = *(const LAS bf16x8*)(scl + wave * 32 + q * 8);
      const f32x4 a0 = MFMA(sf, (trfrag<512, true>(STG, 0, wave * 4, lane)), ((f32x4){0, 0, 0, 0}));
      const f32x4 a1 = MFMA(sf, (trfrag<512, true>(STG, 0, wave * 4 + 2, lane)), ((f32x4){0, 0, 0, 0}));
      const float cv2 = (q == 0 ? a0[0] : a1[0]) * inv_l;
      const unsigned d = pk2(cv2, dppf<0x101>(cv2));
      if (lane < 32 && !(lane & 1)) cc16[wave * 16 + (lane >> 1)] = d;
    }
    bar();
    {
      f32x4 a0 = {0, 0, 0, 0};
#pragma unroll
      for (int s2 = 0; s2 < 8; s2++) a0 = MFMA(*(const LAS bf16x8*)(cc16 + s2 * 16 + q * 4), wvf[s2], a0);
      const unsigned d = canon(pk2(a0[0], dppf<0x101>(a0[0])));
      v4u o = {d, dppu<0x102>(d), dppu<0x104>(d), dppu<0x106>(d)};
      if (lane < 16 && (lane & 7) == 0) st16(rs, S4_OFF + (head * 2 + half) * 256 + wave * 32 + lane * 2, o);
    }
    STAMP(4);
    if (wave < 3) {
      v4u v; int spins = 0;
      for (;;) { CBAR(); v = ld16(rs, SL_OFF + t * 16); if (__all(mx4(v) != SENT) || ++spins > SPIN_CAP) break; __builtin_amdgcn_s_sleep(1); }
      *(LAS v4u*)(ml + t * 4) = v;
    }
    bar();
    const LAS u16* mlh = (const LAS u16*)ml;
    float gam = bflo(mlh[mcol]), sbm = bflo(mlh[512 + mcol]), cb1 = rbf(1.f + bflo(mlh[1024 + mcol]));
    asm volatile("" : "+v"(gam), "+v"(sbm), "+v"(cb1));
    const int gcol = (st0 - wst) * 8 + lane;
    STAMP(5);
    float xnew;
    {
      v4u v; int spins = 0;
      const int off = S5_OFF + (lane >> 3) * 1024 + wave * 128 + (lane & 7) * 16;
      for (;;) { CBAR(); v = ld16(rs, off); if (__all(mx4(v) != SENT) || ++spins > SPIN_CAP) break; POLLSLEEP(); }
      STAMP(6);
      const float b0 = red32(bflo(v.x), bflo(v.z)), b1x = red32(bfhi(v.x), bfhi(v.z)), b2x = red32(bflo(v.y), bflo(v.w)), b3 = red32(bfhi(v.y), bfhi(v.w));
      const float c0 = red16(b0, b2x), c1 = red16(b1x, b3);
      const bool up = lane & 8;
      const float keep = up ? c1 : c0, send = up ? c0 : c1;
      const float ao = keep + dppf<0x128>(send);
      xnew = rbf(xme + rbf(gam * rbf(ao)));
      const float sv = wave_sum2(xnew, xnew * xnew);
      if ((lane & 47) == 0) stat[wave * 2 + (lane >> 4)] = sv;
    }
    bar();
    float h2;
    {
      float v = stat[n];
      v += dppf<0x128>(v); v += dppf<0x124>(v); v += dppf<0x122>(v);
      const float o = dppf<0xB1>(v);
      const float s1 = (lane & 1) ? o : v, s2 = (lane & 1) ? v : o;
      const float mean = s1 * (1.f / 512.f), var = fmaxf(s2 * (1.f / 512.f) - mean * mean, 0.f);
      const float rstd2 = __builtin_amdgcn_rsqf(var + 1e-5f);
      h2 = rbf(rbf(rbf((xnew - mean) * rstd2) * cb1) + sbm);
      const float w = wave_sum2(h2, h2 * h2);
      if ((lane & 47) == 0) stat[16 + wave * 2 + (lane >> 4)] = w;
    }
    bar();
    {
      float v = stat[16 + n];
      v += dppf<0x128>(v); v += dppf<0x124>(v); v += dppf<0x122>(v);
      const float o = dppf<0xB1>(v);
      const float s1 = (lane & 1) ? o : v, s2 = (lane & 1) ? v : o;
      const float mean = s1 * (1.f / 512.f), var = fmaxf(s2 * (1.f / 512.f) - mean * mean, 0.f);
      const float rstd2 = __builtin_amdgcn_rsqf(var + 1e-5f);
      y16[mcol] = __builtin_bit_cast(u16, (__bf16)((h2 - mean) * rstd2));
      LGWAIT0();
      const bf16x8 y0 = *(const LAS bf16x8*)(y16 + wave * 64 + q * 8), y1 = *(const LAS bf16x8*)(y16 + wave * 64 + 32 + q * 8);
      f32x4 z0 = MFMA(y0, w1f[0], ((f32x4){0, 0, 0, 0})), z1 = MFMA(y0, w1f[1], ((f32x4){0, 0, 0, 0}));
      z0 = MFMA(y1, w1f[2], z0); z1 = MFMA(y1, w1f[3], z1);
      if (lane < 32) gpart[wave * 32 + lane] = q == 0 ? z0[0] : z1[0];
    }
    bar();
    if (wave == 0) {
      float a = 0;
#pragma unroll
      for (int w = 0; w < 8; w++) a += gpart[w * 32 + (gcol & 31)];
      const float z = rbf(rbf(a) + b1v);
      const float u = z * (0.7978845608028654f + 0.035677408136300125f * z * z);
      const float gl = z * __builtin_amdgcn_rcpf(1.f + __builtin_amdgcn_exp2f(-2.885390081777927f * u));
      const unsigned d = canon(pk2(gl, dppf<0x101>(gl)));
      v4u o = {d, dppu<0x102>(d), dppu<0x104>(d), dppu<0x106>(d)};
      if ((lane & 7) == 0 && lane < nst * 8) st16(rs, S6_OFF + (st0 + (lane >> 3)) * 16, o);
    }
    if (ti == 0 && wave == 1 && lane < 8) { v4u o = {1, 1, 1, 1}; st16(rs, DN_OFF + lane * 128, o); }
    STAMP(7);
    STAMP_FLUSH();
  } else {
    const int e = r - 16, ti = xc * 8 + e, split = 4 * xc + (e & 3), hg = e >> 2;
    const int s1off = S1_OFF + (8 + xc) * S1_BYTES + (t & 255) * 16;
    const v4u sv4 = {SENT, SENT, SENT, SENT};
    const int o3 = S3_OFF + ((hg * 16 + n) * 32 + split) * 1024 + (wave * 64 + (q & 1) * 16 + (q & 2) * 4) * 2;
    const int ost = ST_OFF + ((hg * 16 + n) * 32 + split) * 8;
    LAS unsigned char* qa = SM;
    LAS unsigned char* Pm = SM + 16896;
    LAS float* wmax = (LAS float*)(SM + 21504);
    LAS float* wsum = wmax + 128;
    LAS float* apart = (LAS float*)(SM + 22528);
    LAS unsigned* pub = (LAS unsigned*)(SM + 23040);
    volatile LAS unsigned* acnt = (volatile LAS unsigned*)(SM + 23104);
    LAS float* fpart = (LAS float*)(SM + 23296);
    const int piece = (lane >> 2) & 3;
    const int pidx = (ti < 32) ? 2 * (4 * ti + piece) : 2 * (4 * (ti - 32) + piece) + 1;
    const int puboff = S1_OFF + ((lane & 3) + 4 * q) * S1_BYTES + pidx * 16;
    unsigned tagc = ((unsigned)wave << 24) + 1u;
    v4u snap = {0, 0, 0, 0};
    if (wave < 4) {
      if (t == 0) *acnt = 0;
      CBAR();
      if (wave == 3 && lane < 32) st16(rs, S2_OFF + ti * 512 + lane * 16, sv4);
      v4u wm[4]; unsigned short cv[4];
      const u16* p = w_mod + (size_t)(t >> 1) * 3072 + ti * 16 + (t & 1) * 8;
#pragma unroll
      for (int i = 0; i < 4; i++) { const int ri = (i + e) & 3; wm[i] = LDNAR(v4u, p + (size_t)ri * 128 * 3072); cv[i] = cvec[(t >> 1) + 128 * ri]; }
      snap = ld16(rs, s1off);
      bar();
      float sv[4];
#pragma unroll
      for (int i = 0; i < 4; i++) { float c = bflo(cv[i]); sv[i] = c * __builtin_amdgcn_rcpf(1.f + __expf(-c)); }
      const unsigned s01 = pk2(sv[0], sv[1]), s23 = pk2(sv[2], sv[3]);
      interleave(wm[0], wm[1]); interleave(wm[2], wm[3]);
      float a[8];
      a[0] = dot2(wm[2].x, s23, dot2(wm[0].x, s01, 0.f)); a[1] = dot2(wm[2].y, s23, dot2(wm[0].y, s01, 0.f));
      a[2] = dot2(wm[2].z, s23, dot2(wm[0].z, s01, 0.f)); a[3] = dot2(wm[2].w, s23, dot2(wm[0].w, s01, 0.f));
      a[4] = dot2(wm[3].x, s23, dot2(wm[1].x, s01, 0.f)); a[5] = dot2(wm[3].y, s23, dot2(wm[1].y, s01, 0.f));
      a[6] = dot2(wm[3].z, s23, dot2(wm[1].z, s01, 0.f)); a[7] = dot2(wm[3].w, s23, dot2(wm[1].w, s01, 0.f));
      float b0 = red32(a[0], a[4]), b1v = red32(a[1], a[5]), b2v = red32(a[2], a[6]), b3 = red32(a[3], a[7]);
      float d0 = red16(b0, b2v), d1 = red16(b1v, b3);
      d0 += dppf<0x128>(d0); d1 += dppf<0x128>(d1); d0 += dppf<0x124>(d0); d1 += dppf<0x124>(d1); d0 += dppf<0x122>(d0); d1 += dppf<0x122>(d1);
      if ((lane & 14) == 0) { f32x2 o = {d0, d1}; *(LAS f32x2*)(apart + wave * 16 + (lane & 1) * 8 + 4 * (lane >> 5) + 2 * ((lane >> 4) & 1)) = o; }
      VMWAIT0();
      LGWAIT0();
      if (lane == 0) __hip_atomic_fetch_add((LAS unsigned*)acnt, 1u, __ATOMIC_RELAXED, __HIP_MEMORY_SCOPE_WORKGROUP);
      if (wave == 0) {
        int spins = 0; while (*acnt < 4u && ++spins < SPIN_CAP) {}
        STAMP(1);
        float v = apart[q * 16 + n];
        v = red16(v, v); v = red32(v, v);
        const unsigned v01 = pk2(dppf<0x00>(v), dppf<0x55>(v)), v23 = pk2(dppf<0xAA>(v), dppf<0xFF>(v));
        v4u o = {v01, tagc, v23, tagc};
        st16(rs, puboff, o); tagc++;
        if (lane < 16 && (lane & 3) == 0) { v2u p2 = {v01, v23}; *(LAS v2u*)(pub + piece * 2) = p2; }
        STAMP(2);
      }
#pragma unroll
      for (int i = 0; i < 4; i++) stclr(rs, S3_OFF + ((hg * 16 + wave * 4 + i) * 32 + split) * 1024 + lane * 16, sv4);
      if (wave == 3 && lane < 16) st8(rs, ost, ((v2u){SENT, SENT}));
      if (wave == 2 && lane < 32) stclr(rs, S4_OFF + ti * 512 + lane * 16, sv4);
    } else {
      bar();
#pragma unroll
      for (int i = 0; i < 32; i++) {
        const int key = ((i * 4 + wave) + hg * 64) & 127;
        dma16(ckv + (size_t)(split * 128 + key) * 512 + ((lane ^ swz(key)) * 8), L + key * 1024);
      }
      VMWAIT0();
    }
#define REPUB() do { v2u p2_ = *(LAS v2u*)(pub + piece * 2); v4u o_ = {p2_.x, tagc, p2_.y, tagc}; st16(rs, puboff, o_); tagc++; } while (0)
    bar();
    STAMP(3);
    bf16x8 ka[16], va[16];
    {
      const int key = wave * 16 + n, f2 = swz(key);
#pragma unroll
      for (int s2 = 0; s2 < 16; s2++) ka[s2] = *(const LAS bf16x8*)(L + key * 1024 + (((4 * s2 + q) ^ f2) * 16));
#pragma unroll
      for (int i = 0; i < 16; i++) va[i] = trfrag<1024, true>(L, 32 * (i & 3), wave * 8 + 2 * (i >> 2), lane);
    }
    if (wave < 4) {
      int spins = 0;
      for (;;) {
        CBAR(); v4u v = ld16(rs, s1off);
        if (__all((v.y != snap.y) & (v.w != snap.w)) || ++spins > SPIN_CAP) break;
        if ((spins & 15) == 0) REPUB();
      }
    }
    VMWAIT0();
    bar();
    tagc |= FLAG;
    if (wave == 0) REPUB();
    STAMP(4);
    {
      const int off = S2_OFF + hg * 16384 + t * 16;
      v4u v0, v1; int spins = 0;
      for (;;) {
        CBAR();
        v0 = ld16(rs, off); v1 = ld16(rs, off + 8192);
        if (__all(max(mx4(v0), mx4(v1)) != SENT) || ++spins > SPIN_CAP) break;
        if ((spins & 15) == 0) REPUB();
        POLLSLEEP();
      }
      *(LAS v4u*)(qa + wave * 1056 + lane * 16) = v0;
      *(LAS v4u*)(qa + (8 + wave) * 1056 + lane * 16) = v1;
    }
    bar();
    STAMP(5);
    f32x4 sc = {0, 0, 0, 0};
#pragma unroll
    for (int s2 = 0; s2 < 16; s2++) sc = MFMA(ka[s2], *(const LAS bf16x8*)(qa + n * 1056 + s2 * 64 + q * 16), sc);
    float mx = fmaxf(fmaxf(sc[0], sc[1]), fmaxf(sc[2], sc[3]));
    mx = max16(mx); mx = max32(mx);
    if (lane < 16) wmax[lane * 8 + wave] = mx;
    bar();
    {
      f32x4 m0 = *(LAS f32x4*)(wmax + n * 8), m1 = *(LAS f32x4*)(wmax + n * 8 + 4);
      mx = fmaxf(fmaxf(fmaxf(m0[0], m0[1]), fmaxf(m0[2], m0[3])), fmaxf(fmaxf(m1[0], m1[1]), fmaxf(m1[2], m1[3])));
    }
    const float p0 = __builtin_amdgcn_exp2f(sc[0] - mx), p1 = __builtin_amdgcn_exp2f(sc[1] - mx), p2 = __builtin_amdgcn_exp2f(sc[2] - mx), p3 = __builtin_amdgcn_exp2f(sc[3] - mx);
    { v2u o = {pk2(p0, p1), pk2(p2, p3)}; *(LAS v2u*)(Pm + n * 288 + (wave * 16 + q * 4) * 2) = o; }
    float ps = (p0 + p1) + (p2 + p3);
    ps = red16(ps, ps); ps = red32(ps, ps);
    if (lane < 16) wsum[lane * 8 + wave] = ps;
    bar();
    {
      bf16x8 bp[4];
#pragma unroll
      for (int ks = 0; ks < 4; ks++) bp[ks] = *(const LAS bf16x8*)(Pm + n * 288 + ks * 64 + q * 16);
      f32x4 acc[4];
#pragma unroll
      for (int T = 0; T < 4; T++) {
        acc[T] = (f32x4){0, 0, 0, 0};
#pragma unroll
        for (int ks = 0; ks < 4; ks++) acc[T] = MFMA(va[T * 4 + ks], bp[ks], acc[T]);
      }
      unsigned d0[4], d1[4];
#pragma unroll
      for (int T = 0; T < 4; T++) { d0[T] = canon(pk2(acc[T][0], acc[T][1])); d1[T] = canon(pk2(acc[T][2], acc[T][3])); }
      auto r0 = __builtin_amdgcn_permlane16_swap(d0[0], d0[1], false, false);
      auto r1 = __builtin_amdgcn_permlane16_swap(d1[0], d1[1], false, false);
      auto r2 = __builtin_amdgcn_permlane16_swap(d0[2], d0[3], false, false);
      auto r3 = __builtin_amdgcn_permlane16_swap(d1[2], d1[3], false, false);
      v4u oa = {r0[0], r1[0], r0[1], r1[1]}, ob = {r2[0], r3[0], r2[1], r3[1]};
      st16(rs, o3, oa); st16(rs, o3 + 64, ob);
      if (wave == 7) {
        f32x4 s0 = *(LAS f32x4*)(wsum + n * 8), s1 = *(LAS f32x4*)(wsum + n * 8 + 4);
        const float l = ((s0[0] + s0[1]) + (s0[2] + s0[3])) + ((s1[0] + s1[1]) + (s1[2] + s1[3]));
        v2u o2 = {canon(__float_as_uint(mx)), canon(__float_as_uint(l))};
        if (lane < 16) st8(rs, ost, o2);
      }
    }
    STAMP(6);
    if (wave < 4) {
      int spins = 0;
      for (;;) {
        CBAR(); v4u v = ld16(rs, s1off);
        if (__all((v.y & v.w & FLAG) != 0) || ++spins > SPIN_CAP) break;
        if ((spins & 3) == 0) REPUB();
        __builtin_amdgcn_s_sleep(4);
      }
    }
    bar();
    if (wave == 0) {
      int spins = 0;
      for (;;) {
        CBAR(); v4u v = ld16(rs, DN_OFF + xc * 128);
        if (v.x != SENT || ++spins > SPIN_CAP) break;
        if ((spins & 3) == 0) REPUB();
        __builtin_amdgcn_s_sleep(8);
      }
    }
    STAMP(7);
    STAMP_FLUSH();
  }
}

extern "C" void kernel_launch(void* const* d_in, const int* in_sizes, int n_in, void* d_out, int out_size, void* d_ws, size_t ws_size, hipStream_t stream) {
  (void)in_sizes; (void)n_in; (void)out_size; (void)ws_size;
  (void)hipFuncSetAttribute((const void*)fused_kernel, hipFuncAttributeMaxDynamicSharedMemorySize, L_TOTAL);
  fused_kernel<<<256, NT, L_TOTAL, stream>>>((const u16*)d_in[0], (const u16*)d_in[1], (const u16*)d_in[2], (const u16*)d_in[3], (const u16*)d_in[4], (const u16*)d_in[5],
                                             (const u16*)d_in[7], (const u16*)d_in[8], (const u16*)d_in[9], (const u16*)d_in[10], (const u16*)d_in[11], (const u16*)d_in[12],
                                             (u16*)d_out, (unsigned char*)d_ws);
}
```

```cpp
#include <hip/hip_runtime.h>
#include <stdint.h>

typedef unsigned short u16;
typedef unsigned v4u __attribute__((ext_vector_type(4)));
typedef unsigned v2u __attribute__((ext_vector_type(2)));
typedef float f32x4 __attribute__((ext_vector_type(4)));
typedef float f32x2 __attribute__((ext_vector_type(2)));
typedef short bf16x8 __attribute__((ext_vector_type(8)));
typedef short s16x4 __attribute__((ext_vector_type(4)));
typedef __bf16 bf2 __attribute__((ext_vector_type(2)));
#define LAS __attribute__((address_space(3)))

constexpr unsigned SENT = 0xFFFFFFFFu;
constexpr int NT = 512;
constexpr int SPIN_CAP = 1 << 20;
#define PSL 0
#define POLLSLEEP() do { if (PSL) __builtin_amdgcn_s_sleep(PSL); } while (0)
#define NREP 2
#define EGL_DELAY 400
#define CLRAUX 16
#define NTAUX 2
#define NTS " nt"
#define LDNT(T, p) __builtin_nontemporal_load((const T*)(p))
constexpr int SU_OFF = 0;
constexpr int SF_OFF = 131072;
constexpr int SQ_OFF = 262144;
constexpr int CLR_OFF = 327680;
constexpr int S2_OFF = CLR_OFF;
constexpr int S5_OFF = S2_OFF + 8 * 32768;
constexpr int S6_OFF = S5_OFF + 65536;
constexpr int SL_OFF = S6_OFF + 49152;
constexpr int DN_OFF = SL_OFF + 32768;
constexpr int EGC_END = DN_OFF + 4096;
constexpr int EGC_PIECES = (EGC_END - S2_OFF) / 16 / 64;
constexpr int S4_OFF = EGC_END;
constexpr int S3R = 1152;
constexpr int S3_OFF = S4_OFF + 16384;
constexpr int CLR_END = S3_OFF + 1024 * S3R;
constexpr unsigned FLAG = 0x80000000u;
constexpr int DUMMY_OFF = CLR_END;
constexpr int WS_USED = CLR_END + 65536;
#define STAMP(i) do {} while (0)
#define STAMP_DECL do {} while (0)
#define STAMP_FLUSH() do {} while (0)

constexpr int L_CKV = 0;
constexpr int L_SM = 131072;
constexpr int L_TOTAL = 131072 + 30720;

__device__ __forceinline__ float bflo(unsigned w) { return __uint_as_float(w << 16); }
__device__ __forceinline__ float bfhi(unsigned w) { return __uint_as_float(w & 0xffff0000u); }
__device__ __forceinline__ unsigned pk2(float a, float b) { bf2 p; p.x = (__bf16)a; p.y = (__bf16)b; return __builtin_bit_cast(unsigned, p); }
__device__ __forceinline__ float rbf(float a) { return (float)(__bf16)a; }
__device__ __forceinline__ unsigned canon(unsigned w) { return w == SENT ? 0x7FC07FC0u : w; }
__device__ __forceinline__ float dot2(unsigned a, unsigned b, float c) { return __builtin_amdgcn_fdot2_f32_bf16(__builtin_bit_cast(bf2, a), __builtin_bit_cast(bf2, b), c, false); }
__device__ __forceinline__ void bar() { __builtin_amdgcn_sched_barrier(0); asm volatile("s_waitcnt lgkmcnt(0)\n\ts_barrier" ::: "memory"); __builtin_amdgcn_sched_barrier(0); }
template <int CTRL> __device__ __forceinline__ float dppf(float v) { return __int_as_float(__builtin_amdgcn_update_dpp(0, __float_as_int(v), CTRL, 0xf, 0xf, false)); }
template <int CTRL> __device__ __forceinline__ unsigned dppu(unsigned v) { return (unsigned)__builtin_amdgcn_update_dpp(0, (int)v, CTRL, 0xf, 0xf, false); }
__device__ __forceinline__ float red32(float a, float b) {
  auto r = __builtin_amdgcn_permlane32_swap(__float_as_uint(a), __float_as_uint(b), false, false);
  return __uint_as_float(r[0]) + __uint_as_float(r[1]);
}
__device__ __forceinline__ float red16(float a, float b) {
  auto r = __builtin_amdgcn_permlane16_swap(__float_as_uint(a), __float_as_uint(b), false, false);
  return __uint_as_float(r[0]) + __uint_as_float(r[1]);
}
__device__ __forceinline__ float max32(float a) { auto r = __builtin_amdgcn_permlane32_swap(__float_as_uint(a), __float_as_uint(a), false, false); return fmaxf(__uint_as_float(r[0]), __uint_as_float(r[1])); }
__device__ __forceinline__ float max16(float a) { auto r = __builtin_amdgcn_permlane16_swap(__float_as_uint(a), __float_as_uint(a), false, false); return fmaxf(__uint_as_float(r[0]), __uint_as_float(r[1])); }
__device__ __forceinline__ float wave_sum(float v) {
  v += dppf<0x128>(v); v += dppf<0x124>(v); v += dppf<0x122>(v); v += dppf<0x121>(v);
  v = red16(v, v); v = red32(v, v); return v;
}
__device__ __forceinline__ float wave_max(float v) {
  v = fmaxf(v, dppf<0x128>(v)); v = fmaxf(v, dppf<0x124>(v)); v = fmaxf(v, dppf<0x122>(v)); v = fmaxf(v, dppf<0x121>(v));
  v = max16(v); v = max32(v); return v;
}
__device__ __forceinline__ unsigned ilo(unsigned a, unsigned b) { return __builtin_amdgcn_perm(b, a, 0x05040100u); }
__device__ __forceinline__ unsigned ihi(unsigned a, unsigned b) { return __builtin_amdgcn_perm(b, a, 0x07060302u); }
__device__ __forceinline__ void interleave(v4u& a, v4u& b) {
  v4u x, y;
  x.x = ilo(a.x, b.x); x.y = ihi(a.x, b.x); x.z = ilo(a.y, b.y); x.w = ihi(a.y, b.y);
  y.x = ilo(a.z, b.z); y.y = ihi(a.z, b.z); y.z = ilo(a.w, b.w); y.w = ihi(a.w, b.w);
  a = x; b = y;
}
__device__ __forceinline__ bool valid4(v4u v) { return (v.x != SENT) & (v.y != SENT) & (v.z != SENT) & (v.w != SENT); }
typedef __amdgpu_buffer_rsrc_t rsrc_t;
__device__ __forceinline__ v4u ld16(rsrc_t r, int off) { return __builtin_amdgcn_raw_buffer_load_b128(r, off, 0, 16); }
__device__ __forceinline__ v2u ld8(rsrc_t r, int off) { return __builtin_amdgcn_raw_buffer_load_b64(r, off, 0, 16); }
__device__ __forceinline__ void st16(rsrc_t r, int off, v4u v) { __builtin_amdgcn_raw_buffer_store_b128(v, r, off, 0, 16); }
__device__ __forceinline__ void st8(rsrc_t r, int off, v2u v) { __builtin_amdgcn_raw_buffer_store_b64(v, r, off, 0, 16); }
__device__ __forceinline__ void glds16(const void* g, unsigned lds_base) {
  unsigned sv; asm volatile("s_mov_b32 %0, m0\n\ts_mov_b32 m0, %2\n\ts_nop 0\n\tglobal_load_lds_dwordx4 %1, off" NTS "\n\ts_mov_b32 m0, %0" : "=&s"(sv) : "v"(g), "s"(lds_base) : "memory");
}
#define CBAR() do { asm volatile("" ::: "memory"); __builtin_amdgcn_sched_barrier(0); } while (0)

__device__ __forceinline__ int swz(int row) { return ((row & 3) | ((row >> 1) & 4)) << 1; }
template <int AUX> __device__ __forceinline__ void dma16a(const void* g, LAS unsigned char* l) { __builtin_amdgcn_global_load_lds((const __attribute__((address_space(1))) unsigned*)g, (LAS unsigned*)l, 16, 0, AUX); }
#define dma16 dma16a<NTAUX>
#define NARROW 2
#define dma16n dma16a<NARROW>
#define LDNAR(T, p) __builtin_nontemporal_load((const T*)(p))
template <int RB, bool SW>
__device__ __forceinline__ bf16x8 trfrag(const LAS unsigned char* img, int k0, int ch0, int lane) {
  const int g = lane >> 4, qq = (lane & 15) >> 2, pp = lane & 3;
  s16x4 h[2];
#pragma unroll
  for (int tt = 0; tt < 2; tt++) {
    const int row = k0 + 8 * g + 4 * tt + qq;
    const int ch = SW ? ((ch0 + (pp >> 1)) ^ swz(row)) : (ch0 + (pp >> 1));
    h[tt] = __builtin_amdgcn_ds_read_tr16_b64_v4i16((LAS s16x4*)(img + row * RB + ch * 16 + (pp & 1) * 8));
  }
  return (bf16x8){h[0].x, h[0].y, h[0].z, h[0].w, h[1].x, h[1].y, h[1].z, h[1].w};
}
#define MFMA(a, b, c) __builtin_amdgcn_mfma_f32_16x16x32_bf16(a, b, c, 0, 0, 0)
#define VMWAIT0() do { asm volatile("" ::: "memory"); __builtin_amdgcn_s_waitcnt(0x0F70); asm volatile("" ::: "memory"); } while (0)
#define LGWAIT0() asm volatile("s_waitcnt lgkmcnt(0)" ::: "memory")
__device__ __forceinline__ float wave_sum2(float a, float b) {
  a += dppf<0x128>(a); b += dppf<0x128>(b); a += dppf<0x124>(a); b += dppf<0x124>(b);
  a += dppf<0x122>(a); b += dppf<0x122>(b); a += dppf<0x121>(a); b += dppf<0x121>(b);
  float v = red16(a, b); return red32(v, v);
}

typedef unsigned long long u64;
__device__ __forceinline__ void ldm4(rsrc_t r, int off, unsigned s1, unsigned s2, unsigned s3, v4u& a, v4u& b, v4u& c, v4u& d, u64 m0, u64 m1, u64 m2, u64 m3) {
  u64 sv;
  asm volatile("s_mov_b64 %0, exec\n\t"
               "s_mov_b64 exec, %10\n\tbuffer_load_dwordx4 %1, %5, %6, 0 offen sc1\n\t"
               "s_mov_b64 exec, %11\n\tbuffer_load_dwordx4 %2, %5, %6, %7 offen sc1\n\t"
               "s_mov_b64 exec, %12\n\tbuffer_load_dwordx4 %3, %5, %6, %8 offen sc1\n\t"
               "s_mov_b64 exec, %13\n\tbuffer_load_dwordx4 %4, %5, %6, %9 offen sc1\n\t"
               "s_mov_b64 exec, %0\n\ts_waitcnt vmcnt(0)"
               : "=&s"(sv), "+v"(a), "+v"(b), "+v"(c), "+v"(d) : "v"(off), "s"(r), "s"(s1), "s"(s2), "s"(s3), "s"(m0), "s"(m1), "s"(m2), "s"(m3) : "memory");
}
__device__ __forceinline__ void ldm2(rsrc_t r, int off, unsigned s1, v4u& a, v4u& b, u64 m0, u64 m1) {
  u64 sv;
  asm volatile("s_mov_b64 %0, exec\n\t"
               "s_mov_b64 exec, %6\n\tbuffer_load_dwordx4 %1, %3, %4, 0 offen sc1\n\t"
               "s_mov_b64 exec, %7\n\tbuffer_load_dwordx4 %2, %3, %4, %5 offen sc1\n\t"
               "s_mov_b64 exec, %0\n\ts_waitcnt vmcnt(0)"
               : "=&s"(sv), "+v"(a), "+v"(b) : "v"(off), "s"(r), "s"(s1), "s"(m0), "s"(m1) : "memory");
}
__device__ __forceinline__ void ldm6(rsrc_t r, int off, v4u (&h)[6], u64 m0, u64 m1, u64 m2, u64 m3, u64 m4, u64 m5) {
  u64 sv;
  asm volatile("s_mov_b64 %0, exec\n\t"
               "s_mov_b64 exec, %9\n\tbuffer_load_dwordx4 %1, %7, %8, 0 offen sc1\n\t"
               "s_mov_b64 exec, %10\n\tbuffer_load_dwordx4 %2, %7, %8, 0 offen offset:512 sc1\n\t"
               "s_mov_b64 exec, %11\n\tbuffer_load_dwordx4 %3, %7, %8, 0 offen offset:1024 sc1\n\t"
               "s_mov_b64 exec, %12\n\tbuffer_load_dwordx4 %4, %7, %8, 0 offen offset:1536 sc1\n\t"
               "s_mov_b64 exec, %13\n\tbuffer_load_dwordx4 %5, %7, %8, 0 offen offset:2048 sc1\n\t"
               "s_mov_b64 exec, %14\n\tbuffer_load_dwordx4 %6, %7, %8, 0 offen offset:2560 sc1\n\t"
               "s_mov_b64 exec, %0\n\ts_waitcnt vmcnt(0)"
               : "=&s"(sv), "+v"(h[0]), "+v"(h[1]), "+v"(h[2]), "+v"(h[3]), "+v"(h[4]), "+v"(h[5]) : "v"(off), "s"(r), "s"(m0), "s"(m1), "s"(m2), "s"(m3), "s"(m4), "s"(m5) : "memory");
}
__device__ __forceinline__ u64 inval(v4u v) { return __builtin_amdgcn_ballot_w64(!valid4(v)); }

__device__ __forceinline__ unsigned mx4(v4u v) { return max(max(v.x, v.y), max(v.z, v.w)); }
__device__ __forceinline__ void stclr(rsrc_t r, int off, v4u v) { __builtin_amdgcn_raw_buffer_store_b128(v, r, off, 0, CLRAUX); }

__global__ void __launch_bounds__(NT, 2) fused_kernel(
    const u16* __restrict__ x, const u16* __restrict__ ckv, const u16* __restrict__ w_q, const u16* __restrict__ w_uk,
    const u16* __restrict__ w_uv, const u16* __restrict__ w_o, const u16* __restrict__ w1, const u16* __restrict__ b1,
    const u16* __restrict__ w2, const u16* __restrict__ b2, const u16* __restrict__ cvec, const u16* __restrict__ w_mod,
    u16* __restrict__ out, unsigned char* ws) {
  extern __shared__ __attribute__((aligned(16))) unsigned char lds_raw[];
  LAS unsigned char* L = (LAS unsigned char*)lds_raw;
  const int t = threadIdx.x, lane = t & 63, wave = __builtin_amdgcn_readfirstlane(t >> 6);
  const int b = blockIdx.x, xc = b & 7, r = b >> 3;
  const int n = lane & 15, q = lane >> 4;
  const rsrc_t rs = __builtin_amdgcn_make_buffer_rsrc((void*)ws, 0, WS_USED, 0x00020000);
  LAS unsigned char* SM = L + L_SM;
  const v4u sv4 = {SENT, SENT, SENT, SENT};
  STAMP_DECL;
  const u64 tst = __builtin_amdgcn_s_memrealtime();
  (void)tst;
  STAMP(0);
#define DELAY(x) do { while (__builtin_amdgcn_s_memrealtime() - tst < (x)) __builtin_amdgcn_s_sleep(2); } while (0)

  if (r < 16) {
    const int head = 4 * xc + (r >> 2), cs = r & 3;
    LAS unsigned char* hp = SM;
    LAS unsigned* q16 = (LAS unsigned*)(SM + 2304);
    LAS unsigned char* qst = SM + 8192;
    LAS unsigned char* WKI = L + 98304;
    LAS unsigned char* WQI = L + 65536;
    const int sqoff = SQ_OFF + head * 2048 + (t & 127) * 16;
    const int sfoff = SF_OFF + xc * 8192 + lane * 128;
    v4u snapq = {0, 0, 0, 0}; unsigned snapf = 0;
    const unsigned short cvl0 = cvec[(r & 7) * 64 + (t >> 4)], cvl1 = cvec[(r & 7) * 64 + 32 + (t >> 4)];
    if (wave < 4) {
      const int suoff = SU_OFF + xc * 16384 + cs * 4096 + t * 16;
      const v4u snapu = ld16(rs, suoff);
      snapq = ld16(rs, sqoff);
      snapf = ld8(rs, sfoff).y;
      bar();
      {
        const int cb = S3_OFF + (head * 32 + cs * 8) * S3R + t * 16;
        stclr(rs, cb, sv4); stclr(rs, cb + 4096, sv4);
        if (wave == 0) stclr(rs, cb + 8192, sv4);
        else if (wave == 1 && lane < 8) stclr(rs, S4_OFF + head * 512 + cs * 128 + lane * 16, sv4);
      }
      v4u v = snapu; int spins = 0; bool ok = false;
      for (;;) {
        CBAR();
        if (!ok) v = ld16(rs, suoff);
        ok = (v.y != snapu.y) & (v.w != snapu.w);
        if (__all(ok) || ++spins > SPIN_CAP) break;
      }
      v2u hh = {v.x, v.z};
      *(LAS v2u*)(hp + ((t >> 4) & 7) * 272 + (t >> 7) * 128 + (t & 15) * 8) = hh;
      STAMP(1);
    } else {
      bar();
#pragma unroll
      for (int i = 0; i < 16; i++) {
        const int j = 4 * i + (wave - 4), rg = j & 31, row = 4 * rg + q;
        const u16* src = (i < 8 ? w_q : w_uk) + (size_t)(cs * 128 + row) * 4096 + head * 128 + ((n ^ swz(row)) * 8);
        dma16(src, L + 65536 + j * 1024);
      }
#pragma unroll
      for (int i = 0; i < 4; i++) { const int rg = 4 * i + (wave - 4); dma16(w_mod + (size_t)((r & 7) * 64 + 4 * rg + q) * 3072 + 1024 + (xc * 2 + (r >> 3)) * 128 + n * 8, L + 49152 + rg * 1024); }
      VMWAIT0();
    }
    bar();
    STAMP(2);
    bf16x8 wk[4];
    {
      f32x4 a0 = {0, 0, 0, 0};
#pragma unroll
      for (int i = 0; i < 4; i++) a0 = MFMA(*(const LAS bf16x8*)(hp + (n & 7) * 272 + i * 64 + q * 16), (trfrag<256, true>(WQI, 32 * i, 2 * wave, lane)), a0);
      float sq = (a0[0] + a0[1]) + (a0[2] + a0[3]);
      sq = red16(sq, sq);
      const unsigned d = pk2(sq, dppf<0x101>(sq));
      if (lane < 16 && !(lane & 1)) q16[wave * 8 + (lane >> 1)] = d;
    }
    bar();
#pragma unroll
    for (int i = 0; i < 4; i++) wk[i] = *(const LAS bf16x8*)(WKI + (wave * 16 + n) * 256 + (((4 * i + q) ^ swz(n)) * 16));
    if (wave < 2) {
      const bool mine = (t >> 5) == cs;
      const v2u dq = *(const LAS v2u*)(q16 + (t & 31) * 2);
      if (mine) { v4u o = {dq.x, snapq.y + 1u, dq.y, snapq.w + 1u}; st16(rs, sqoff, o); }
      STAMP(12);
      v4u v = {dq.x, 0, dq.y, 0}; int spins = 0; bool ok = mine;
      for (;;) {
        CBAR();
        if (!ok) v = ld16(rs, sqoff);
        ok = mine | ((v.y != snapq.y) & (v.w != snapq.w));
        if (__all(ok) || ++spins > SPIN_CAP) break;
      }
      v2u o2 = {v.x, v.z};
      *(LAS v2u*)(qst + (t >> 5) * 272 + (t & 31) * 8) = o2;
    } else if (wave == 2) {
      int spins = 0; bool ok = false;
      for (;;) { CBAR(); if (!ok) ok = ld8(rs, sfoff).y != snapf; if (__all(ok) || ++spins > SPIN_CAP) break; }
    }
    bar();
    STAMP(13);
    if (wave < 2 && (t >> 5) == cs) { const v2u dq = *(const LAS v2u*)(q16 + (t & 31) * 2); v4u o = {dq.x, snapq.y + 2u, dq.y, snapq.w + 2u}; st16(rs, sqoff, o); }
    {
      f32x4 a = {0, 0, 0, 0};
#pragma unroll
      for (int i = 0; i < 4; i++) a = MFMA(wk[i], *(const LAS bf16x8*)(qst + (n & 3) * 272 + i * 64 + q * 16), a);
#pragma unroll
      for (int i = 0; i < 4; i++) { a[i] += dppf<0xB1>(a[i]); a[i] += dppf<0x4E>(a[i]); }
      const float SC = 0.08838834764831845f * 1.4426950408889634f;
      v2u o = {canon(pk2(a[0] * SC, a[1] * SC)), canon(pk2(a[2] * SC, a[3] * SC))};
      if (n == 0) *(LAS v2u*)(q16 + 64 + wave * 8 + q * 2) = o;
    }
    bar();
    if (wave < 2) { const v4u o = *(const LAS v4u*)(q16 + 64 + n * 4); st16(rs, S2_OFF + (wave * 4 + q) * 32768 + head * 1024 + cs * 256 + n * 16, o); }
    STAMP(3);
    bar();
    {
      const int kj = r & 7, cg = xc * 2 + (r >> 3);
      v4u w0 = *(const LAS v4u*)(L + 49152 + (t >> 4) * 256 + n * 16), w1r = *(const LAS v4u*)(L + 49152 + (32 + (t >> 4)) * 256 + n * 16);
      const float c0 = bflo(cvl0), c1 = bflo(cvl1);
      const unsigned s01 = pk2(c0 * __builtin_amdgcn_rcpf(1.f + __expf(-c0)), c1 * __builtin_amdgcn_rcpf(1.f + __expf(-c1)));
      interleave(w0, w1r);
      float a[8] = {dot2(w0.x, s01, 0.f), dot2(w0.y, s01, 0.f), dot2(w0.z, s01, 0.f), dot2(w0.w, s01, 0.f),
                    dot2(w1r.x, s01, 0.f), dot2(w1r.y, s01, 0.f), dot2(w1r.z, s01, 0.f), dot2(w1r.w, s01, 0.f)};
      float b0 = red32(a[0], a[4]), b1x = red32(a[1], a[5]), b2x = red32(a[2], a[6]), b3 = red32(a[3], a[7]);
      f32x2 o = {red16(b0, b2x), red16(b1x, b3)};
      LAS float* apart = (LAS float*)(SM + 12288);
      *(LAS f32x2*)(apart + wave * 128 + n * 8 + 4 * (lane >> 5) + 2 * ((lane >> 4) & 1)) = o;
      bar();
      if (wave == 7) {
        f32x2 sacc = {0.f, 0.f};
#pragma unroll
        for (int w = 0; w < 8; w++) sacc += *(LAS f32x2*)(apart + w * 128 + lane * 2);
        const unsigned d = canon(pk2(sacc.x, sacc.y));
        v4u o4 = {dppu<0x00>(d), dppu<0x55>(d), dppu<0xAA>(d), dppu<0xFF>(d)};
        if ((lane & 3) == 0) st16(rs, SL_OFF + kj * 4096 + cg * 256 + lane * 4, o4);
      }
      bar();
    }
    if (r < 8) {
#pragma unroll
      for (int i = 0; i < 8; i++) {
        const int rg = 8 * i + wave, row = 8 * rg + (lane >> 3);
        dma16(w_o + (size_t)(xc * 512 + row) * 512 + r * 64 + (((lane & 7) ^ ((row & 2) | ((row >> 1) & 4))) * 8), L + rg * 1024);
      }
      CBAR();
      LAS float* fpart = (LAS float*)(SM + 3072);
      LAS unsigned char* cst = SM + 4096;
      VMWAIT0();
      bar();
      bf16x8 wof[8];
      {
        const int g = lane >> 4, qq = (lane & 15) >> 2, pp = lane & 3;
#pragma unroll
        for (int i = 0; i < 8; i++) {
          s16x4 h[2];
#pragma unroll
          for (int tt = 0; tt < 2; tt++) {
            const int row = 32 * ((wave >> 2) * 8 + i) + 8 * g + 4 * tt + qq;
            const int ch = (2 * (wave & 3) + (pp >> 1)) ^ ((row & 2) | ((row >> 1) & 4));
            h[tt] = __builtin_amdgcn_ds_read_tr16_b64_v4i16((LAS s16x4*)(L + row * 128 + ch * 16 + (pp & 1) * 8));
          }
          wof[i] = (bf16x8){h[0].x, h[0].y, h[0].z, h[0].w, h[1].x, h[1].y, h[1].z, h[1].w};
        }
      }
      if (wave >= 4) {
        int spins = 0;
        for (;;) { CBAR(); v4u v = ld16(rs, S2_OFF + xc * 32768 + xc * 4096 + (t - 256) * 16); if (__all(mx4(v) != SENT) || ++spins > SPIN_CAP) break; __builtin_amdgcn_s_sleep(2); }
      }
      bar();
      STAMP(4);
      if (wave < 2) {
        v4u v; int spins = 0;
        for (;;) { CBAR(); v = ld16(rs, S4_OFF + xc * 2048 + t * 16); if (__all(mx4(v) != SENT) || ++spins > SPIN_CAP) break; }
        *(LAS v4u*)(cst + t * 16) = v;
      }
      bar();
      STAMP(10);
      {
        const LAS unsigned char* ap = cst + ((2 * (wave >> 2)) * 2 + (n & 1)) * 256 + q * 16;
        f32x4 a = {0, 0, 0, 0}, a2 = {0, 0, 0, 0};
#pragma unroll
        for (int i = 0; i < 4; i++) {
          a = MFMA(*(const LAS bf16x8*)(ap + ((2 * i) >> 2) * 512 + ((2 * i) & 3) * 64), wof[2 * i], a);
          a2 = MFMA(*(const LAS bf16x8*)(ap + ((2 * i + 1) >> 2) * 512 + ((2 * i + 1) & 3) * 64), wof[2 * i + 1], a2);
        }
        const float v = (a[0] + a[1]) + (a2[0] + a2[1]);
        if (lane < 16) fpart[wave * 16 + lane] = v;
        bar();
        if (wave == 0) {
          const f32x4 l0 = *(const LAS f32x4*)(fpart + (lane & 7) * 8), l1 = *(const LAS f32x4*)(fpart + (lane & 7) * 8 + 4);
          const f32x4 h0 = *(const LAS f32x4*)(fpart + 64 + (lane & 7) * 8), h1 = *(const LAS f32x4*)(fpart + 64 + (lane & 7) * 8 + 4);
          v4u o = {canon(pk2(l0[0] + h0[0], l0[1] + h0[1])), canon(pk2(l0[2] + h0[2], l0[3] + h0[3])), canon(pk2(l1[0] + h1[0], l1[1] + h1[1])), canon(pk2(l1[2] + h1[2], l1[3] + h1[3]))};
          st16(rs, S5_OFF + (lane >> 3) * 8192 + xc * 1024 + r * 128 + (lane & 7) * 16, o);
        }
      }
      STAMP(11);
    } else {
      const int ti = xc * 8 + (r - 8);
      LAS unsigned char* W2I = L;
      const float b2v = bflo(b2[ti * 8 + (lane & 7)]);
      const float x8 = bflo(x[ti * 8 + (lane & 7)]);
      CBAR();
#pragma unroll
      for (int i = 0; i < 3; i++) { const int rg = 8 * i + wave; if (rg < 22) { int row = rg * 64 + lane; row = row < 1376 ? row : 1375; dma16n(w2 + (size_t)row * 512 + ti * 8, W2I + rg * 1024); } }
      CBAR();
      LAS float* hpart = (LAS float*)(SM + 2048);
      LAS unsigned* sbuf = (LAS unsigned*)(SM + 2560);
      VMWAIT0();
      bar();
      STAMP(5);
      bf16x8 w2f[6];
#pragma unroll
      for (int i = 0; i < 6; i++) {
        const int ks = wave + 8 * i < 43 ? wave + 8 * i : 0;
        const int g = lane >> 4, qq = (lane & 15) >> 2, pp = lane & 3;
        s16x4 h0 = __builtin_amdgcn_ds_read_tr16_b64_v4i16((LAS s16x4*)(W2I + (32 * ks + 8 * g + qq) * 16 + (pp & 1) * 8));
        s16x4 h1 = __builtin_amdgcn_ds_read_tr16_b64_v4i16((LAS s16x4*)(W2I + (32 * ks + 8 * g + 4 + qq) * 16 + (pp & 1) * 8));
        w2f[i] = (bf16x8){h0.x, h0.y, h0.z, h0.w, h1.x, h1.y, h1.z, h1.w};
      }
      float xn8 = 0.f, gbv = 0.f;
      if (wave == 0) {
        const int off = lane < 8 ? S5_OFF + xc * 8192 + lane * 1024 + ti * 16 : SL_OFF + (lane & 7) * 4096 + ((lane & 16) ? 3072 : 0) + ti * 16;
        v4u v; int spins = 0;
        for (;;) { CBAR(); v = ld16(rs, off); if (__all(mx4(v) != SENT) || ++spins > SPIN_CAP) break; __builtin_amdgcn_s_sleep(4); }
        if (lane < 24) *(LAS v4u*)(sbuf + lane * 4) = v;
        LGWAIT0();
        const LAS u16* sh = (const LAS u16*)sbuf;
        float ao = 0, ga8 = 0;
#pragma unroll
        for (int k = 0; k < 8; k++) { ao += bflo(sh[k * 8 + (lane & 7)]); ga8 += bflo(sh[64 + k * 8 + (lane & 7)]); gbv += bflo(sh[128 + k * 8 + (lane & 7)]); }
        ga8 = rbf(ga8); gbv = rbf(gbv);
        xn8 = rbf(x8 + rbf(ga8 * rbf(ao)));
      }
      bar();
      STAMP(6);
      {
        v4u hv[6]; int spins = 0;
        const int off = S6_OFF + xc * 6144 + wave * 128 + q * 16, off5 = wave < 3 ? off + 5120 : off;
        for (;;) {
          CBAR();
#pragma unroll
          for (int i = 0; i < 5; i++) hv[i] = ld16(rs, off + i * 1024);
          hv[5] = ld16(rs, off5);
          const unsigned m = max(max(max(mx4(hv[0]), mx4(hv[1])), max(mx4(hv[2]), mx4(hv[3]))), max(mx4(hv[4]), mx4(hv[5])));
          if (__all(m != SENT) || ++spins > SPIN_CAP) break;
          POLLSLEEP();
        }
        if (wave >= 3) hv[5] = (v4u){0, 0, 0, 0};
        STAMP(8);
        f32x4 a = {0, 0, 0, 0}, a2 = {0, 0, 0, 0};
#pragma unroll
        for (int i = 0; i < 3; i++) { a = MFMA(__builtin_bit_cast(bf16x8, hv[2 * i]), w2f[2 * i], a); a2 = MFMA(__builtin_bit_cast(bf16x8, hv[2 * i + 1]), w2f[2 * i + 1], a2); }
        if (lane < 8) hpart[wave * 8 + lane] = a[0] + a2[0];
      }
      bar();
      if (wave == 0) {
        float a = hpart[lane];
        a += dppf<0x128>(a); a = red16(a, a); a = red32(a, a);
        const float o = rbf(rbf(a) + b2v);
        const float res = xn8 + rbf(gbv * o);
        if (lane < 8) out[ti * 8 + lane] = __builtin_bit_cast(u16, (__bf16)res);
      }
      STAMP(9);
    }
    STAMP_FLUSH();
  } else if (r >= 24) {
    const int e = r - 24, head = 4 * xc + (e >> 1), half = e & 1, ti = xc * 8 + e;
    const int gi = e * 8 + xc;
    const int wst = (gi < 43 ? gi : 0) * 4;
    const int mcol = wave * 64 + 8 * (lane & 7) + 4 * (lane >> 5) + 2 * ((lane >> 4) & 1) + ((lane >> 3) & 1);
    LAS unsigned char* W1I = L + 65536;
    LAS unsigned char* STG = L + 98304;
    LAS u16* scl = (LAS u16*)(SM);
    LAS unsigned* cc16 = (LAS unsigned*)(SM + 1024);
    LAS unsigned* ml = (LAS unsigned*)(SM + 2048);
    LAS float* stat = (LAS float*)(SM + 5120);
    LAS u16* y16 = (LAS u16*)(SM + 5376);
    LAS float* gpart = (LAS float*)(SM + 6400);
    LAS unsigned* pub = (LAS unsigned*)(SM + 7424);
    LAS unsigned* cnt = (LAS unsigned*)(SM + 7680);
    LAS unsigned* tbase = (LAS unsigned*)(SM + 8192);
    LAS float* apart = (LAS float*)(SM + 10240);
    LAS float* fpart = (LAS float*)(SM + 14336);
    LAS unsigned char* cst = SM + 15360;
    const int puoff = SU_OFF + q * 16384 + (xc * 8 + e) * 256 + n * 16;
    const int pfoff = SF_OFF + n * 8192 + ti * 128;
    const int sfoff = SF_OFF + (8 + xc) * 8192 + lane * 128;
#define LDSADD(p) __hip_atomic_fetch_add((p), 1u, __ATOMIC_RELAXED, __HIP_MEMORY_SCOPE_WORKGROUP)
#define REPUB() do { v2u p2_ = *(LAS v2u*)(pub + n * 2); v4u tb_ = *(LAS v4u*)(tbase + lane * 8); unsigned tf_ = tbase[lane * 8 + 4]; unsigned k_ = 0; if (lane == 0) k_ = LDSADD(cnt); k_ = __builtin_amdgcn_readfirstlane(k_); \
      v4u o_ = {p2_.x, tb_.x + k_, p2_.y, tb_.y + k_}; st16(rs, puoff, o_); v4u o2_ = {p2_.x, tb_.z + k_, p2_.y, tb_.w + k_}; st16(rs, puoff + 65536, o2_); \
      if (lane < 16) { v2u f_ = {0u, tf_ + k_}; st8(rs, pfoff, f_); } } while (0)
    v2u pd = {0, 0};
    v4u old0 = {0, 0, 0, 0}, old1 = {0, 0, 0, 0}, xv = {0, 0, 0, 0}; unsigned snapf = 0, xw = 0;
    const unsigned oldf = ld8(rs, pfoff).y;
    if (wave == 0) { old0 = ld16(rs, puoff); old1 = ld16(rs, puoff + 65536); xv = *(const v4u*)(x + lane * 8); xw = ((const unsigned*)x)[xc * 32 + (lane & 31)]; }
    if (wave == 1) snapf = ld8(rs, sfoff).y;
    CBAR();
    v4u wm[2]; unsigned short cv[2];
    {
      const u16* p = w_mod + (size_t)(e * 64 + (t >> 4)) * 3072 + (n >> 3) * 512 + xc * 64 + (n & 7) * 8;
      wm[0] = LDNT(v4u, p); wm[1] = LDNT(v4u, p + (size_t)32 * 3072);
      cv[0] = cvec[e * 64 + (t >> 4)]; cv[1] = cvec[e * 64 + 32 + (t >> 4)];
    }
    CBAR();
    {
      const int pc = t >= 64 ? t - 64 : t;
      st16(rs, wave == 0 ? DUMMY_OFF + ti * 1024 + lane * 16 : S2_OFF + ti * (EGC_PIECES * 16) + (pc < EGC_PIECES ? pc : pc - EGC_PIECES) * 16, sv4);
    }
    CBAR();
    float ln0 = 0.f, ln1 = 0.f;
    if (wave == 0) {
      float xs[8] = {bflo(xv.x), bfhi(xv.x), bflo(xv.y), bfhi(xv.y), bflo(xv.z), bfhi(xv.z), bflo(xv.w), bfhi(xv.w)};
      float s = 0;
#pragma unroll
      for (int i = 0; i < 8; i++) s += xs[i];
      const float mean = wave_sum(s) * (1.f / 512.f);
      float vs = 0;
#pragma unroll
      for (int i = 0; i < 8; i++) { float d = xs[i] - mean; vs += d * d; }
      const float rstd = rsqrtf(wave_sum(vs) * (1.f / 512.f) + 1e-5f);
      ln0 = rbf((bflo(xw) - mean) * rstd); ln1 = rbf((bfhi(xw) - mean) * rstd);
      v4u tb = {old0.y, old0.w, old1.y, old1.w};
      *(LAS v4u*)(tbase + lane * 8) = tb; tbase[lane * 8 + 4] = oldf;
      if (lane == 0) { cnt[0] = 2u + NREP; cnt[1] = 0u; }
    }
    CBAR();
    {
      const float c0 = bflo(cv[0]), c1 = bflo(cv[1]);
      const unsigned s01 = pk2(c0 * __builtin_amdgcn_rcpf(1.f + __expf(-c0)), c1 * __builtin_amdgcn_rcpf(1.f + __expf(-c1)));
      interleave(wm[0], wm[1]);
      float a[8] = {dot2(wm[0].x, s01, 0.f), dot2(wm[0].y, s01, 0.f), dot2(wm[0].z, s01, 0.f), dot2(wm[0].w, s01, 0.f),
                    dot2(wm[1].x, s01, 0.f), dot2(wm[1].y, s01, 0.f), dot2(wm[1].z, s01, 0.f), dot2(wm[1].w, s01, 0.f)};
      float b0 = red32(a[0], a[4]), b1x = red32(a[1], a[5]), b2x = red32(a[2], a[6]), b3 = red32(a[3], a[7]);
      f32x2 o = {red16(b0, b2x), red16(b1x, b3)};
      *(LAS f32x2*)(apart + wave * 128 + n * 8 + 4 * (lane >> 5) + 2 * ((lane >> 4) & 1)) = o;
    }
    bar();
    STAMP(8);
    if (wave == 0) {
      f32x2 sacc = {0.f, 0.f};
#pragma unroll
      for (int w = 0; w < 8; w++) sacc += *(LAS f32x2*)(apart + w * 128 + lane * 2);
      auto r0 = __builtin_amdgcn_permlane32_swap(__float_as_uint(sacc.x), __float_as_uint(sacc.x), false, false);
      auto r1 = __builtin_amdgcn_permlane32_swap(__float_as_uint(sacc.y), __float_as_uint(sacc.y), false, false);
      const float one = e == 0 ? 1.f : 0.f;
      const float u0 = ln0 * (one + __uint_as_float(r0[1])) + __uint_as_float(r0[0]), u1 = ln1 * (one + __uint_as_float(r1[1])) + __uint_as_float(r1[0]);
      if (lane < 32) pub[lane] = pk2(u0, u1);
      LGWAIT0();
      pd = *(LAS v2u*)(pub + n * 2);
      v4u o = {pd.x, old0.y + 1u, pd.y, old0.w + 1u}; st16(rs, puoff, o);
      v4u o2 = {pd.x, old1.y + 1u, pd.y, old1.w + 1u}; st16(rs, puoff + 65536, o2);
    }
    STAMP(9);
    unsigned karr = 0;
    if (wave != 0) { VMWAIT0(); if (lane == 0) karr = LDSADD(cnt + 1); }
    const bool lastw = __builtin_amdgcn_readfirstlane(karr) == 6u;
    if (lastw && lane < 16) { v2u f = {0u, oldf + 1u}; st8(rs, pfoff, f); }
    STAMP(10);
    DELAY(EGL_DELAY);
    const float xme = bflo(x[mcol]);
    const float b1v = bflo(b1[wst * 8 + (lane & 31)]);
    CBAR();
#pragma unroll
    for (int i = 0; i < 8; i++) {
      const int rg = 8 * i + wave, row = 4 * rg + q;
      dma16(w_uv + (size_t)(half * 256 + row) * 4096 + head * 128 + ((n ^ swz(row)) * 8), L + rg * 1024);
    }
#pragma unroll
    for (int i = 0; i < 4; i++) if (gi < 43) { const int rg = 8 * i + wave; dma16n(w1 + (size_t)(rg * 16 + (lane >> 2)) * 1376 + wst * 8 + (lane & 3) * 8, W1I + rg * 1024); }
    CBAR();
    if (wave == 0 || lastw) {
#pragma unroll 1
      for (int k = 0; k < NREP; k++) {
        __builtin_amdgcn_s_sleep(14);
        if (wave == 0) { v4u o_ = {pd.x, old0.y + 2u + k, pd.y, old0.w + 2u + k}; st16(rs, puoff, o_); v4u o2_ = {pd.x, old1.y + 2u + k, pd.y, old1.w + 2u + k}; st16(rs, puoff + 65536, o2_); }
        if (lastw && lane < 16) { v2u f_ = {0u, oldf + 2u + k}; st8(rs, pfoff, f_); }
      }
    }
    VMWAIT0();
    bar();
    STAMP(1);
    bf16x8 wvf[8], w1f[4];
#pragma unroll
    for (int s2 = 0; s2 < 8; s2++) wvf[s2] = trfrag<256, true>(L, 32 * s2, 2 * wave, lane);
#pragma unroll
    for (int i = 0; i < 4; i++) w1f[i] = trfrag<64, false>(W1I, 64 * wave + 32 * (i >> 1), 2 * (i & 1), lane);
    if (wave == 1) {
      int spins = 0; bool ok = false;
      for (;;) { CBAR(); if (!ok) ok = ld8(rs, sfoff).y != snapf; if (__all(ok) || ++spins > SPIN_CAP) break; if ((spins & 3) == 0) REPUB(); __builtin_amdgcn_s_sleep(2); }
      spins = 0;
      for (;;) { CBAR(); v4u v = ld16(rs, S2_OFF + xc * 32768 + head * 1024 + lane * 16); if (__all(mx4(v) != SENT) || ++spins > SPIN_CAP) break; if ((spins & 3) == 0) REPUB(); __builtin_amdgcn_s_sleep(2); }
    }
    bar();
    STAMP(2);
    LAS float* invl = (LAS float*)(SM + 512);
    {
      v4u p0, p1;
      const int off = S3_OFF + (head * 32 + 4 * wave + (lane >> 5)) * S3R + half * 512 + (lane & 31) * 16;
      int spins = 0;
      if (wave == 7) {
        const int soff = S3_OFF + (head * 32 + (lane & 31)) * S3R + 1024;
        v2u stt;
        for (;;) {
          CBAR();
          stt = ld8(rs, soff); p0 = ld16(rs, off); p1 = ld16(rs, off + 2 * S3R);
          const unsigned m = max(max(mx4(p0), mx4(p1)), max(stt.x, stt.y));
          if (__all(m != SENT) || ++spins > SPIN_CAP) break;
          if ((spins & 3) == 0) REPUB();
        }
        const float m = __uint_as_float(stt.x), l = __uint_as_float(stt.y);
        const float mg = wave_max(m);
        const __bf16 scb = (__bf16)__builtin_amdgcn_exp2f(m - mg);
        if (lane < 32) scl[lane] = __builtin_bit_cast(u16, scb);
        const float il = 2.f * __builtin_amdgcn_rcpf(wave_sum((float)scb * l));
        if (lane == 0) *invl = il;
      } else {
        for (;;) {
          CBAR();
          p0 = ld16(rs, off); p1 = ld16(rs, off + 2 * S3R);
          if (__all(max(mx4(p0), mx4(p1)) != SENT) || ++spins > SPIN_CAP) break;
          if ((spins & 15) == 0) REPUB();
        }
      }
      STAMP(3);
      { const int row = 4 * wave + (lane >> 5); *(LAS v4u*)(STG + row * 512 + (((lane & 31) ^ swz(row)) * 16)) = p0; *(LAS v4u*)(STG + (row + 2) * 512 + (((lane & 31) ^ swz(row + 2)) * 16)) = p1; }
    }
    bar();
    {
      const bf16x8 sf = *(const LAS bf16x8*)(scl + q * 8);
      const float inv_l = *invl;
      const f32x4 a0 = MFMA(sf, (trfrag<512, true>(STG, 0, wave * 4, lane)), ((f32x4){0, 0, 0, 0}));
      const f32x4 a1 = MFMA(sf, (trfrag<512, true>(STG, 0, wave * 4 + 2, lane)), ((f32x4){0, 0, 0, 0}));
      const float cv2 = (q == 0 ? a0[0] : a1[0]) * inv_l;
      const unsigned d = pk2(cv2, dppf<0x101>(cv2));
      if (lane < 32 && !(lane & 1)) cc16[wave * 16 + (lane >> 1)] = d;
    }
    bar();
    {
      f32x4 a0 = {0, 0, 0, 0};
#pragma unroll
      for (int s2 = 0; s2 < 8; s2++) a0 = MFMA(*(const LAS bf16x8*)(cc16 + s2 * 16 + q * 4), wvf[s2], a0);
      const unsigned d = canon(pk2(a0[0], dppf<0x101>(a0[0])));
      if (lane < 16 && !(lane & 1)) cc16[128 + wave * 8 + (lane >> 1)] = d;
    }
    bar();
    if (t < 16) { const v4u o = *(const LAS v4u*)(cc16 + 128 + t * 4); st16(rs, S4_OFF + (head * 2 + half) * 256 + t * 16, o); }
    STAMP(4);
    if (gi >= 43) {
      if (wave == 0) {
        int spins = 0;
        for (;;) {
          CBAR(); v4u v = ld16(rs, DN_OFF + xc * 128);
          if (v.x != SENT || ++spins > SPIN_CAP) break;
          if ((spins & 3) == 0) REPUB();
          __builtin_amdgcn_s_sleep(8);
        }
      }
      STAMP_FLUSH(); return;
    }
    {
      v4u v0, v1, v2; int spins = 0;
      const int off = SL_OFF + wave * 4096 + lane * 16;
      for (;;) { CBAR(); v0 = ld16(rs, off); v1 = ld16(rs, off + 1024); v2 = ld16(rs, off + 2048); if (__all(max(max(mx4(v0), mx4(v1)), mx4(v2)) != SENT) || ++spins > SPIN_CAP) break; if ((spins & 15) == 0) REPUB(); __builtin_amdgcn_s_sleep(1); }
      *(LAS v4u*)(L + wave * 3072 + lane * 16) = v0; *(LAS v4u*)(L + wave * 3072 + 1024 + lane * 16) = v1; *(LAS v4u*)(L + wave * 3072 + 2048 + lane * 16) = v2;
    }
    bar();
    float gam = 0.f, sbm = 0.f, cb1 = 0.f;
    {
      const LAS u16* mlh = (const LAS u16*)L;
#pragma unroll
      for (int k = 0; k < 8; k++) { gam += bflo(mlh[k * 1536 + mcol]); sbm += bflo(mlh[k * 1536 + 512 + mcol]); cb1 += bflo(mlh[k * 1536 + 1024 + mcol]); }
      gam = rbf(gam); sbm = rbf(sbm); cb1 = rbf(1.f + rbf(cb1));
    }
    asm volatile("" : "+v"(gam), "+v"(sbm), "+v"(cb1));
    STAMP(5);
    float xnew;
    {
      v4u v; int spins = 0;
      const int off = S5_OFF + xc * 8192 + (lane >> 3) * 1024 + wave * 128 + (lane & 7) * 16;
      for (;;) { CBAR(); v = ld16(rs, off); if (__all(mx4(v) != SENT) || ++spins > SPIN_CAP) break; if ((spins & 15) == 0) REPUB(); }
      STAMP(6);
      const float b0 = red32(bflo(v.x), bflo(v.z)), b1x = red32(bfhi(v.x), bfhi(v.z)), b2x = red32(bflo(v.y), bflo(v.w)), b3 = red32(bfhi(v.y), bfhi(v.w));
      const float c0 = red16(b0, b2x), c1 = red16(b1x, b3);
      const bool up = lane & 8;
      const float keep = up ? c1 : c0, send = up ? c0 : c1;
      const float ao = keep + dppf<0x128>(send);
      xnew = rbf(xme + rbf(gam * rbf(ao)));
      const float sv = wave_sum2(xnew, xnew * xnew);
      if ((lane & 47) == 0) stat[wave * 2 + (lane >> 4)] = sv;
    }
    bar();
    float h2;
    {
      float v = stat[n];
      v += dppf<0x128>(v); v += dppf<0x124>(v); v += dppf<0x122>(v);
      const float o = dppf<0xB1>(v);
      const float s1 = (lane & 1) ? o : v, s2 = (lane & 1) ? v : o;
      const float mean = s1 * (1.f / 512.f), var = fmaxf(s2 * (1.f / 512.f) - mean * mean, 0.f);
      const float rstd2 = __builtin_amdgcn_rsqf(var + 1e-5f);
      h2 = rbf(rbf(rbf((xnew - mean) * rstd2) * cb1) + sbm);
      const float w = wave_sum2(h2, h2 * h2);
      if ((lane & 47) == 0) stat[16 + wave * 2 + (lane >> 4)] = w;
    }
    bar();
    {
      float v = stat[16 + n];
      v += dppf<0x128>(v); v += dppf<0x124>(v); v += dppf<0x122>(v);
      const float o = dppf<0xB1>(v);
      const float s1 = (lane & 1) ? o : v, s2 = (lane & 1) ? v : o;
      const float mean = s1 * (1.f / 512.f), var = fmaxf(s2 * (1.f / 512.f) - mean * mean, 0.f);
      const float rstd2 = __builtin_amdgcn_rsqf(var + 1e-5f);
      y16[mcol] = __builtin_bit_cast(u16, (__bf16)((h2 - mean) * rstd2));
      LGWAIT0();
      const bf16x8 y0 = *(const LAS bf16x8*)(y16 + wave * 64 + q * 8), y1 = *(const LAS bf16x8*)(y16 + wave * 64 + 32 + q * 8);
      f32x4 z0 = MFMA(y0, w1f[0], ((f32x4){0, 0, 0, 0})), z1 = MFMA(y0, w1f[1], ((f32x4){0, 0, 0, 0}));
      z0 = MFMA(y1, w1f[2], z0); z1 = MFMA(y1, w1f[3], z1);
      if (lane < 32) gpart[wave * 32 + lane] = q == 0 ? z0[0] : z1[0];
    }
    bar();
    if (wave == 0) {
      float a = 0;
#pragma unroll
      for (int w = 0; w < 8; w++) a += gpart[w * 32 + (lane & 31)];
      const float z = rbf(rbf(a) + b1v);
      const float u = z * (0.7978845608028654f + 0.035677408136300125f * z * z);
      const float gl = z * __builtin_amdgcn_rcpf(1.f + __builtin_amdgcn_exp2f(-2.885390081777927f * u));
      const unsigned d = canon(pk2(gl, dppf<0x101>(gl)));
      if (lane < 32 && !(lane & 1)) ((LAS unsigned*)y16)[lane >> 1] = d;
      LGWAIT0();
      const v4u o = *(const LAS v4u*)((LAS unsigned*)y16 + (lane & 3) * 4);
      if (lane < 32) st16(rs, S6_OFF + (lane >> 2) * 6144 + gi * 128 + (lane & 3) * 16, o);
    }
    if (ti == 0 && wave == 1 && lane < 8) { v4u o = {1, 1, 1, 1}; st16(rs, DN_OFF + lane * 128, o); }
    STAMP(7);
    STAMP_FLUSH();
  } else {
    const int e = r - 16, split = 4 * xc + (e & 3), hg = e >> 2;
    const int sfoff = SF_OFF + (8 + xc) * 8192 + lane * 128;
    LAS unsigned char* qa = SM;
    LAS unsigned char* Pm = SM + 16896;
    LAS float* wmax = (LAS float*)(SM + 21504);
    LAS float* wsum = wmax + 128;
    unsigned snapf = 0;
    if (wave == 0) snapf = ld8(rs, sfoff).y;
    bar();
    CBAR();
#pragma unroll
    for (int i = 0; i < 16; i++) {
      const int key = ((i * 8 + wave) + hg * 64) & 127;
      dma16(ckv + (size_t)(split * 128 + key) * 512 + ((lane ^ swz(key)) * 8), L + key * 1024);
    }
    VMWAIT0();
    bar();
    STAMP(3);
    bf16x8 ka[16], va[16];
    {
      const int key = wave * 16 + n, f2 = swz(key);
#pragma unroll
      for (int s2 = 0; s2 < 16; s2++) ka[s2] = *(const LAS bf16x8*)(L + key * 1024 + (((4 * s2 + q) ^ f2) * 16));
#pragma unroll
      for (int i = 0; i < 16; i++) va[i] = trfrag<1024, true>(L, 32 * (i & 3), wave * 8 + 2 * (i >> 2), lane);
    }
    if (wave == 0) {
      int spins = 0; bool ok = false;
      for (;;) { CBAR(); if (!ok) ok = ld8(rs, sfoff).y != snapf; if (__all(ok) || ++spins > SPIN_CAP) break; __builtin_amdgcn_s_sleep(1); }
    }
    bar();
    STAMP(4);
    {
      const int off = S2_OFF + xc * 32768 + hg * 16384 + t * 16;
      v4u v0, v1; int spins = 0;
      for (;;) {
        CBAR();
        v0 = ld16(rs, off); v1 = ld16(rs, off + 8192);
        if (__all(max(mx4(v0), mx4(v1)) != SENT) || ++spins > SPIN_CAP) break;
      }
      *(LAS v4u*)(qa + wave * 1056 + lane * 16) = v0;
      *(LAS v4u*)(qa + (8 + wave) * 1056 + lane * 16) = v1;
    }
    bar();
    STAMP(5);
    f32x4 sc = {0, 0, 0, 0};
#pragma unroll
    for (int s2 = 0; s2 < 16; s2++) sc = MFMA(ka[s2], *(const LAS bf16x8*)(qa + n * 1056 + s2 * 64 + q * 16), sc);
    float mx = fmaxf(fmaxf(sc[0], sc[1]), fmaxf(sc[2], sc[3]));
    mx = max16(mx); mx = max32(mx);
    if (lane < 16) wmax[lane * 8 + wave] = mx;
    bar();
    {
      f32x4 m0 = *(LAS f32x4*)(wmax + n * 8), m1 = *(LAS f32x4*)(wmax + n * 8 + 4);
      mx = fmaxf(fmaxf(fmaxf(m0[0], m0[1]), fmaxf(m0[2], m0[3])), fmaxf(fmaxf(m1[0], m1[1]), fmaxf(m1[2], m1[3])));
    }
    const float p0 = __builtin_amdgcn_exp2f(sc[0] - mx), p1 = __builtin_amdgcn_exp2f(sc[1] - mx), p2 = __builtin_amdgcn_exp2f(sc[2] - mx), p3 = __builtin_amdgcn_exp2f(sc[3] - mx);
    { v2u o = {pk2(p0, p1), pk2(p2, p3)}; *(LAS v2u*)(Pm + n * 288 + (wave * 16 + q * 4) * 2) = o; }
    float ps = (p0 + p1) + (p2 + p3);
    ps = red16(ps, ps); ps = red32(ps, ps);
    if (lane < 16) wsum[lane * 8 + wave] = ps;
    bar();
    {
      bf16x8 bp[4];
#pragma unroll
      for (int ks = 0; ks < 4; ks++) bp[ks] = *(const LAS bf16x8*)(Pm + n * 288 + ks * 64 + q * 16);
      f32x4 acc[4];
#pragma unroll
      for (int T = 0; T < 4; T++) {
        acc[T] = (f32x4){0, 0, 0, 0};
#pragma unroll
        for (int ks = 0; ks < 4; ks++) acc[T] = MFMA(va[T * 4 + ks], bp[ks], acc[T]);
      }
      unsigned d0[4], d1[4];
#pragma unroll
      for (int T = 0; T < 4; T++) { d0[T] = canon(pk2(acc[T][0], acc[T][1])); d1[T] = canon(pk2(acc[T][2], acc[T][3])); }
      auto r0 = __builtin_amdgcn_permlane16_swap(d0[0], d0[1], false, false);
      auto r1 = __builtin_amdgcn_permlane16_swap(d1[0], d1[1], false, false);
      auto r2 = __builtin_amdgcn_permlane16_swap(d0[2], d0[3], false, false);
      auto r3 = __builtin_amdgcn_permlane16_swap(d1[2], d1[3], false, false);
      v4u oa = {r0[0], r1[0], r0[1], r1[1]}, ob = {r2[0], r3[0], r2[1], r3[1]};
      const v4u obs = {dppu<0x128>(ob.x), dppu<0x128>(ob.y), dppu<0x128>(ob.z), dppu<0x128>(ob.w)};
      const bool lo = n < 8;
      const int o3b = S3_OFF + ((hg * 16 + (n & 7)) * 32 + split) * S3R + (wave * 64 + (q & 1) * 16 + (q & 2) * 4) * 2;
      st16(rs, o3b + (lo ? 0 : 64), lo ? oa : obs);
      st16(rs, o3b + 8 * 32 * S3R + (lo ? 64 : 0), lo ? obs : oa);
      if (wave == 7) {
        f32x4 s0 = *(LAS f32x4*)(wsum + n * 8), s1 = *(LAS f32x4*)(wsum + n * 8 + 4);
        const float l = ((s0[0] + s0[1]) + (s0[2] + s0[3])) + ((s1[0] + s1[1]) + (s1[2] + s1[3]));
        v2u o2 = {canon(__float_as_uint(mx)), canon(__float_as_uint(l))};
        if (lane < 16) st8(rs, S3_OFF + ((hg * 16 + lane) * 32 + split) * S3R + 1024, o2);
      }
    }
    STAMP(6);
    STAMP_FLUSH();
  }
}

extern "C" void kernel_launch(void* const* d_in, const int* in_sizes, int n_in, void* d_out, int out_size, void* d_ws, size_t ws_size, hipStream_t stream) {
  (void)in_sizes; (void)n_in; (void)out_size; (void)ws_size;
  (void)hipFuncSetAttribute((const void*)fused_kernel, hipFuncAttributeMaxDynamicSharedMemorySize, L_TOTAL);
  fused_kernel<<<256, NT, L_TOTAL, stream>>>((const u16*)d_in[0], (const u16*)d_in[1], (const u16*)d_in[2], (const u16*)d_in[3], (const u16*)d_in[4], (const u16*)d_in[5],
                                             (const u16*)d_in[7], (const u16*)d_in[8], (const u16*)d_in[9], (const u16*)d_in[10], (const u16*)d_in[11], (const u16*)d_in[12],
                                             (u16*)d_out, (unsigned char*)d_ws);
}
```

```cpp
#include <hip/hip_runtime.h>
#include <stdint.h>

typedef unsigned short u16;
typedef unsigned v4u __attribute__((ext_vector_type(4)));
typedef unsigned v2u __attribute__((ext_vector_type(2)));
typedef float f32x4 __attribute__((ext_vector_type(4)));
typedef float f32x2 __attribute__((ext_vector_type(2)));
typedef short bf16x8 __attribute__((ext_vector_type(8)));
typedef short s16x4 __attribute__((ext_vector_type(4)));
typedef __bf16 bf2 __attribute__((ext_vector_type(2)));
#define LAS __attribute__((address_space(3)))

constexpr unsigned SENT = 0xFFFFFFFFu;
constexpr int NT = 512;
constexpr int SPIN_CAP = 1 << 20;
#define PSL 0
#define POLLSLEEP() do { if (PSL) __builtin_amdgcn_s_sleep(PSL); } while (0)
#define NREP 2
#define EGL_DELAY 400
#define CLRAUX 16
#define NTAUX 2
#define NTS " nt"
#define LDNT(T, p) __builtin_nontemporal_load((const T*)(p))
constexpr int SU_OFF = 0;
constexpr int SF_OFF = 131072;
constexpr int SQ_OFF = 262144;
constexpr int CLR_OFF = 327680;
constexpr int S2_OFF = CLR_OFF;
constexpr int S5_OFF = S2_OFF + 8 * 32768;
constexpr int S6_OFF = S5_OFF + 65536;
constexpr int SL_OFF = S6_OFF + 49152;
constexpr int DN_OFF = SL_OFF + 32768;
constexpr int EGC_END = DN_OFF + 4096;
constexpr int EGC_PIECES = (EGC_END - S2_OFF) / 16 / 64;
constexpr int S4_OFF = EGC_END;
constexpr int S3R = 1152;
constexpr int S3_OFF = S4_OFF + 16384;
constexpr int CLR_END = S3_OFF + 1024 * S3R;
constexpr unsigned FLAG = 0x80000000u;
constexpr int DUMMY_OFF = CLR_END;
constexpr int WS_USED = CLR_END + 65536;
#define STAMP(i) do {} while (0)
#define STAMP_DECL do {} while (0)
#define STAMP_FLUSH() do {} while (0)

constexpr int L_CKV = 0;
constexpr int L_SM = 131072;
constexpr int L_TOTAL = 131072 + 30720;

__device__ __forceinline__ float bflo(unsigned w) { return __uint_as_float(w << 16); }
__device__ __forceinline__ float bfhi(unsigned w) { return __uint_as_float(w & 0xffff0000u); }
__device__ __forceinline__ unsigned pk2(float a, float b) { bf2 p; p.x = (__bf16)a; p.y = (__bf16)b; return __builtin_bit_cast(unsigned, p); }
__device__ __forceinline__ float rbf(float a) { return (float)(__bf16)a; }
__device__ __forceinline__ unsigned canon(unsigned w) { return w == SENT ? 0x7FC07FC0u : w; }
__device__ __forceinline__ float dot2(unsigned a, unsigned b, float c) { return __builtin_amdgcn_fdot2_f32_bf16(__builtin_bit_cast(bf2, a), __builtin_bit_cast(bf2, b), c, false); }
__device__ __forceinline__ void bar() { __builtin_amdgcn_sched_barrier(0); asm volatile("s_waitcnt lgkmcnt(0)\n\ts_barrier" ::: "memory"); __builtin_amdgcn_sched_barrier(0); }
template <int CTRL> __device__ __forceinline__ float dppf(float v) { return __int_as_float(__builtin_amdgcn_update_dpp(0, __float_as_int(v), CTRL, 0xf, 0xf, false)); }
template <int CTRL> __device__ __forceinline__ unsigned dppu(unsigned v) { return (unsigned)__builtin_amdgcn_update_dpp(0, (int)v, CTRL, 0xf, 0xf, false); }
__device__ __forceinline__ float red32(float a, float b) {
  auto r = __builtin_amdgcn_permlane32_swap(__float_as_uint(a), __float_as_uint(b), false, false);
  return __uint_as_float(r[0]) + __uint_as_float(r[1]);
}
__device__ __forceinline__ float red16(float a, float b) {
  auto r = __builtin_amdgcn_permlane16_swap(__float_as_uint(a), __float_as_uint(b), false, false);
  return __uint_as_float(r[0]) + __uint_as_float(r[1]);
}
__device__ __forceinline__ float max32(float a) { auto r = __builtin_amdgcn_permlane32_swap(__float_as_uint(a), __float_as_uint(a), false, false); return fmaxf(__uint_as_float(r[0]), __uint_as_float(r[1])); }
__device__ __forceinline__ float max16(float a) { auto r = __builtin_amdgcn_permlane16_swap(__float_as_uint(a), __float_as_uint(a), false, false); return fmaxf(__uint_as_float(r[0]), __uint_as_float(r[1])); }
__device__ __forceinline__ float wave_sum(float v) {
  v += dppf<0x128>(v); v += dppf<0x124>(v); v += dppf<0x122>(v); v += dppf<0x121>(v);
  v = red16(v, v); v = red32(v, v); return v;
}
__device__ __forceinline__ float wave_max(float v) {
  v = fmaxf(v, dppf<0x128>(v)); v = fmaxf(v, dppf<0x124>(v)); v = fmaxf(v, dppf<0x122>(v)); v = fmaxf(v, dppf<0x121>(v));
  v = max16(v); v = max32(v); return v;
}
__device__ __forceinline__ unsigned ilo(unsigned a, unsigned b) { return __builtin_amdgcn_perm(b, a, 0x05040100u); }
__device__ __forceinline__ unsigned ihi(unsigned a, unsigned b) { return __builtin_amdgcn_perm(b, a, 0x07060302u); }
__device__ __forceinline__ void interleave(v4u& a, v4u& b) {
  v4u x, y;
  x.x = ilo(a.x, b.x); x.y = ihi(a.x, b.x); x.z = ilo(a.y, b.y); x.w = ihi(a.y, b.y);
  y.x = ilo(a.z, b.z); y.y = ihi(a.z, b.z); y.z = ilo(a.w, b.w); y.w = ihi(a.w, b.w);
  a = x; b = y;
}
__device__ __forceinline__ bool valid4(v4u v) { return (v.x != SENT) & (v.y != SENT) & (v.z != SENT) & (v.w != SENT); }
typedef __amdgpu_buffer_rsrc_t rsrc_t;
__device__ __forceinline__ v4u ld16(rsrc_t r, int off) { return __builtin_amdgcn_raw_buffer_load_b128(r, off, 0, 16); }
__device__ __forceinline__ v2u ld8(rsrc_t r, int off) { return __builtin_amdgcn_raw_buffer_load_b64(r, off, 0, 16); }
__device__ __forceinline__ void st16(rsrc_t r, int off, v4u v) { __builtin_amdgcn_raw_buffer_store_b128(v, r, off, 0, 16); }
__device__ __forceinline__ void st8(rsrc_t r, int off, v2u v) { __builtin_amdgcn_raw_buffer_store_b64(v, r, off, 0, 16); }
__device__ __forceinline__ void glds16(const void* g, unsigned lds_base) {
  unsigned sv; asm volatile("s_mov_b32 %0, m0\n\ts_mov_b32 m0, %2\n\ts_nop 0\n\tglobal_load_lds_dwordx4 %1, off" NTS "\n\ts_mov_b32 m0, %0" : "=&s"(sv) : "v"(g), "s"(lds_base) : "memory");
}
#define SB() __builtin_amdgcn_sched_barrier(0)
#define CBAR() do { asm volatile("" ::: "memory"); __builtin_amdgcn_sched_barrier(0); } while (0)

__device__ __forceinline__ int swz(int row) { return ((row & 3) | ((row >> 1) & 4)) << 1; }
template <int AUX> __device__ __forceinline__ void dma16a(const void* g, LAS unsigned char* l) { __builtin_amdgcn_global_load_lds((const __attribute__((address_space(1))) unsigned*)g, (LAS unsigned*)l, 16, 0, AUX); }
#define dma16 dma16a<NTAUX>
#define NARROW 2
#define dma16n dma16a<NARROW>
#define LDNAR(T, p) __builtin_nontemporal_load((const T*)(p))
template <int RB, bool SW>
__device__ __forceinline__ bf16x8 trfrag(const LAS unsigned char* img, int k0, int ch0, int lane) {
  const int g = lane >> 4, qq = (lane & 15) >> 2, pp = lane & 3;
  s16x4 h[2];
#pragma unroll
  for (int tt = 0; tt < 2; tt++) {
    const int row = k0 + 8 * g + 4 * tt + qq;
    const int ch = SW ? ((ch0 + (pp >> 1)) ^ swz(row)) : (ch0 + (pp >> 1));
    h[tt] = __builtin_amdgcn_ds_read_tr16_b64_v4i16((LAS s16x4*)(img + row * RB + ch * 16 + (pp & 1) * 8));
  }
  return (bf16x8){h[0].x, h[0].y, h[0].z, h[0].w, h[1].x, h[1].y, h[1].z, h[1].w};
}
#define MFMA(a, b, c) __builtin_amdgcn_mfma_f32_16x16x32_bf16(a, b, c, 0, 0, 0)
#define VMWAIT0() do { asm volatile("" ::: "memory"); __builtin_amdgcn_s_waitcnt(0x0F70); asm volatile("" ::: "memory"); } while (0)
#define LGWAIT0() asm volatile("s_waitcnt lgkmcnt(0)" ::: "memory")
__device__ __forceinline__ float wave_sum2(float a, float b) {
  a += dppf<0x128>(a); b += dppf<0x128>(b); a += dppf<0x124>(a); b += dppf<0x124>(b);
  a += dppf<0x122>(a); b += dppf<0x122>(b); a += dppf<0x121>(a); b += dppf<0x121>(b);
  float v = red16(a, b); return red32(v, v);
}

typedef unsigned long long u64;
__device__ __forceinline__ void ldm4(rsrc_t r, int off, unsigned s1, unsigned s2, unsigned s3, v4u& a, v4u& b, v4u& c, v4u& d, u64 m0, u64 m1, u64 m2, u64 m3) {
  u64 sv;
  asm volatile("s_mov_b64 %0, exec\n\t"
               "s_mov_b64 exec, %10\n\tbuffer_load_dwordx4 %1, %5, %6, 0 offen sc1\n\t"
               "s_mov_b64 exec, %11\n\tbuffer_load_dwordx4 %2, %5, %6, %7 offen sc1\n\t"
               "s_mov_b64 exec, %12\n\tbuffer_load_dwordx4 %3, %5, %6, %8 offen sc1\n\t"
               "s_mov_b64 exec, %13\n\tbuffer_load_dwordx4 %4, %5, %6, %9 offen sc1\n\t"
               "s_mov_b64 exec, %0\n\ts_waitcnt vmcnt(0)"
               : "=&s"(sv), "+v"(a), "+v"(b), "+v"(c), "+v"(d) : "v"(off), "s"(r), "s"(s1), "s"(s2), "s"(s3), "s"(m0), "s"(m1), "s"(m2), "s"(m3) : "memory");
}
__device__ __forceinline__ void ldm2(rsrc_t r, int off, unsigned s1, v4u& a, v4u& b, u64 m0, u64 m1) {
  u64 sv;
  asm volatile("s_mov_b64 %0, exec\n\t"
               "s_mov_b64 exec, %6\n\tbuffer_load_dwordx4 %1, %3, %4, 0 offen sc1\n\t"
               "s_mov_b64 exec, %7\n\tbuffer_load_dwordx4 %2, %3, %4, %5 offen sc1\n\t"
               "s_mov_b64 exec, %0\n\ts_waitcnt vmcnt(0)"
               : "=&s"(sv), "+v"(a), "+v"(b) : "v"(off), "s"(r), "s"(s1), "s"(m0), "s"(m1) : "memory");
}
__device__ __forceinline__ void ldm6(rsrc_t r, int off, v4u (&h)[6], u64 m0, u64 m1, u64 m2, u64 m3, u64 m4, u64 m5) {
  u64 sv;
  asm volatile("s_mov_b64 %0, exec\n\t"
               "s_mov_b64 exec, %9\n\tbuffer_load_dwordx4 %1, %7, %8, 0 offen sc1\n\t"
               "s_mov_b64 exec, %10\n\tbuffer_load_dwordx4 %2, %7, %8, 0 offen offset:512 sc1\n\t"
               "s_mov_b64 exec, %11\n\tbuffer_load_dwordx4 %3, %7, %8, 0 offen offset:1024 sc1\n\t"
               "s_mov_b64 exec, %12\n\tbuffer_load_dwordx4 %4, %7, %8, 0 offen offset:1536 sc1\n\t"
               "s_mov_b64 exec, %13\n\tbuffer_load_dwordx4 %5, %7, %8, 0 offen offset:2048 sc1\n\t"
               "s_mov_b64 exec, %14\n\tbuffer_load_dwordx4 %6, %7, %8, 0 offen offset:2560 sc1\n\t"
               "s_mov_b64 exec, %0\n\ts_waitcnt vmcnt(0)"
               : "=&s"(sv), "+v"(h[0]), "+v"(h[1]), "+v"(h[2]), "+v"(h[3]), "+v"(h[4]), "+v"(h[5]) : "v"(off), "s"(r), "s"(m0), "s"(m1), "s"(m2), "s"(m3), "s"(m4), "s"(m5) : "memory");
}
__device__ __forceinline__ u64 inval(v4u v) { return __builtin_amdgcn_ballot_w64(!valid4(v)); }

__device__ __forceinline__ unsigned mx4(v4u v) { return max(max(v.x, v.y), max(v.z, v.w)); }
__device__ __forceinline__ void stclr(rsrc_t r, int off, v4u v) { __builtin_amdgcn_raw_buffer_store_b128(v, r, off, 0, CLRAUX); }

__global__ void __launch_bounds__(NT, 2) fused_kernel(
    const u16* __restrict__ x, const u16* __restrict__ ckv, const u16* __restrict__ w_q, const u16* __restrict__ w_uk,
    const u16* __restrict__ w_uv, const u16* __restrict__ w_o, const u16* __restrict__ w1, const u16* __restrict__ b1,
    const u16* __restrict__ w2, const u16* __restrict__ b2, const u16* __restrict__ cvec, const u16* __restrict__ w_mod,
    u16* __restrict__ out, unsigned char* ws) {
  extern __shared__ __attribute__((aligned(16))) unsigned char lds_raw[];
  LAS unsigned char* L = (LAS unsigned char*)lds_raw;
  const int t = threadIdx.x, lane = t & 63, wave = __builtin_amdgcn_readfirstlane(t >> 6);
  const int b = blockIdx.x, xc = b & 7, r = b >> 3;
  const int n = lane & 15, q = lane >> 4;
  const rsrc_t rs = __builtin_amdgcn_make_buffer_rsrc((void*)ws, 0, WS_USED, 0x00020000);
  LAS unsigned char* SM = L + L_SM;
  const v4u sv4 = {SENT, SENT, SENT, SENT};
  STAMP_DECL;
  const u64 tst = __builtin_amdgcn_s_memrealtime();
  (void)tst;
  STAMP(0);
#define DELAY(x) do { while (__builtin_amdgcn_s_memrealtime() - tst < (x)) __builtin_amdgcn_s_sleep(2); } while (0)

  if (r < 16) {
    const int head = 4 * xc + (r >> 2), cs = r & 3;
    LAS unsigned char* hp = SM;
    LAS unsigned* q16 = (LAS unsigned*)(SM + 2304);
    LAS unsigned char* qst = SM + 8192;
    LAS unsigned char* WKI = L + 98304;
    LAS unsigned char* WQI = L + 65536;
    const int sfoff = SF_OFF + xc * 8192 + lane * 128;
    LAS unsigned* lcnt = (LAS unsigned*)(SM + 9472);
    const unsigned short cvl0 = cvec[(r & 7) * 64 + (t >> 4)], cvl1 = cvec[(r & 7) * 64 + 32 + (t >> 4)];
    bf16x8 wk[4];
    if (wave < 4) {
      const int suoff = SU_OFF + xc * 16384 + cs * 4096 + t * 16;
      const int sqoff = SQ_OFF + head * 2048 + (t & 127) * 16;
      const v4u snapu = ld16(rs, suoff);
      const v4u snapq = ld16(rs, sqoff);
      const unsigned snapf = ld8(rs, sfoff).y;
      if (t == 0) *lcnt = 0u;
      bar();
      v4u v = snapu; int spins = 0; bool ok = false;
      for (;;) {
        CBAR();
        if (!ok) v = ld16(rs, suoff);
        ok = (v.y != snapu.y) & (v.w != snapu.w);
        if (__all(ok) || ++spins > SPIN_CAP) break;
      }
      v2u hh = {v.x, v.z};
      *(LAS v2u*)(hp + ((t >> 4) & 7) * 272 + (t >> 7) * 128 + (t & 15) * 8) = hh;
      STAMP(1);
      bar();
      STAMP(2);
#pragma unroll
      for (int i = 0; i < 4; i++) wk[i] = *(const LAS bf16x8*)(WKI + (wave * 16 + n) * 256 + (((4 * i + q) ^ swz(n)) * 16));
      if (wave < 2) {
        spins = 0; ok = false;
        for (;;) {
          CBAR();
          if (!ok) v = ld16(rs, sqoff);
          ok = (v.y != snapq.y) & (v.w != snapq.w);
          if (__all(ok) || ++spins > SPIN_CAP) break;
        }
        v2u o2 = {v.x, v.z};
        *(LAS v2u*)(qst + (t >> 5) * 272 + (t & 31) * 8) = o2;
      } else if (wave == 2) {
        spins = 0; ok = false;
        for (;;) { CBAR(); if (!ok) ok = ld8(rs, sfoff).y != snapf; if (__all(ok) || ++spins > SPIN_CAP) break; }
      }
      bar();
    } else {
      const int lw = wave - 4;
      const int sqo = SQ_OFF + head * 2048 + cs * 512 + lw * 128 + ((lane >> 2) & 7) * 16;
      const v4u oldq = ld16(rs, sqo);
      bar();
#pragma unroll
      for (int i = 0; i < 16; i++) {
        const int j = 4 * i + lw, rg = j & 31, row = 4 * rg + q;
        const u16* src = (i < 8 ? w_q : w_uk) + (size_t)(cs * 128 + row) * 4096 + head * 128 + ((n ^ swz(row)) * 8);
        dma16(src, L + 65536 + j * 1024);
      }
      VMWAIT0();
      if (lane == 0) __hip_atomic_fetch_add(lcnt, 1u, __ATOMIC_RELAXED, __HIP_MEMORY_SCOPE_WORKGROUP);
      { int spins = 0; while (__hip_atomic_load(lcnt, __ATOMIC_RELAXED, __HIP_MEMORY_SCOPE_WORKGROUP) < 4u && ++spins < SPIN_CAP) {} }
      CBAR();
      bf16x8 wqf[8];
#pragma unroll
      for (int i = 0; i < 8; i++) wqf[i] = trfrag<256, true>(WQI, 32 * (i & 3), 4 * lw + 2 * (i >> 2), lane);
#pragma unroll
      for (int i = 0; i < 4; i++) wk[i] = *(const LAS bf16x8*)(WKI + (wave * 16 + n) * 256 + (((4 * i + q) ^ swz(n)) * 16));
      bar();
      f32x4 a0 = {0, 0, 0, 0}, a1 = {0, 0, 0, 0}; bf16x8 hf[4];
#pragma unroll
      for (int i = 0; i < 4; i++) hf[i] = *(const LAS bf16x8*)(hp + (n & 7) * 272 + i * 64 + q * 16);
      SB();
#pragma unroll
      for (int i = 0; i < 4; i++) { a0 = MFMA(hf[i], wqf[i], a0); a1 = MFMA(hf[i], wqf[4 + i], a1); }
      const float sq = red16((a0[0] + a0[1]) + (a0[2] + a0[3]), (a1[0] + a1[1]) + (a1[2] + a1[3]));
      const unsigned d = pk2(sq, dppf<0x101>(sq));
      const unsigned d2 = dppu<0x102>(d);
      if (lane < 32 && (lane & 3) == 0) { v4u o = {d, oldq.y + 1u, d2, oldq.w + 1u}; st16(rs, sqo, o); }
      {
        const int cb = S3_OFF + (head * 32 + cs * 8) * S3R + (t - 256) * 16;
        stclr(rs, cb, sv4); stclr(rs, cb + 4096, sv4);
        if (wave == 4) stclr(rs, cb + 8192, sv4);
        else if (wave == 5 && lane < 8) stclr(rs, S4_OFF + head * 512 + cs * 128 + lane * 16, sv4);
      }
      bar();
      if (lane < 32 && (lane & 3) == 0) { v4u o = {d, oldq.y + 2u, d2, oldq.w + 2u}; st16(rs, sqo, o); }
    }
    STAMP(13);
    {
      f32x4 a = {0, 0, 0, 0}; bf16x8 qf[4];
#pragma unroll
      for (int i = 0; i < 4; i++) qf[i] = *(const LAS bf16x8*)(qst + (n & 3) * 272 + i * 64 + q * 16);
      SB();
      f32x4 ab = {0, 0, 0, 0};
      a = MFMA(wk[0], qf[0], a); ab = MFMA(wk[1], qf[1], ab); a = MFMA(wk[2], qf[2], a); ab = MFMA(wk[3], qf[3], ab);
      a += ab;
#pragma unroll
      for (int i = 0; i < 4; i++) { a[i] += dppf<0xB1>(a[i]); a[i] += dppf<0x4E>(a[i]); }
      const float SC = 0.08838834764831845f * 1.4426950408889634f;
      v2u o = {canon(pk2(a[0] * SC, a[1] * SC)), canon(pk2(a[2] * SC, a[3] * SC))};
      if (n == 0) *(LAS v2u*)(q16 + 64 + wave * 8 + q * 2) = o;
    }
    if (wave >= 4) VMWAIT0();
    bar();
    if (wave < 2) { const v4u o = *(const LAS v4u*)(q16 + 64 + n * 4); st16(rs, S2_OFF + (wave * 4 + q) * 32768 + head * 1024 + cs * 256 + n * 16, o); }
    STAMP(3);
    bar();
    {
      const int kj = r & 7, cg = xc * 2 + (r >> 3);
      const u16* pl = w_mod + (size_t)(kj * 64 + (t >> 4)) * 3072 + 1024 + cg * 128 + n * 8;
      v4u w0 = LDNT(v4u, pl), w1r = LDNT(v4u, pl + (size_t)32 * 3072);
      CBAR();
      if (r < 8) {
#pragma unroll
        for (int i = 0; i < 8; i++) {
          const int rg = 8 * i + wave, row = 8 * rg + (lane >> 3);
          glds16(w_o + (size_t)(xc * 512 + row) * 512 + r * 64 + (((lane & 7) ^ ((row & 2) | ((row >> 1) & 4))) * 8), (unsigned)(rg * 1024));
        }
        asm volatile("s_waitcnt vmcnt(8)" ::: "memory");
      }
      const float c0 = bflo(cvl0), c1 = bflo(cvl1);
      const unsigned s01 = pk2(c0 * __builtin_amdgcn_rcpf(1.f + __expf(-c0)), c1 * __builtin_amdgcn_rcpf(1.f + __expf(-c1)));
      interleave(w0, w1r);
      float a[8] = {dot2(w0.x, s01, 0.f), dot2(w0.y, s01, 0.f), dot2(w0.z, s01, 0.f), dot2(w0.w, s01, 0.f),
                    dot2(w1r.x, s01, 0.f), dot2(w1r.y, s01, 0.f), dot2(w1r.z, s01, 0.f), dot2(w1r.w, s01, 0.f)};
      float b0 = red32(a[0], a[4]), b1x = red32(a[1], a[5]), b2x = red32(a[2], a[6]), b3 = red32(a[3], a[7]);
      f32x2 o = {red16(b0, b2x), red16(b1x, b3)};
      LAS float* apart = (LAS float*)(SM + 12288);
      *(LAS f32x2*)(apart + wave * 128 + n * 8 + 4 * (lane >> 5) + 2 * ((lane >> 4) & 1)) = o;
      bar();
      if (wave == 7) {
        f32x2 sacc = {0.f, 0.f};
#pragma unroll
        for (int w = 0; w < 8; w++) sacc += *(LAS f32x2*)(apart + w * 128 + lane * 2);
        const unsigned d = canon(pk2(sacc.x, sacc.y));
        v4u o4 = {dppu<0x00>(d), dppu<0x55>(d), dppu<0xAA>(d), dppu<0xFF>(d)};
        if ((lane & 3) == 0) st16(rs, SL_OFF + kj * 4096 + cg * 256 + lane * 4, o4);
      }
      bar();
    }
    if (r < 8) {
      LAS float* fpart = (LAS float*)(SM + 3072);
      LAS unsigned char* cst = SM + 4096;
      VMWAIT0();
      bar();
      bf16x8 wof[8];
      {
        const int g = lane >> 4, qq = (lane & 15) >> 2, pp = lane & 3;
#pragma unroll
        for (int i = 0; i < 8; i++) {
          s16x4 h[2];
#pragma unroll
          for (int tt = 0; tt < 2; tt++) {
            const int row = 32 * ((wave >> 2) * 8 + i) + 8 * g + 4 * tt + qq;
            const int ch = (2 * (wave & 3) + (pp >> 1)) ^ ((row & 2) | ((row >> 1) & 4));
            h[tt] = __builtin_amdgcn_ds_read_tr16_b64_v4i16((LAS s16x4*)(L + row * 128 + ch * 16 + (pp & 1) * 8));
          }
          wof[i] = (bf16x8){h[0].x, h[0].y, h[0].z, h[0].w, h[1].x, h[1].y, h[1].z, h[1].w};
        }
      }
      if (wave >= 4) {
        int spins = 0;
        for (;;) { CBAR(); v4u v = ld16(rs, S2_OFF + xc * 32768 + xc * 4096 + (t - 256) * 16); if (__all(mx4(v) != SENT) || ++spins > SPIN_CAP) break; __builtin_amdgcn_s_sleep(2); }
      }
      bar();
      STAMP(4);
      if (wave < 2) {
        v4u v; int spins = 0;
        for (;;) { CBAR(); v = ld16(rs, S4_OFF + xc * 2048 + t * 16); if (__all(mx4(v) != SENT) || ++spins > SPIN_CAP) break; }
        *(LAS v4u*)(cst + t * 16) = v;
      }
      bar();
      STAMP(10);
      {
        const LAS unsigned char* ap = cst + ((2 * (wave >> 2)) * 2 + (n & 1)) * 256 + q * 16;
        f32x4 a = {0, 0, 0, 0}, a2 = {0, 0, 0, 0};
        bf16x8 cf[8];
#pragma unroll
        for (int i = 0; i < 8; i++) cf[i] = *(const LAS bf16x8*)(ap + (i >> 2) * 512 + (i & 3) * 64);
        SB();
#pragma unroll
        for (int i = 0; i < 4; i++) { a = MFMA(cf[2 * i], wof[2 * i], a); a2 = MFMA(cf[2 * i + 1], wof[2 * i + 1], a2); }
        const float v = (a[0] + a[1]) + (a2[0] + a2[1]);
        if (lane < 16) fpart[wave * 16 + lane] = v;
        bar();
        if (wave == 0) {
          const f32x4 l0 = *(const LAS f32x4*)(fpart + (lane & 7) * 8), l1 = *(const LAS f32x4*)(fpart + (lane & 7) * 8 + 4);
          const f32x4 h0 = *(const LAS f32x4*)(fpart + 64 + (lane & 7) * 8), h1 = *(const LAS f32x4*)(fpart + 64 + (lane & 7) * 8 + 4);
          v4u o = {canon(pk2(l0[0] + h0[0], l0[1] + h0[1])), canon(pk2(l0[2] + h0[2], l0[3] + h0[3])), canon(pk2(l1[0] + h1[0], l1[1] + h1[1])), canon(pk2(l1[2] + h1[2], l1[3] + h1[3]))};
          st16(rs, S5_OFF + (lane >> 3) * 8192 + xc * 1024 + r * 128 + (lane & 7) * 16, o);
        }
      }
      STAMP(11);
    } else {
      const int ti = xc * 8 + (r - 8);
      LAS unsigned char* W2I = L;
      const float b2v = bflo(b2[ti * 8 + (lane & 7)]);
      const float x8 = bflo(x[ti * 8 + (lane & 7)]);
      CBAR();
#pragma unroll
      for (int i = 0; i < 3; i++) { const int rg = 8 * i + wave; if (rg < 22) { int row = rg * 64 + lane; row = row < 1376 ? row : 1375; dma16n(w2 + (size_t)row * 512 + ti * 8, W2I + rg * 1024); } }
      CBAR();
      LAS float* hpart = (LAS float*)(SM + 2048);
      LAS unsigned* sbuf = (LAS unsigned*)(SM + 2560);
      VMWAIT0();
      bar();
      STAMP(5);
      bf16x8 w2f[6];
#pragma unroll
      for (int i = 0; i < 6; i++) {
        const int ks = wave + 8 * i < 43 ? wave + 8 * i : 0;
        const int g = lane >> 4, qq = (lane & 15) >> 2, pp = lane & 3;
        s16x4 h0 = __builtin_amdgcn_ds_read_tr16_b64_v4i16((LAS s16x4*)(W2I + (32 * ks + 8 * g + qq) * 16 + (pp & 1) * 8));
        s16x4 h1 = __builtin_amdgcn_ds_read_tr16_b64_v4i16((LAS s16x4*)(W2I + (32 * ks + 8 * g + 4 + qq) * 16 + (pp & 1) * 8));
        w2f[i] = (bf16x8){h0.x, h0.y, h0.z, h0.w, h1.x, h1.y, h1.z, h1.w};
      }
      float xn8 = 0.f, gbv = 0.f;
      if (wave == 0) {
        const int off = lane < 8 ? S5_OFF + xc * 8192 + lane * 1024 + ti * 16 : SL_OFF + (lane & 7) * 4096 + ((lane & 16) ? 3072 : 0) + ti * 16;
        v4u v; int spins = 0;
        for (;;) { CBAR(); v = ld16(rs, off); if (__all(mx4(v) != SENT) || ++spins > SPIN_CAP) break; __builtin_amdgcn_s_sleep(4); }
        if (lane < 24) *(LAS v4u*)(sbuf + lane * 4) = v;
        LGWAIT0();
        const LAS u16* sh = (const LAS u16*)sbuf;
        float ao = 0, ga8 = 0;
#pragma unroll
        for (int k = 0; k < 8; k++) { ao += bflo(sh[k * 8 + (lane & 7)]); ga8 += bflo(sh[64 + k * 8 + (lane & 7)]); gbv += bflo(sh[128 + k * 8 + (lane & 7)]); }
        ga8 = rbf(ga8); gbv = rbf(gbv);
        xn8 = rbf(x8 + rbf(ga8 * rbf(ao)));
      }
      bar();
      STAMP(6);
      {
        v4u hv[6]; int spins = 0;
        const int off = S6_OFF + xc * 6144 + wave * 128 + q * 16, off5 = wave < 3 ? off + 5120 : off;
        for (;;) {
          CBAR();
#pragma unroll
          for (int i = 0; i < 5; i++) hv[i] = ld16(rs, off + i * 1024);
          hv[5] = ld16(rs, off5);
          const unsigned m = max(max(max(mx4(hv[0]), mx4(hv[1])), max(mx4(hv[2]), mx4(hv[3]))), max(mx4(hv[4]), mx4(hv[5])));
          if (__all(m != SENT) || ++spins > SPIN_CAP) break;
          POLLSLEEP();
        }
        if (wave >= 3) hv[5] = (v4u){0, 0, 0, 0};
        STAMP(8);
        f32x4 a = {0, 0, 0, 0}, a2 = {0, 0, 0, 0};
#pragma unroll
        for (int i = 0; i < 3; i++) { a = MFMA(__builtin_bit_cast(bf16x8, hv[2 * i]), w2f[2 * i], a); a2 = MFMA(__builtin_bit_cast(bf16x8, hv[2 * i + 1]), w2f[2 * i + 1], a2); }
        if (lane < 8) hpart[wave * 8 + lane] = a[0] + a2[0];
      }
      bar();
      if (wave == 0) {
        float a = hpart[lane];
        a += dppf<0x128>(a); a = red16(a, a); a = red32(a, a);
        const float o = rbf(rbf(a) + b2v);
        const float res = xn8 + rbf(gbv * o);
        if (lane < 8) out[ti * 8 + lane] = __builtin_bit_cast(u16, (__bf16)res);
      }
      STAMP(9);
    }
    STAMP_FLUSH();
  } else if (r >= 24) {
    const int e = r - 24, head = 4 * xc + (e >> 1), half = e & 1, ti = xc * 8 + e;
    const int gi = e * 8 + xc;
    const int wst = (gi < 43 ? gi : 0) * 4;
    const int mcol = wave * 64 + 8 * (lane & 7) + 4 * (lane >> 5) + 2 * ((lane >> 4) & 1) + ((lane >> 3) & 1);
    LAS unsigned char* W1I = L + 65536;
    LAS unsigned char* STG = L + 98304;
    LAS u16* scl = (LAS u16*)(SM);
    LAS unsigned* cc16 = (LAS unsigned*)(SM + 1024);
    LAS unsigned* ml = (LAS unsigned*)(SM + 2048);
    LAS float* stat = (LAS float*)(SM + 5120);
    LAS u16* y16 = (LAS u16*)(SM + 5376);
    LAS float* gpart = (LAS float*)(SM + 6400);
    LAS unsigned* pub = (LAS unsigned*)(SM + 7424);
    LAS unsigned* cnt = (LAS unsigned*)(SM + 7680);
    LAS unsigned* tbase = (LAS unsigned*)(SM + 8192);
    LAS float* apart = (LAS float*)(SM + 10240);
    LAS float* fpart = (LAS float*)(SM + 14336);
    LAS unsigned char* cst = SM + 15360;
    const int puoff = SU_OFF + q * 16384 + (xc * 8 + e) * 256 + n * 16;
    const int pfoff = SF_OFF + n * 8192 + ti * 128;
    const int sfoff = SF_OFF + (8 + xc) * 8192 + lane * 128;
#define LDSADD(p) __hip_atomic_fetch_add((p), 1u, __ATOMIC_RELAXED, __HIP_MEMORY_SCOPE_WORKGROUP)
#define REPUB() do { v2u p2_ = *(LAS v2u*)(pub + n * 2); v4u tb_ = *(LAS v4u*)(tbase + lane * 8); unsigned tf_ = tbase[lane * 8 + 4]; unsigned k_ = 0; if (lane == 0) k_ = LDSADD(cnt); k_ = __builtin_amdgcn_readfirstlane(k_); \
      v4u o_ = {p2_.x, tb_.x + k_, p2_.y, tb_.y + k_}; st16(rs, puoff, o_); v4u o2_ = {p2_.x, tb_.z + k_, p2_.y, tb_.w + k_}; st16(rs, puoff + 65536, o2_); \
      if (lane < 16) { v2u f_ = {0u, tf_ + k_}; st8(rs, pfoff, f_); } } while (0)
    v2u pd = {0, 0};
    v4u old0 = {0, 0, 0, 0}, old1 = {0, 0, 0, 0}, xv = {0, 0, 0, 0}; unsigned snapf = 0, xw = 0;
    const unsigned oldf = ld8(rs, pfoff).y;
    if (wave == 0) { old0 = ld16(rs, puoff); old1 = ld16(rs, puoff + 65536); xv = *(const v4u*)(x + lane * 8); xw = ((const unsigned*)x)[xc * 32 + (lane & 31)]; }
    if (wave == 1) snapf = ld8(rs, sfoff).y;
    CBAR();
    v4u wm[2]; unsigned short cv[2];
    {
      const u16* p = w_mod + (size_t)(e * 64 + (t >> 4)) * 3072 + (n >> 3) * 512 + xc * 64 + (n & 7) * 8;
      wm[0] = LDNT(v4u, p); wm[1] = LDNT(v4u, p + (size_t)32 * 3072);
      cv[0] = cvec[e * 64 + (t >> 4)]; cv[1] = cvec[e * 64 + 32 + (t >> 4)];
    }
    CBAR();
    {
      const int pc = t >= 64 ? t - 64 : t;
      st16(rs, wave == 0 ? DUMMY_OFF + ti * 1024 + lane * 16 : S2_OFF + ti * (EGC_PIECES * 16) + (pc < EGC_PIECES ? pc : pc - EGC_PIECES) * 16, sv4);
    }
    CBAR();
    float ln0 = 0.f, ln1 = 0.f;
    if (wave == 0) {
      float xs[8] = {bflo(xv.x), bfhi(xv.x), bflo(xv.y), bfhi(xv.y), bflo(xv.z), bfhi(xv.z), bflo(xv.w), bfhi(xv.w)};
      float s = 0;
#pragma unroll
      for (int i = 0; i < 8; i++) s += xs[i];
      const float mean = wave_sum(s) * (1.f / 512.f);
      float vs = 0;
#pragma unroll
      for (int i = 0; i < 8; i++) { float d = xs[i] - mean; vs += d * d; }
      const float rstd = rsqrtf(wave_sum(vs) * (1.f / 512.f) + 1e-5f);
      ln0 = rbf((bflo(xw) - mean) * rstd); ln1 = rbf((bfhi(xw) - mean) * rstd);
      v4u tb = {old0.y, old0.w, old1.y, old1.w};
      *(LAS v4u*)(tbase + lane * 8) = tb; tbase[lane * 8 + 4] = oldf;
      if (lane == 0) { cnt[0] = 2u + NREP; cnt[1] = 0u; }
    }
    CBAR();
    {
      const float c0 = bflo(cv[0]), c1 = bflo(cv[1]);
      const unsigned s01 = pk2(c0 * __builtin_amdgcn_rcpf(1.f + __expf(-c0)), c1 * __builtin_amdgcn_rcpf(1.f + __expf(-c1)));
      interleave(wm[0], wm[1]);
      float a[8] = {dot2(wm[0].x, s01, 0.f), dot2(wm[0].y, s01, 0.f), dot2(wm[0].z, s01, 0.f), dot2(wm[0].w, s01, 0.f),
                    dot2(wm[1].x, s01, 0.f), dot2(wm[1].y, s01, 0.f), dot2(wm[1].z, s01, 0.f), dot2(wm[1].w, s01, 0.f)};
      float b0 = red32(a[0], a[4]), b1x = red32(a[1], a[5]), b2x = red32(a[2], a[6]), b3 = red32(a[3], a[7]);
      f32x2 o = {red16(b0, b2x), red16(b1x, b3)};
      *(LAS f32x2*)(apart + wave * 128 + n * 8 + 4 * (lane >> 5) + 2 * ((lane >> 4) & 1)) = o;
    }
    bar();
    STAMP(8);
    if (wave == 0) {
      f32x2 sacc = {0.f, 0.f};
#pragma unroll
      for (int w = 0; w < 8; w++) sacc += *(LAS f32x2*)(apart + w * 128 + lane * 2);
      auto r0 = __builtin_amdgcn_permlane32_swap(__float_as_uint(sacc.x), __float_as_uint(sacc.x), false, false);
      auto r1 = __builtin_amdgcn_permlane32_swap(__float_as_uint(sacc.y), __float_as_uint(sacc.y), false, false);
      const float one = e == 0 ? 1.f : 0.f;
      const float u0 = ln0 * (one + __uint_as_float(r0[1])) + __uint_as_float(r0[0]), u1 = ln1 * (one + __uint_as_float(r1[1])) + __uint_as_float(r1[0]);
      if (lane < 32) pub[lane] = pk2(u0, u1);
      LGWAIT0();
      pd = *(LAS v2u*)(pub + n * 2);
      v4u o = {pd.x, old0.y + 1u, pd.y, old0.w + 1u}; st16(rs, puoff, o);
      v4u o2 = {pd.x, old1.y + 1u, pd.y, old1.w + 1u}; st16(rs, puoff + 65536, o2);
    }
    STAMP(9);
    unsigned karr = 0;
    if (wave != 0) { VMWAIT0(); if (lane == 0) karr = LDSADD(cnt + 1); }
    const bool lastw = __builtin_amdgcn_readfirstlane(karr) == 6u;
    if (lastw && lane < 16) { v2u f = {0u, oldf + 1u}; st8(rs, pfoff, f); }
    STAMP(10);
#pragma unroll
    for (int i = 0; i < 4; i++) if (gi < 43) { const int rg = 8 * i + wave; dma16n(w1 + (size_t)(rg * 16 + (lane >> 2)) * 1376 + wst * 8 + (lane & 3) * 8, W1I + rg * 1024); }
    CBAR();
    DELAY(EGL_DELAY);
    const float xme = bflo(x[mcol]);
    const float b1v = bflo(b1[wst * 8 + (lane & 31)]);
    CBAR();
#pragma unroll
    for (int i = 0; i < 8; i++) {
      const int rg = 8 * i + wave, row = 4 * rg + q;
      dma16(w_uv + (size_t)(half * 256 + row) * 4096 + head * 128 + ((n ^ swz(row)) * 8), L + rg * 1024);
    }
    CBAR();
    if (wave == 0 || lastw) {
#pragma unroll 1
      for (int k = 0; k < NREP; k++) {
        __builtin_amdgcn_s_sleep(14);
        if (wave == 0) { v4u o_ = {pd.x, old0.y + 2u + k, pd.y, old0.w + 2u + k}; st16(rs, puoff, o_); v4u o2_ = {pd.x, old1.y + 2u + k, pd.y, old1.w + 2u + k}; st16(rs, puoff + 65536, o2_); }
        if (lastw && lane < 16) { v2u f_ = {0u, oldf + 2u + k}; st8(rs, pfoff, f_); }
      }
    }
    VMWAIT0();
    bar();
    STAMP(1);
    bf16x8 wvf[8], w1f[4];
#pragma unroll
    for (int s2 = 0; s2 < 8; s2++) wvf[s2] = trfrag<256, true>(L, 32 * wave, 2 * s2, lane);
#pragma unroll
    for (int i = 0; i < 4; i++) w1f[i] = trfrag<64, false>(W1I, 64 * wave + 32 * (i >> 1), 2 * (i & 1), lane);
    if (wave == 1) {
      int spins = 0; bool ok = false;
      for (;;) { CBAR(); if (!ok) ok = ld8(rs, sfoff).y != snapf; if (__all(ok) || ++spins > SPIN_CAP) break; if ((spins & 3) == 0) REPUB(); __builtin_amdgcn_s_sleep(2); }
      spins = 0;
      for (;;) { CBAR(); v4u v = ld16(rs, S2_OFF + xc * 32768 + head * 1024 + lane * 16); if (__all(mx4(v) != SENT) || ++spins > SPIN_CAP) break; if ((spins & 3) == 0) REPUB(); __builtin_amdgcn_s_sleep(2); }
    }
    bar();
    STAMP(2);
    {
      LAS float* sclf = (LAS float*)(SM);
      LAS float* epart = (LAS float*)(STG);
      float sc8[8];
      {
        const int soff = S3_OFF + (head * 32 + (lane & 31)) * S3R + 1024;
        if (wave == 7) {
          v2u stt; int spins = 0;
          for (;;) { CBAR(); stt = ld8(rs, soff); if (__all(max(stt.x, stt.y) != SENT) || ++spins > SPIN_CAP) break; if ((spins & 3) == 0) REPUB(); }
          const float m = __uint_as_float(stt.x), l = __uint_as_float(stt.y);
          const float mg = wave_max(m);
          const float sc = __builtin_amdgcn_exp2f(m - mg);
          const float il = 2.f * __builtin_amdgcn_rcpf(wave_sum(sc * l));
          if (lane < 32) sclf[lane] = sc * il;
        }
      }
      v4u p0, p1; int spins = 0;
      const int off = S3_OFF + (head * 32 + n) * S3R + half * 512 + wave * 64 + q * 16;
      for (;;) {
        CBAR();
        p0 = ld16(rs, off); p1 = ld16(rs, off + 16 * S3R);
        if (__all(max(mx4(p0), mx4(p1)) != SENT) || ++spins > SPIN_CAP) break;
        if ((spins & 15) == 0) REPUB();
      }
      STAMP(3);
      f32x4 y0[8], y1[8];
#pragma unroll
      for (int T = 0; T < 8; T++) {
        y0[T] = MFMA(__builtin_bit_cast(bf16x8, p0), wvf[T], ((f32x4){0, 0, 0, 0}));
        y1[T] = MFMA(__builtin_bit_cast(bf16x8, p1), wvf[T], ((f32x4){0, 0, 0, 0}));
      }
      bar();
      { const f32x4 a = *(const LAS f32x4*)(sclf + 4 * q), b = *(const LAS f32x4*)(sclf + 16 + 4 * q); sc8[0] = a[0]; sc8[1] = a[1]; sc8[2] = a[2]; sc8[3] = a[3]; sc8[4] = b[0]; sc8[5] = b[1]; sc8[6] = b[2]; sc8[7] = b[3]; }
      float z[8];
#pragma unroll
      for (int T = 0; T < 8; T++)
        z[T] = ((y0[T][0] * sc8[0] + y0[T][1] * sc8[1]) + (y0[T][2] * sc8[2] + y0[T][3] * sc8[3])) + ((y1[T][0] * sc8[4] + y1[T][1] * sc8[5]) + (y1[T][2] * sc8[6] + y1[T][3] * sc8[7]));
      const float r0 = red32(z[0], z[4]), r1 = red32(z[1], z[5]), r2 = red32(z[2], z[6]), r3 = red32(z[3], z[7]);
      const float u0 = red16(r0, r2), u1 = red16(r1, r3);
      const int T0 = 4 * (lane >> 5) + 2 * ((lane >> 4) & 1);
      epart[wave * 128 + T0 * 16 + n] = u0; epart[wave * 128 + T0 * 16 + 16 + n] = u1;
      bar();
      if (wave == 0) {
        f32x2 sacc = {0.f, 0.f};
#pragma unroll
        for (int w = 0; w < 8; w++) sacc += *(LAS f32x2*)(epart + w * 128 + lane * 2);
        const unsigned d = canon(pk2(sacc.x, sacc.y));
        v4u o4 = {dppu<0x00>(d), dppu<0x55>(d), dppu<0xAA>(d), dppu<0xFF>(d)};
        if ((lane & 3) == 0) st16(rs, S4_OFF + (head * 2 + half) * 256 + lane * 4, o4);
      }
    }
    STAMP(4);
    if (gi >= 43) {
      if (wave == 0) {
        int spins = 0;
        for (;;) {
          CBAR(); v4u v = ld16(rs, DN_OFF + xc * 128);
          if (v.x != SENT || ++spins > SPIN_CAP) break;
          if ((spins & 3) == 0) REPUB();
          __builtin_amdgcn_s_sleep(8);
        }
      }
      STAMP_FLUSH(); return;
    }
    {
      v4u v0, v1, v2; int spins = 0;
      const int off = SL_OFF + wave * 4096 + lane * 16;
      for (;;) { CBAR(); v0 = ld16(rs, off); v1 = ld16(rs, off + 1024); v2 = ld16(rs, off + 2048); if (__all(max(max(mx4(v0), mx4(v1)), mx4(v2)) != SENT) || ++spins > SPIN_CAP) break; if ((spins & 15) == 0) REPUB(); __builtin_amdgcn_s_sleep(1); }
      *(LAS v4u*)(L + wave * 3072 + lane * 16) = v0; *(LAS v4u*)(L + wave * 3072 + 1024 + lane * 16) = v1; *(LAS v4u*)(L + wave * 3072 + 2048 + lane * 16) = v2;
    }
    bar();
    float gam = 0.f, sbm = 0.f, cb1 = 0.f;
    {
      const LAS u16* mlh = (const LAS u16*)L;
#pragma unroll
      for (int k = 0; k < 8; k++) { gam += bflo(mlh[k * 1536 + mcol]); sbm += bflo(mlh[k * 1536 + 512 + mcol]); cb1 += bflo(mlh[k * 1536 + 1024 + mcol]); }
      gam = rbf(gam); sbm = rbf(sbm); cb1 = rbf(1.f + rbf(cb1));
    }
    asm volatile("" : "+v"(gam), "+v"(sbm), "+v"(cb1));
    STAMP(5);
    float xnew;
    {
      v4u v; int spins = 0;
      const int off = S5_OFF + xc * 8192 + (lane >> 3) * 1024 + wave * 128 + (lane & 7) * 16;
      for (;;) { CBAR(); v = ld16(rs, off); if (__all(mx4(v) != SENT) || ++spins > SPIN_CAP) break; if ((spins & 15) == 0) REPUB(); }
      STAMP(6);
      const float b0 = red32(bflo(v.x), bflo(v.z)), b1x = red32(bfhi(v.x), bfhi(v.z)), b2x = red32(bflo(v.y), bflo(v.w)), b3 = red32(bfhi(v.y), bfhi(v.w));
      const float c0 = red16(b0, b2x), c1 = red16(b1x, b3);
      const bool up = lane & 8;
      const float keep = up ? c1 : c0, send = up ? c0 : c1;
      const float ao = keep + dppf<0x128>(send);
      xnew = rbf(xme + rbf(gam * rbf(ao)));
      const float sv = wave_sum2(xnew, xnew * xnew);
      if ((lane & 47) == 0) stat[wave * 2 + (lane >> 4)] = sv;
    }
    bar();
    float h2;
    {
      float v = stat[n];
      v += dppf<0x128>(v); v += dppf<0x124>(v); v += dppf<0x122>(v);
      const float o = dppf<0xB1>(v);
      const float s1 = (lane & 1) ? o : v, s2 = (lane & 1) ? v : o;
      const float mean = s1 * (1.f / 512.f), var = fmaxf(s2 * (1.f / 512.f) - mean * mean, 0.f);
      const float rstd2 = __builtin_amdgcn_rsqf(var + 1e-5f);
      h2 = rbf(rbf(rbf((xnew - mean) * rstd2) * cb1) + sbm);
      const float w = wave_sum2(h2, h2 * h2);
      if ((lane & 47) == 0) stat[16 + wave * 2 + (lane >> 4)] = w;
    }
    bar();
    {
      float v = stat[16 + n];
      v += dppf<0x128>(v); v += dppf<0x124>(v); v += dppf<0x122>(v);
      const float o = dppf<0xB1>(v);
      const float s1 = (lane & 1) ? o : v, s2 = (lane & 1) ? v : o;
      const float mean = s1 * (1.f / 512.f), var = fmaxf(s2 * (1.f / 512.f) - mean * mean, 0.f);
      const float rstd2 = __builtin_amdgcn_rsqf(var + 1e-5f);
      y16[mcol] = __builtin_bit_cast(u16, (__bf16)((h2 - mean) * rstd2));
      LGWAIT0();
      const bf16x8 y0 = *(const LAS bf16x8*)(y16 + wave * 64 + q * 8), y1 = *(const LAS bf16x8*)(y16 + wave * 64 + 32 + q * 8);
      f32x4 z0 = MFMA(y0, w1f[0], ((f32x4){0, 0, 0, 0})), z1 = MFMA(y0, w1f[1], ((f32x4){0, 0, 0, 0}));
      z0 = MFMA(y1, w1f[2], z0); z1 = MFMA(y1, w1f[3], z1);
      if (lane < 32) gpart[wave * 32 + lane] = q == 0 ? z0[0] : z1[0];
    }
    bar();
    if (wave == 0) {
      float a = 0;
#pragma unroll
      for (int w = 0; w < 8; w++) a += gpart[w * 32 + (lane & 31)];
      const float z = rbf(rbf(a) + b1v);
      const float u = z * (0.7978845608028654f + 0.035677408136300125f * z * z);
      const float gl = z * __builtin_amdgcn_rcpf(1.f + __builtin_amdgcn_exp2f(-2.885390081777927f * u));
      const unsigned d = canon(pk2(gl, dppf<0x101>(gl)));
      if (lane < 32 && !(lane & 1)) ((LAS unsigned*)y16)[lane >> 1] = d;
      LGWAIT0();
      const v4u o = *(const LAS v4u*)((LAS unsigned*)y16 + (lane & 3) * 4);
      if (lane < 32) st16(rs, S6_OFF + (lane >> 2) * 6144 + gi * 128 + (lane & 3) * 16, o);
    }
    if (ti == 0 && wave == 1 && lane < 8) { v4u o = {1, 1, 1, 1}; st16(rs, DN_OFF + lane * 128, o); }
    STAMP(7);
    STAMP_FLUSH();
  } else {
    const int e = r - 16, split = 4 * xc + (e & 3), hg = e >> 2;
    const int sfoff = SF_OFF + (8 + xc) * 8192 + lane * 128;
    LAS unsigned char* qa = SM;
    LAS unsigned char* Pm = SM + 16896;
    LAS float* wmax = (LAS float*)(SM + 21504);
    LAS float* wsum = wmax + 128;
    unsigned snapf = 0;
    if (wave == 0) snapf = ld8(rs, sfoff).y;
    bar();
    CBAR();
#pragma unroll
    for (int i = 0; i < 16; i++) {
      const int key = ((i * 8 + wave) + hg * 64) & 127;
      dma16(ckv + (size_t)(split * 128 + key) * 512 + ((lane ^ swz(key)) * 8), L + key * 1024);
    }
    VMWAIT0();
    bar();
    STAMP(3);
    bf16x8 ka[16], va[16];
    {
      const int key = wave * 16 + n, f2 = swz(key);
#pragma unroll
      for (int s2 = 0; s2 < 16; s2++) ka[s2] = *(const LAS bf16x8*)(L + key * 1024 + (((4 * s2 + q) ^ f2) * 16));
#pragma unroll
      for (int i = 0; i < 16; i++) va[i] = trfrag<1024, true>(L, 32 * (i & 3), wave * 8 + 2 * (i >> 2), lane);
    }
    if (wave == 0) {
      int spins = 0; bool ok = false;
      for (;;) { CBAR(); if (!ok) ok = ld8(rs, sfoff).y != snapf; if (__all(ok) || ++spins > SPIN_CAP) break; __builtin_amdgcn_s_sleep(1); }
    }
    bar();
    STAMP(4);
    {
      const int off = S2_OFF + xc * 32768 + hg * 16384 + t * 16;
      v4u v0, v1; int spins = 0;
      for (;;) {
        CBAR();
        v0 = ld16(rs, off); v1 = ld16(rs, off + 8192);
        if (__all(max(mx4(v0), mx4(v1)) != SENT) || ++spins > SPIN_CAP) break;
      }
      *(LAS v4u*)(qa + wave * 1056 + lane * 16) = v0;
      *(LAS v4u*)(qa + (8 + wave) * 1056 + lane * 16) = v1;
    }
    bar();
    STAMP(5);
    f32x4 sc = {0, 0, 0, 0};
    {
      bf16x8 qf[16];
#pragma unroll
      for (int s2 = 0; s2 < 16; s2++) qf[s2] = *(const LAS bf16x8*)(qa + n * 1056 + s2 * 64 + q * 16);
      SB();
      f32x4 sc2 = {0, 0, 0, 0};
#pragma unroll
      for (int s2 = 0; s2 < 8; s2++) { sc = MFMA(ka[2 * s2], qf[2 * s2], sc); sc2 = MFMA(ka[2 * s2 + 1], qf[2 * s2 + 1], sc2); }
      sc += sc2;
    }
    float mx = fmaxf(fmaxf(sc[0], sc[1]), fmaxf(sc[2], sc[3]));
    mx = max16(mx); mx = max32(mx);
    if (lane < 16) wmax[lane * 8 + wave] = mx;
    bar();
    {
      f32x4 m0 = *(LAS f32x4*)(wmax + n * 8), m1 = *(LAS f32x4*)(wmax + n * 8 + 4);
      mx = fmaxf(fmaxf(fmaxf(m0[0], m0[1]), fmaxf(m0[2], m0[3])), fmaxf(fmaxf(m1[0], m1[1]), fmaxf(m1[2], m1[3])));
    }
    const float p0 = __builtin_amdgcn_exp2f(sc[0] - mx), p1 = __builtin_amdgcn_exp2f(sc[1] - mx), p2 = __builtin_amdgcn_exp2f(sc[2] - mx), p3 = __builtin_amdgcn_exp2f(sc[3] - mx);
    { v2u o = {pk2(p0, p1), pk2(p2, p3)}; *(LAS v2u*)(Pm + n * 288 + (wave * 16 + q * 4) * 2) = o; }
    float ps = (p0 + p1) + (p2 + p3);
    ps = red16(ps, ps); ps = red32(ps, ps);
    if (lane < 16) wsum[lane * 8 + wave] = ps;
    bar();
    {
      if (wave == 7) {
        f32x4 s0 = *(LAS f32x4*)(wsum + n * 8), s1 = *(LAS f32x4*)(wsum + n * 8 + 4);
        const float l = ((s0[0] + s0[1]) + (s0[2] + s0[3])) + ((s1[0] + s1[1]) + (s1[2] + s1[3]));
        v2u o2 = {canon(__float_as_uint(mx)), canon(__float_as_uint(l))};
        if (lane < 16) st8(rs, S3_OFF + ((hg * 16 + lane) * 32 + split) * S3R + 1024, o2);
      }
      bf16x8 bp[4];
#pragma unroll
      for (int ks = 0; ks < 4; ks++) bp[ks] = *(const LAS bf16x8*)(Pm + n * 288 + ks * 64 + q * 16);
      SB();
      f32x4 acc[4];
#pragma unroll
      for (int T = 0; T < 4; T++) {
        acc[T] = (f32x4){0, 0, 0, 0};
#pragma unroll
        for (int ks = 0; ks < 4; ks++) acc[T] = MFMA(va[T * 4 + ks], bp[ks], acc[T]);
      }
      unsigned d0[4], d1[4];
#pragma unroll
      for (int T = 0; T < 4; T++) { d0[T] = canon(pk2(acc[T][0], acc[T][1])); d1[T] = canon(pk2(acc[T][2], acc[T][3])); }
      auto r0 = __builtin_amdgcn_permlane16_swap(d0[0], d0[1], false, false);
      auto r1 = __builtin_amdgcn_permlane16_swap(d1[0], d1[1], false, false);
      auto r2 = __builtin_amdgcn_permlane16_swap(d0[2], d0[3], false, false);
      auto r3 = __builtin_amdgcn_permlane16_swap(d1[2], d1[3], false, false);
      v4u oa = {r0[0], r1[0], r0[1], r1[1]}, ob = {r2[0], r3[0], r2[1], r3[1]};
      const v4u obs = {dppu<0x128>(ob.x), dppu<0x128>(ob.y), dppu<0x128>(ob.z), dppu<0x128>(ob.w)};
      const bool lo = n < 8;
      const int o3b = S3_OFF + ((hg * 16 + (n & 7)) * 32 + split) * S3R + (wave * 64 + (q & 1) * 16 + (q & 2) * 4) * 2;
      st16(rs, o3b + (lo ? 0 : 64), lo ? oa : obs);
      st16(rs, o3b + 8 * 32 * S3R + (lo ? 64 : 0), lo ? obs : oa);
    }
    STAMP(6);
    STAMP_FLUSH();
  }
}

extern "C" void kernel_launch(void* const* d_in, const int* in_sizes, int n_in, void* d_out, int out_size, void* d_ws, size_t ws_size, hipStream_t stream) {
  (void)in_sizes; (void)n_in; (void)out_size; (void)ws_size;
  (void)hipFuncSetAttribute((const void*)fused_kernel, hipFuncAttributeMaxDynamicSharedMemorySize, L_TOTAL);
  fused_kernel<<<256, NT, L_TOTAL, stream>>>((const u16*)d_in[0], (const u16*)d_in[1], (const u16*)d_in[2], (const u16*)d_in[3], (const u16*)d_in[4], (const u16*)d_in[5],
                                             (const u16*)d_in[7], (const u16*)d_in[8], (const u16*)d_in[9], (const u16*)d_in[10], (const u16*)d_in[11], (const u16*)d_in[12],
                                             (u16*)d_out, (unsigned char*)d_ws);
}
```

```cpp
#include <hip/hip_runtime.h>
#include <stdint.h>

typedef unsigned short u16;
typedef unsigned v4u __attribute__((ext_vector_type(4)));
typedef unsigned v2u __attribute__((ext_vector_type(2)));
typedef float f32x4 __attribute__((ext_vector_type(4)));
typedef float f32x2 __attribute__((ext_vector_type(2)));
typedef short bf16x8 __attribute__((ext_vector_type(8)));
typedef short s16x4 __attribute__((ext_vector_type(4)));
typedef __bf16 bf2 __attribute__((ext_vector_type(2)));
#define LAS __attribute__((address_space(3)))

constexpr unsigned SENT = 0xFFFFFFFFu;
constexpr int NT = 512;
constexpr int SPIN_CAP = 1 << 20;
#define PSL 0
#define POLLSLEEP() do { if (PSL) __builtin_amdgcn_s_sleep(PSL); } while (0)
#define NREP 2
#define EGL_DELAY 400
#define ROFF 24
#define CLRAUX 16
#define NTAUX 2
#define NTS " nt"
#define LDNT(T, p) __builtin_nontemporal_load((const T*)(p))
constexpr int SU_OFF = 0;
constexpr int SF_OFF = 131072;
constexpr int SQ_OFF = 262144;
constexpr int CLR_OFF = 327680;
constexpr int S2_OFF = CLR_OFF;
constexpr int S5_OFF = S2_OFF + 8 * 32768;
constexpr int S6_OFF = S5_OFF + 65536;
constexpr int SL_OFF = S6_OFF + 49152;
constexpr int DN_OFF = SL_OFF + 32768;
constexpr int EGC_END = DN_OFF + 4096;
constexpr int EGC_PIECES = (EGC_END - S2_OFF) / 16 / 64;
constexpr int S4_OFF = EGC_END;
constexpr int S3R = 1152;
constexpr int S3_OFF = S4_OFF + 16384;
constexpr int CLR_END = S3_OFF + 1024 * S3R;
constexpr unsigned FLAG = 0x80000000u;
constexpr int DUMMY_OFF = CLR_END;
constexpr int WS_USED = CLR_END + 65536;
#define STAMP(i) do {} while (0)
#define STAMP_DECL do {} while (0)
#define STAMP_FLUSH() do {} while (0)

constexpr int L_CKV = 0;
constexpr int L_SM = 131072;
constexpr int L_TOTAL = 131072 + 30720;

__device__ __forceinline__ float bflo(unsigned w) { return __uint_as_float(w << 16); }
__device__ __forceinline__ float bfhi(unsigned w) { return __uint_as_float(w & 0xffff0000u); }
__device__ __forceinline__ unsigned pk2(float a, float b) { bf2 p; p.x = (__bf16)a; p.y = (__bf16)b; return __builtin_bit_cast(unsigned, p); }
__device__ __forceinline__ float rbf(float a) { return (float)(__bf16)a; }
__device__ __forceinline__ unsigned canon(unsigned w) { return w == SENT ? 0x7FC07FC0u : w; }
__device__ __forceinline__ float dot2(unsigned a, unsigned b, float c) { return __builtin_amdgcn_fdot2_f32_bf16(__builtin_bit_cast(bf2, a), __builtin_bit_cast(bf2, b), c, false); }
__device__ __forceinline__ void bar() { __builtin_amdgcn_sched_barrier(0); asm volatile("s_waitcnt lgkmcnt(0)\n\ts_barrier" ::: "memory"); __builtin_amdgcn_sched_barrier(0); }
template <int CTRL> __device__ __forceinline__ float dppf(float v) { return __int_as_float(__builtin_amdgcn_update_dpp(0, __float_as_int(v), CTRL, 0xf, 0xf, false)); }
template <int CTRL> __device__ __forceinline__ unsigned dppu(unsigned v) { return (unsigned)__builtin_amdgcn_update_dpp(0, (int)v, CTRL, 0xf, 0xf, false); }
__device__ __forceinline__ float red32(float a, float b) {
  auto r = __builtin_amdgcn_permlane32_swap(__float_as_uint(a), __float_as_uint(b), false, false);
  return __uint_as_float(r[0]) + __uint_as_float(r[1]);
}
__device__ __forceinline__ float red16(float a, float b) {
  auto r = __builtin_amdgcn_permlane16_swap(__float_as_uint(a), __float_as_uint(b), false, false);
  return __uint_as_float(r[0]) + __uint_as_float(r[1]);
}
__device__ __forceinline__ float max32(float a) { auto r = __builtin_amdgcn_permlane32_swap(__float_as_uint(a), __float_as_uint(a), false, false); return fmaxf(__uint_as_float(r[0]), __uint_as_float(r[1])); }
__device__ __forceinline__ float max16(float a) { auto r = __builtin_amdgcn_permlane16_swap(__float_as_uint(a), __float_as_uint(a), false, false); return fmaxf(__uint_as_float(r[0]), __uint_as_float(r[1])); }
__device__ __forceinline__ float wave_sum(float v) {
  v += dppf<0x128>(v); v += dppf<0x124>(v); v += dppf<0x122>(v); v += dppf<0x121>(v);
  v = red16(v, v); v = red32(v, v); return v;
}
__device__ __forceinline__ float wave_max(float v) {
  v = fmaxf(v, dppf<0x128>(v)); v = fmaxf(v, dppf<0x124>(v)); v = fmaxf(v, dppf<0x122>(v)); v = fmaxf(v, dppf<0x121>(v));
  v = max16(v); v = max32(v); return v;
}
__device__ __forceinline__ unsigned ilo(unsigned a, unsigned b) { return __builtin_amdgcn_perm(b, a, 0x05040100u); }
__device__ __forceinline__ unsigned ihi(unsigned a, unsigned b) { return __builtin_amdgcn_perm(b, a, 0x07060302u); }
__device__ __forceinline__ void interleave(v4u& a, v4u& b) {
  v4u x, y;
  x.x = ilo(a.x, b.x); x.y = ihi(a.x, b.x); x.z = ilo(a.y, b.y); x.w = ihi(a.y, b.y);
  y.x = ilo(a.z, b.z); y.y = ihi(a.z, b.z); y.z = ilo(a.w, b.w); y.w = ihi(a.w, b.w);
  a = x; b = y;
}
__device__ __forceinline__ bool valid4(v4u v) { return (v.x != SENT) & (v.y != SENT) & (v.z != SENT) & (v.w != SENT); }
typedef __amdgpu_buffer_rsrc_t rsrc_t;
__device__ __forceinline__ v4u ld16(rsrc_t r, int off) { return __builtin_amdgcn_raw_buffer_load_b128(r, off, 0, 16); }
__device__ __forceinline__ v2u ld8(rsrc_t r, int off) { return __builtin_amdgcn_raw_buffer_load_b64(r, off, 0, 16); }
__device__ __forceinline__ void st16(rsrc_t r, int off, v4u v) { __builtin_amdgcn_raw_buffer_store_b128(v, r, off, 0, 16); }
__device__ __forceinline__ void st8(rsrc_t r, int off, v2u v) { __builtin_amdgcn_raw_buffer_store_b64(v, r, off, 0, 16); }
__device__ __forceinline__ void glds16(const void* g, unsigned lds_base) {
  unsigned sv; asm volatile("s_mov_b32 %0, m0\n\ts_mov_b32 m0, %2\n\ts_nop 0\n\tglobal_load_lds_dwordx4 %1, off" NTS "\n\ts_mov_b32 m0, %0" : "=&s"(sv) : "v"(g), "s"(lds_base) : "memory");
}
#define RL(v, l) __int_as_float(__builtin_amdgcn_readlane(__float_as_int(v), (l)))
#define SB() __builtin_amdgcn_sched_barrier(0)
#define CBAR() do { asm volatile("" ::: "memory"); __builtin_amdgcn_sched_barrier(0); } while (0)

__device__ __forceinline__ int swz(int row) { return ((row & 3) | ((row >> 1) & 4)) << 1; }
template <int AUX> __device__ __forceinline__ void dma16a(const void* g, LAS unsigned char* l) { __builtin_amdgcn_global_load_lds((const __attribute__((address_space(1))) unsigned*)g, (LAS unsigned*)l, 16, 0, AUX); }
#define dma16 dma16a<NTAUX>
#define NARROW 2
#define dma16n dma16a<NARROW>
#define LDNAR(T, p) __builtin_nontemporal_load((const T*)(p))
template <int RB, bool SW>
__device__ __forceinline__ bf16x8 trfrag(const LAS unsigned char* img, int k0, int ch0, int lane) {
  const int g = lane >> 4, qq = (lane & 15) >> 2, pp = lane & 3;
  s16x4 h[2];
#pragma unroll
  for (int tt = 0; tt < 2; tt++) {
    const int row = k0 + 8 * g + 4 * tt + qq;
    const int ch = SW ? ((ch0 + (pp >> 1)) ^ swz(row)) : (ch0 + (pp >> 1));
    h[tt] = __builtin_amdgcn_ds_read_tr16_b64_v4i16((LAS s16x4*)(img + row * RB + ch * 16 + (pp & 1) * 8));
  }
  return (bf16x8){h[0].x, h[0].y, h[0].z, h[0].w, h[1].x, h[1].y, h[1].z, h[1].w};
}
#define MFMA(a, b, c) __builtin_amdgcn_mfma_f32_16x16x32_bf16(a, b, c, 0, 0, 0)
#define VMWAIT0() do { asm volatile("" ::: "memory"); __builtin_amdgcn_s_waitcnt(0x0F70); asm volatile("" ::: "memory"); } while (0)
#define LGWAIT0() asm volatile("s_waitcnt lgkmcnt(0)" ::: "memory")
__device__ __forceinline__ float wave_sum2(float a, float b) {
  a += dppf<0x128>(a); b += dppf<0x128>(b); a += dppf<0x124>(a); b += dppf<0x124>(b);
  a += dppf<0x122>(a); b += dppf<0x122>(b); a += dppf<0x121>(a); b += dppf<0x121>(b);
  float v = red16(a, b); return red32(v, v);
}

typedef unsigned long long u64;
__device__ __forceinline__ void ldm4(rsrc_t r, int off, unsigned s1, unsigned s2, unsigned s3, v4u& a, v4u& b, v4u& c, v4u& d, u64 m0, u64 m1, u64 m2, u64 m3) {
  u64 sv;
  asm volatile("s_mov_b64 %0, exec\n\t"
               "s_mov_b64 exec, %10\n\tbuffer_load_dwordx4 %1, %5, %6, 0 offen sc1\n\t"
               "s_mov_b64 exec, %11\n\tbuffer_load_dwordx4 %2, %5, %6, %7 offen sc1\n\t"
               "s_mov_b64 exec, %12\n\tbuffer_load_dwordx4 %3, %5, %6, %8 offen sc1\n\t"
               "s_mov_b64 exec, %13\n\tbuffer_load_dwordx4 %4, %5, %6, %9 offen sc1\n\t"
               "s_mov_b64 exec, %0\n\ts_waitcnt vmcnt(0)"
               : "=&s"(sv), "+v"(a), "+v"(b), "+v"(c), "+v"(d) : "v"(off), "s"(r), "s"(s1), "s"(s2), "s"(s3), "s"(m0), "s"(m1), "s"(m2), "s"(m3) : "memory");
}
__device__ __forceinline__ void ldm2(rsrc_t r, int off, unsigned s1, v4u& a, v4u& b, u64 m0, u64 m1) {
  u64 sv;
  asm volatile("s_mov_b64 %0, exec\n\t"
               "s_mov_b64 exec, %6\n\tbuffer_load_dwordx4 %1, %3, %4, 0 offen sc1\n\t"
               "s_mov_b64 exec, %7\n\tbuffer_load_dwordx4 %2, %3, %4, %5 offen sc1\n\t"
               "s_mov_b64 exec, %0\n\ts_waitcnt vmcnt(0)"
               : "=&s"(sv), "+v"(a), "+v"(b) : "v"(off), "s"(r), "s"(s1), "s"(m0), "s"(m1) : "memory");
}
__device__ __forceinline__ void ldm6(rsrc_t r, int off, v4u (&h)[6], u64 m0, u64 m1, u64 m2, u64 m3, u64 m4, u64 m5) {
  u64 sv;
  asm volatile("s_mov_b64 %0, exec\n\t"
               "s_mov_b64 exec, %9\n\tbuffer_load_dwordx4 %1, %7, %8, 0 offen sc1\n\t"
               "s_mov_b64 exec, %10\n\tbuffer_load_dwordx4 %2, %7, %8, 0 offen offset:512 sc1\n\t"
               "s_mov_b64 exec, %11\n\tbuffer_load_dwordx4 %3, %7, %8, 0 offen offset:1024 sc1\n\t"
               "s_mov_b64 exec, %12\n\tbuffer_load_dwordx4 %4, %7, %8, 0 offen offset:1536 sc1\n\t"
               "s_mov_b64 exec, %13\n\tbuffer_load_dwordx4 %5, %7, %8, 0 offen offset:2048 sc1\n\t"
               "s_mov_b64 exec, %14\n\tbuffer_load_dwordx4 %6, %7, %8, 0 offen offset:2560 sc1\n\t"
               "s_mov_b64 exec, %0\n\ts_waitcnt vmcnt(0)"
               : "=&s"(sv), "+v"(h[0]), "+v"(h[1]), "+v"(h[2]), "+v"(h[3]), "+v"(h[4]), "+v"(h[5]) : "v"(off), "s"(r), "s"(m0), "s"(m1), "s"(m2), "s"(m3), "s"(m4), "s"(m5) : "memory");
}
__device__ __forceinline__ u64 inval(v4u v) { return __builtin_amdgcn_ballot_w64(!valid4(v)); }

__device__ __forceinline__ unsigned mx4(v4u v) { return max(max(v.x, v.y), max(v.z, v.w)); }
__device__ __forceinline__ void stclr(rsrc_t r, int off, v4u v) { __builtin_amdgcn_raw_buffer_store_b128(v, r, off, 0, CLRAUX); }

__global__ void __launch_bounds__(NT, 2) fused_kernel(
    const u16* __restrict__ x, const u16* __restrict__ ckv, const u16* __restrict__ w_q, const u16* __restrict__ w_uk,
    const u16* __restrict__ w_uv, const u16* __restrict__ w_o, const u16* __restrict__ w1, const u16* __restrict__ b1,
    const u16* __restrict__ w2, const u16* __restrict__ b2, const u16* __restrict__ cvec, const u16* __restrict__ w_mod,
    u16* __restrict__ out, unsigned char* ws) {
  extern __shared__ __attribute__((aligned(16))) unsigned char lds_raw[];
  LAS unsigned char* L = (LAS unsigned char*)lds_raw;
  const int t = threadIdx.x, lane = t & 63, wave = __builtin_amdgcn_readfirstlane(t >> 6);
  const int b = blockIdx.x, xc = b & 7, r = ((b >> 3) + ROFF) & 31;
  const int n = lane & 15, q = lane >> 4;
  const rsrc_t rs = __builtin_amdgcn_make_buffer_rsrc((void*)ws, 0, WS_USED, 0x00020000);
  LAS unsigned char* SM = L + L_SM;
  const v4u sv4 = {SENT, SENT, SENT, SENT};
  STAMP_DECL;
  const u64 tst = __builtin_amdgcn_s_memrealtime();
  (void)tst;
  STAMP(0);
#define DELAY(x) do { while (__builtin_amdgcn_s_memrealtime() - tst < (x)) __builtin_amdgcn_s_sleep(2); } while (0)

  if (r < 16) {
    const int head = 4 * xc + (r >> 2), cs = r & 3;
    LAS unsigned char* hp = SM;
    LAS unsigned* q16 = (LAS unsigned*)(SM + 2304);
    LAS unsigned char* qst = SM + 8192;
    LAS unsigned char* WKI = L + 98304;
    LAS unsigned char* WQI = L + 65536;
    const int sfoff = SF_OFF + xc * 8192 + lane * 128;
    LAS unsigned* lcnt = (LAS unsigned*)(SM + 9472);
    const unsigned short cvl0 = cvec[(r & 7) * 64 + (t >> 4)], cvl1 = cvec[(r & 7) * 64 + 32 + (t >> 4)];
    bf16x8 wk[4];
    if (wave < 4) {
      const int suoff = SU_OFF + xc * 16384 + cs * 4096 + t * 16;
      const int sqoff = SQ_OFF + head * 2048 + (t & 127) * 16;
      const v4u snapu = ld16(rs, suoff);
      const v4u snapq = ld16(rs, sqoff);
      const unsigned snapf = ld8(rs, sfoff).y;
      if (t == 0) *lcnt = 0u;
      bar();
      v4u v = snapu; int spins = 0; bool ok = false;
      for (;;) {
        CBAR();
        if (!ok) v = ld16(rs, suoff);
        ok = (v.y != snapu.y) & (v.w != snapu.w);
        if (__all(ok) || ++spins > SPIN_CAP) break;
      }
      v2u hh = {v.x, v.z};
      *(LAS v2u*)(hp + ((t >> 4) & 7) * 272 + (t >> 7) * 128 + (t & 15) * 8) = hh;
      STAMP(1);
      bar();
      STAMP(2);
#pragma unroll
      for (int i = 0; i < 4; i++) wk[i] = *(const LAS bf16x8*)(WKI + (wave * 16 + n) * 256 + (((4 * i + q) ^ swz(n)) * 16));
      if (wave < 2) {
        spins = 0; ok = false;
        for (;;) {
          CBAR();
          if (!ok) v = ld16(rs, sqoff);
          ok = (v.y != snapq.y) & (v.w != snapq.w);
          if (__all(ok) || ++spins > SPIN_CAP) break;
        }
        v2u o2 = {v.x, v.z};
        *(LAS v2u*)(qst + (t >> 5) * 272 + (t & 31) * 8) = o2;
      } else if (wave == 2) {
        spins = 0; ok = false;
        for (;;) { CBAR(); if (!ok) ok = ld8(rs, sfoff).y != snapf; if (__all(ok) || ++spins > SPIN_CAP) break; }
      }
      bar();
    } else {
      const int lw = wave - 4;
      const int sqo = SQ_OFF + head * 2048 + cs * 512 + lw * 128 + ((lane >> 2) & 7) * 16;
      const v4u oldq = ld16(rs, sqo);
      bar();
#pragma unroll
      for (int i = 0; i < 16; i++) {
        const int j = 4 * i + lw, rg = j & 31, row = 4 * rg + q;
        const u16* src = (i < 8 ? w_q : w_uk) + (size_t)(cs * 128 + row) * 4096 + head * 128 + ((n ^ swz(row)) * 8);
        dma16(src, L + 65536 + j * 1024);
      }
      VMWAIT0();
      if (lane == 0) __hip_atomic_fetch_add(lcnt, 1u, __ATOMIC_RELAXED, __HIP_MEMORY_SCOPE_WORKGROUP);
      { int spins = 0; while (__hip_atomic_load(lcnt, __ATOMIC_RELAXED, __HIP_MEMORY_SCOPE_WORKGROUP) < 4u && ++spins < SPIN_CAP) {} }
      CBAR();
      bf16x8 wqf[8];
#pragma unroll
      for (int i = 0; i < 8; i++) wqf[i] = trfrag<256, true>(WQI, 32 * (i & 3), 4 * lw + 2 * (i >> 2), lane);
#pragma unroll
      for (int i = 0; i < 4; i++) wk[i] = *(const LAS bf16x8*)(WKI + (wave * 16 + n) * 256 + (((4 * i + q) ^ swz(n)) * 16));
      bar();
      f32x4 a0 = {0, 0, 0, 0}, a1 = {0, 0, 0, 0}; bf16x8 hf[4];
#pragma unroll
      for (int i = 0; i < 4; i++) hf[i] = *(const LAS bf16x8*)(hp + (n & 7) * 272 + i * 64 + q * 16);
      SB();
#pragma unroll
      for (int i = 0; i < 4; i++) { a0 = MFMA(hf[i], wqf[i], a0); a1 = MFMA(hf[i], wqf[4 + i], a1); }
      const float sq = red16((a0[0] + a0[1]) + (a0[2] + a0[3]), (a1[0] + a1[1]) + (a1[2] + a1[3]));
      const unsigned d = pk2(sq, dppf<0x101>(sq));
      const unsigned d2 = dppu<0x102>(d);
      if (lane < 32 && (lane & 3) == 0) { v4u o = {d, oldq.y + 1u, d2, oldq.w + 1u}; st16(rs, sqo, o); }
      {
        const int cb = S3_OFF + (head * 32 + cs * 8) * S3R + (t - 256) * 16;
        stclr(rs, cb, sv4); stclr(rs, cb + 4096, sv4);
        if (wave == 4) stclr(rs, cb + 8192, sv4);
        else if (wave == 5 && lane < 8) stclr(rs, S4_OFF + head * 512 + cs * 128 + lane * 16, sv4);
      }
      bar();
      if (lane < 32 && (lane & 3) == 0) { v4u o = {d, oldq.y + 2u, d2, oldq.w + 2u}; st16(rs, sqo, o); }
    }
    STAMP(13);
    {
      f32x4 a = {0, 0, 0, 0}; bf16x8 qf[4];
#pragma unroll
      for (int i = 0; i < 4; i++) qf[i] = *(const LAS bf16x8*)(qst + (n & 3) * 272 + i * 64 + q * 16);
      SB();
      f32x4 ab = {0, 0, 0, 0};
      a = MFMA(wk[0], qf[0], a); ab = MFMA(wk[1], qf[1], ab); a = MFMA(wk[2], qf[2], a); ab = MFMA(wk[3], qf[3], ab);
      a += ab;
#pragma unroll
      for (int i = 0; i < 4; i++) { a[i] += dppf<0xB1>(a[i]); a[i] += dppf<0x4E>(a[i]); }
      const float SC = 0.08838834764831845f * 1.4426950408889634f;
      v2u o = {canon(pk2(a[0] * SC, a[1] * SC)), canon(pk2(a[2] * SC, a[3] * SC))};
      if (n == 0) *(LAS v2u*)(q16 + 64 + wave * 8 + q * 2) = o;
    }
    if (wave >= 4) VMWAIT0();
    bar();
    if (wave < 2) { const v4u o = *(const LAS v4u*)(q16 + 64 + n * 4); st16(rs, S2_OFF + (wave * 4 + q) * 32768 + head * 1024 + cs * 256 + n * 16, o); }
    STAMP(3);
    bar();
    {
      const int kj = r & 7, cg = xc * 2 + (r >> 3);
      const u16* pl = w_mod + (size_t)(kj * 64 + (t >> 4)) * 3072 + 1024 + cg * 128 + n * 8;
      v4u w0 = LDNT(v4u, pl), w1r = LDNT(v4u, pl + (size_t)32 * 3072);
      CBAR();
      if (r < 8) {
#pragma unroll
        for (int i = 0; i < 8; i++) {
          const int rg = 8 * i + wave, row = 8 * rg + (lane >> 3);
          glds16(w_o + (size_t)(xc * 512 + row) * 512 + r * 64 + (((lane & 7) ^ ((row & 2) | ((row >> 1) & 4))) * 8), (unsigned)(rg * 1024));
        }
        asm volatile("s_waitcnt vmcnt(8)" ::: "memory");
      }
      const float c0 = bflo(cvl0), c1 = bflo(cvl1);
      const unsigned s01 = pk2(c0 * __builtin_amdgcn_rcpf(1.f + __expf(-c0)), c1 * __builtin_amdgcn_rcpf(1.f + __expf(-c1)));
      interleave(w0, w1r);
      float a[8] = {dot2(w0.x, s01, 0.f), dot2(w0.y, s01, 0.f), dot2(w0.z, s01, 0.f), dot2(w0.w, s01, 0.f),
                    dot2(w1r.x, s01, 0.f), dot2(w1r.y, s01, 0.f), dot2(w1r.z, s01, 0.f), dot2(w1r.w, s01, 0.f)};
      float b0 = red32(a[0], a[4]), b1x = red32(a[1], a[5]), b2x = red32(a[2], a[6]), b3 = red32(a[3], a[7]);
      f32x2 o = {red16(b0, b2x), red16(b1x, b3)};
      LAS float* apart = (LAS float*)(SM + 12288);
      *(LAS f32x2*)(apart + wave * 128 + n * 8 + 4 * (lane >> 5) + 2 * ((lane >> 4) & 1)) = o;
      bar();
      if (wave == 7) {
        f32x2 sacc = {0.f, 0.f};
#pragma unroll
        for (int w = 0; w < 8; w++) sacc += *(LAS f32x2*)(apart + w * 128 + lane * 2);
        const unsigned d = canon(pk2(sacc.x, sacc.y));
        v4u o4 = {dppu<0x00>(d), dppu<0x55>(d), dppu<0xAA>(d), dppu<0xFF>(d)};
        if ((lane & 3) == 0) st16(rs, SL_OFF + kj * 4096 + cg * 256 + lane * 4, o4);
      }
      bar();
    }
    if (r < 8) {
      LAS float* fpart = (LAS float*)(SM + 3072);
      LAS unsigned char* cst = SM + 4096;
      LAS float* fp64 = (LAS float*)(SM + 16384);
      VMWAIT0();
      bar();
      bf16x8 wof[8];
      {
        const int g = lane >> 4, qq = (lane & 15) >> 2, pp = lane & 3;
#pragma unroll
        for (int i = 0; i < 8; i++) {
          s16x4 h[2];
#pragma unroll
          for (int tt = 0; tt < 2; tt++) {
            const int row = 32 * (2 * wave + (i >> 2)) + 8 * g + 4 * tt + qq;
            const int ch = (2 * (i & 3) + (pp >> 1)) ^ ((row & 2) | ((row >> 1) & 4));
            h[tt] = __builtin_amdgcn_ds_read_tr16_b64_v4i16((LAS s16x4*)(L + row * 128 + ch * 16 + (pp & 1) * 8));
          }
          wof[i] = (bf16x8){h[0].x, h[0].y, h[0].z, h[0].w, h[1].x, h[1].y, h[1].z, h[1].w};
        }
      }
      if (wave >= 4) {
        int spins = 0;
        for (;;) { CBAR(); v4u v = ld16(rs, S2_OFF + xc * 32768 + xc * 4096 + (t - 256) * 16); if (__all(mx4(v) != SENT) || ++spins > SPIN_CAP) break; __builtin_amdgcn_s_sleep(2); }
      }
      bar();
      STAMP(4);
      {
        v4u av0, av1; int spins = 0;
        const int off = S4_OFF + ((4 * xc + (wave >> 1)) * 2 + (n & 1)) * 256 + (wave & 1) * 128 + q * 16;
        for (;;) { CBAR(); av0 = ld16(rs, off); av1 = ld16(rs, off + 64); if (__all(max(mx4(av0), mx4(av1)) != SENT) || ++spins > SPIN_CAP) break; }
        STAMP(10);
        f32x4 a[4];
#pragma unroll
        for (int T = 0; T < 4; T++) { a[T] = MFMA(__builtin_bit_cast(bf16x8, av0), wof[T], ((f32x4){0, 0, 0, 0})); a[T] = MFMA(__builtin_bit_cast(bf16x8, av1), wof[4 + T], a[T]); }
        const f32x4 sel = q == 0 ? a[0] : q == 1 ? a[1] : q == 2 ? a[2] : a[3];
        fp64[wave * 64 + lane] = sel[0] + sel[1];
        bar();
        if (wave == 0) {
          float v = 0.f;
#pragma unroll
          for (int w = 0; w < 8; w++) v += fp64[w * 64 + lane];
          const unsigned d = canon(pk2(v, dppf<0x101>(v)));
          if (!(lane & 1)) ((LAS unsigned*)cst)[lane >> 1] = d;
          LGWAIT0();
          const v4u o = *(const LAS v4u*)(cst + (lane & 7) * 16);
          st16(rs, S5_OFF + (lane >> 3) * 8192 + xc * 1024 + r * 128 + (lane & 7) * 16, o);
        }
      }
      STAMP(11);
    } else {
      const int ti = xc * 8 + (r - 8);
      LAS unsigned char* W2I = L;
      const float b2v = bflo(b2[ti * 8 + (lane & 7)]);
      const float x8 = bflo(x[ti * 8 + (lane & 7)]);
      CBAR();
#pragma unroll
      for (int i = 0; i < 3; i++) { const int rg = 8 * i + wave; if (rg < 22) { int row = rg * 64 + lane; row = row < 1376 ? row : 1375; dma16n(w2 + (size_t)row * 512 + ti * 8, W2I + rg * 1024); } }
      CBAR();
      LAS float* hpart = (LAS float*)(SM + 2048);
      LAS unsigned* sbuf = (LAS unsigned*)(SM + 2560);
      VMWAIT0();
      bar();
      STAMP(5);
      bf16x8 w2f[6];
#pragma unroll
      for (int i = 0; i < 6; i++) {
        const int ks = wave + 8 * i < 43 ? wave + 8 * i : 0;
        const int g = lane >> 4, qq = (lane & 15) >> 2, pp = lane & 3;
        s16x4 h0 = __builtin_amdgcn_ds_read_tr16_b64_v4i16((LAS s16x4*)(W2I + (32 * ks + 8 * g + qq) * 16 + (pp & 1) * 8));
        s16x4 h1 = __builtin_amdgcn_ds_read_tr16_b64_v4i16((LAS s16x4*)(W2I + (32 * ks + 8 * g + 4 + qq) * 16 + (pp & 1) * 8));
        w2f[i] = (bf16x8){h0.x, h0.y, h0.z, h0.w, h1.x, h1.y, h1.z, h1.w};
      }
      float xn8 = 0.f, gbv = 0.f;
      if (wave == 0) {
        const int off = lane < 8 ? S5_OFF + xc * 8192 + lane * 1024 + ti * 16 : SL_OFF + (lane & 7) * 4096 + ((lane & 16) ? 3072 : 0) + ti * 16;
        v4u v; int spins = 0;
        for (;;) { CBAR(); v = ld16(rs, off); if (__all(mx4(v) != SENT) || ++spins > SPIN_CAP) break; __builtin_amdgcn_s_sleep(4); }
        if (lane < 24) *(LAS v4u*)(sbuf + lane * 4) = v;
        LGWAIT0();
        const LAS u16* sh = (const LAS u16*)sbuf;
        float ao = 0, ga8 = 0;
#pragma unroll
        for (int k = 0; k < 8; k++) { ao += bflo(sh[k * 8 + (lane & 7)]); ga8 += bflo(sh[64 + k * 8 + (lane & 7)]); gbv += bflo(sh[128 + k * 8 + (lane & 7)]); }
        ga8 = rbf(ga8); gbv = rbf(gbv);
        xn8 = rbf(x8 + rbf(ga8 * rbf(ao)));
      }
      bar();
      STAMP(6);
      {
        v4u hv[6]; int spins = 0;
        const int off = S6_OFF + xc * 6144 + wave * 128 + q * 16, off5 = wave < 3 ? off + 5120 : off;
        for (;;) {
          CBAR();
#pragma unroll
          for (int i = 0; i < 5; i++) hv[i] = ld16(rs, off + i * 1024);
          hv[5] = ld16(rs, off5);
          const unsigned m = max(max(max(mx4(hv[0]), mx4(hv[1])), max(mx4(hv[2]), mx4(hv[3]))), max(mx4(hv[4]), mx4(hv[5])));
          if (__all(m != SENT) || ++spins > SPIN_CAP) break;
          POLLSLEEP();
        }
        if (wave >= 3) hv[5] = (v4u){0, 0, 0, 0};
        STAMP(8);
        f32x4 a = {0, 0, 0, 0}, a2 = {0, 0, 0, 0};
#pragma unroll
        for (int i = 0; i < 3; i++) { a = MFMA(__builtin_bit_cast(bf16x8, hv[2 * i]), w2f[2 * i], a); a2 = MFMA(__builtin_bit_cast(bf16x8, hv[2 * i + 1]), w2f[2 * i + 1], a2); }
        if (lane < 8) hpart[wave * 8 + lane] = a[0] + a2[0];
      }
      bar();
      if (wave == 0) {
        float a = hpart[lane];
        a += dppf<0x128>(a); a = red16(a, a); a = red32(a, a);
        const float o = rbf(rbf(a) + b2v);
        const float res = xn8 + rbf(gbv * o);
        if (lane < 8) out[ti * 8 + lane] = __builtin_bit_cast(u16, (__bf16)res);
      }
      STAMP(9);
    }
    STAMP_FLUSH();
  } else if (r >= 24) {
    const int e = r - 24, head = 4 * xc + (e >> 1), half = e & 1, ti = xc * 8 + e;
    const int gi = e * 8 + xc;
    const int wst = (gi < 43 ? gi : 0) * 4;
    const int mcol = wave * 64 + 8 * (lane & 7) + 4 * (lane >> 5) + 2 * ((lane >> 4) & 1) + ((lane >> 3) & 1);
    LAS unsigned char* W1I = L + 65536;
    LAS unsigned char* STG = L + 98304;
    LAS u16* scl = (LAS u16*)(SM);
    LAS unsigned* cc16 = (LAS unsigned*)(SM + 1024);
    LAS unsigned* ml = (LAS unsigned*)(SM + 2048);
    LAS float* stat = (LAS float*)(SM + 5120);
    LAS u16* y16 = (LAS u16*)(SM + 5376);
    LAS float* gpart = (LAS float*)(SM + 6400);
    LAS unsigned* pub = (LAS unsigned*)(SM + 7424);
    LAS unsigned* cnt = (LAS unsigned*)(SM + 7680);
    LAS unsigned* tbase = (LAS unsigned*)(SM + 8192);
    LAS float* apart = (LAS float*)(SM + 10240);
    LAS float* fpart = (LAS float*)(SM + 14336);
    LAS unsigned char* cst = SM + 15360;
    const int puoff = SU_OFF + q * 16384 + (xc * 8 + e) * 256 + n * 16;
    const int pfoff = SF_OFF + n * 8192 + ti * 128;
    const int sfoff = SF_OFF + (8 + xc) * 8192 + lane * 128;
#define LDSADD(p) __hip_atomic_fetch_add((p), 1u, __ATOMIC_RELAXED, __HIP_MEMORY_SCOPE_WORKGROUP)
#define REPUB() do { v2u p2_ = *(LAS v2u*)(pub + n * 2); v4u tb_ = *(LAS v4u*)(tbase + lane * 8); unsigned tf_ = tbase[lane * 8 + 4]; unsigned k_ = 0; if (lane == 0) k_ = LDSADD(cnt); k_ = __builtin_amdgcn_readfirstlane(k_); \
      v4u o_ = {p2_.x, tb_.x + k_, p2_.y, tb_.y + k_}; st16(rs, puoff, o_); v4u o2_ = {p2_.x, tb_.z + k_, p2_.y, tb_.w + k_}; st16(rs, puoff + 65536, o2_); \
      if (lane < 16) { v2u f_ = {0u, tf_ + k_}; st8(rs, pfoff, f_); } } while (0)
    v2u pd = {0, 0};
    v4u old0 = {0, 0, 0, 0}, old1 = {0, 0, 0, 0}, xv = {0, 0, 0, 0}; unsigned snapf = 0, xw = 0;
    v4u wm[2]; unsigned short cv[2];
    {
      const u16* p = w_mod + (size_t)(e * 64 + (t >> 4)) * 3072 + (n >> 3) * 512 + xc * 64 + (n & 7) * 8;
      wm[0] = LDNT(v4u, p); wm[1] = LDNT(v4u, p + (size_t)32 * 3072);
      cv[0] = cvec[e * 64 + (t >> 4)]; cv[1] = cvec[e * 64 + 32 + (t >> 4)];
    }
    CBAR();
    const unsigned oldf = ld8(rs, pfoff).y;
    if (wave == 0) { old0 = ld16(rs, puoff); old1 = ld16(rs, puoff + 65536); xv = *(const v4u*)(x + lane * 8); xw = ((const unsigned*)x)[xc * 32 + (lane & 31)]; }
    if (wave == 1) snapf = ld8(rs, sfoff).y;
    CBAR();
    {
      const int pc = t >= 64 ? t - 64 : t;
      st16(rs, wave == 0 ? DUMMY_OFF + ti * 1024 + lane * 16 : S2_OFF + ti * (EGC_PIECES * 16) + (pc < EGC_PIECES ? pc : pc - EGC_PIECES) * 16, sv4);
    }
    CBAR();
    float ln0 = 0.f, ln1 = 0.f;
    if (wave == 0) {
      float xs[8] = {bflo(xv.x), bfhi(xv.x), bflo(xv.y), bfhi(xv.y), bflo(xv.z), bfhi(xv.z), bflo(xv.w), bfhi(xv.w)};
      float s = 0;
#pragma unroll
      for (int i = 0; i < 8; i++) s += xs[i];
      const float mean = wave_sum(s) * (1.f / 512.f);
      float vs = 0;
#pragma unroll
      for (int i = 0; i < 8; i++) { float d = xs[i] - mean; vs += d * d; }
      const float rstd = rsqrtf(wave_sum(vs) * (1.f / 512.f) + 1e-5f);
      ln0 = rbf((bflo(xw) - mean) * rstd); ln1 = rbf((bfhi(xw) - mean) * rstd);
      v4u tb = {old0.y, old0.w, old1.y, old1.w};
      *(LAS v4u*)(tbase + lane * 8) = tb; tbase[lane * 8 + 4] = oldf;
      if (lane == 0) { cnt[0] = 2u + NREP; cnt[1] = 0u; }
    }
    CBAR();
    {
      const float c0 = bflo(cv[0]), c1 = bflo(cv[1]);
      const unsigned s01 = pk2(c0 * __builtin_amdgcn_rcpf(1.f + __expf(-c0)), c1 * __builtin_amdgcn_rcpf(1.f + __expf(-c1)));
      interleave(wm[0], wm[1]);
      float a[8] = {dot2(wm[0].x, s01, 0.f), dot2(wm[0].y, s01, 0.f), dot2(wm[0].z, s01, 0.f), dot2(wm[0].w, s01, 0.f),
                    dot2(wm[1].x, s01, 0.f), dot2(wm[1].y, s01, 0.f), dot2(wm[1].z, s01, 0.f), dot2(wm[1].w, s01, 0.f)};
      float b0 = red32(a[0], a[4]), b1x = red32(a[1], a[5]), b2x = red32(a[2], a[6]), b3 = red32(a[3], a[7]);
      f32x2 o = {red16(b0, b2x), red16(b1x, b3)};
      *(LAS f32x2*)(apart + wave * 128 + n * 8 + 4 * (lane >> 5) + 2 * ((lane >> 4) & 1)) = o;
    }
    bar();
    STAMP(8);
    if (wave == 0) {
      f32x2 sacc = {0.f, 0.f};
#pragma unroll
      for (int w = 0; w < 8; w++) sacc += *(LAS f32x2*)(apart + w * 128 + lane * 2);
      auto r0 = __builtin_amdgcn_permlane32_swap(__float_as_uint(sacc.x), __float_as_uint(sacc.x), false, false);
      auto r1 = __builtin_amdgcn_permlane32_swap(__float_as_uint(sacc.y), __float_as_uint(sacc.y), false, false);
      const float one = e == 0 ? 1.f : 0.f;
      const float u0 = ln0 * (one + __uint_as_float(r0[1])) + __uint_as_float(r0[0]), u1 = ln1 * (one + __uint_as_float(r1[1])) + __uint_as_float(r1[0]);
      if (lane < 32) pub[lane] = pk2(u0, u1);
      LGWAIT0();
      pd = *(LAS v2u*)(pub + n * 2);
      v4u o = {pd.x, old0.y + 1u, pd.y, old0.w + 1u}; st16(rs, puoff, o);
      v4u o2 = {pd.x, old1.y + 1u, pd.y, old1.w + 1u}; st16(rs, puoff + 65536, o2);
    }
    STAMP(9);
    unsigned karr = 0;
    if (wave != 0) { VMWAIT0(); if (lane == 0) karr = LDSADD(cnt + 1); }
    const bool lastw = __builtin_amdgcn_readfirstlane(karr) == 6u;
    if (lastw && lane < 16) { v2u f = {0u, oldf + 1u}; st8(rs, pfoff, f); }
    STAMP(10);
#pragma unroll
    for (int i = 0; i < 4; i++) if (gi < 43) { const int rg = 8 * i + wave; dma16n(w1 + (size_t)(rg * 16 + (lane >> 2)) * 1376 + wst * 8 + (lane & 3) * 8, W1I + rg * 1024); }
    CBAR();
    DELAY(EGL_DELAY);
    const float xme = bflo(x[mcol]);
    const float b1v = bflo(b1[wst * 8 + (lane & 31)]);
    CBAR();
#pragma unroll
    for (int i = 0; i < 8; i++) {
      const int rg = 8 * i + wave, row = 4 * rg + q;
      dma16(w_uv + (size_t)(half * 256 + row) * 4096 + head * 128 + ((n ^ swz(row)) * 8), L + rg * 1024);
    }
    CBAR();
    if (wave == 0 || lastw) {
#pragma unroll 1
      for (int k = 0; k < NREP; k++) {
        __builtin_amdgcn_s_sleep(14);
        if (wave == 0) { v4u o_ = {pd.x, old0.y + 2u + k, pd.y, old0.w + 2u + k}; st16(rs, puoff, o_); v4u o2_ = {pd.x, old1.y + 2u + k, pd.y, old1.w + 2u + k}; st16(rs, puoff + 65536, o2_); }
        if (lastw && lane < 16) { v2u f_ = {0u, oldf + 2u + k}; st8(rs, pfoff, f_); }
      }
    }
    VMWAIT0();
    bar();
    STAMP(1);
    bf16x8 wvf[8], w1f[4];
#pragma unroll
    for (int s2 = 0; s2 < 8; s2++) wvf[s2] = trfrag<256, true>(L, 32 * wave, 2 * s2, lane);
#pragma unroll
    for (int i = 0; i < 4; i++) w1f[i] = trfrag<64, false>(W1I, 64 * wave + 32 * (i >> 1), 2 * (i & 1), lane);
    if (wave == 1) {
      int spins = 0; bool ok = false;
      for (;;) { CBAR(); if (!ok) ok = ld8(rs, sfoff).y != snapf; if (__all(ok) || ++spins > SPIN_CAP) break; if ((spins & 3) == 0) REPUB(); __builtin_amdgcn_s_sleep(2); }
      spins = 0;
      for (;;) { CBAR(); v4u v = ld16(rs, S2_OFF + xc * 32768 + head * 1024 + lane * 16); if (__all(mx4(v) != SENT) || ++spins > SPIN_CAP) break; if ((spins & 3) == 0) REPUB(); __builtin_amdgcn_s_sleep(2); }
    }
    bar();
    STAMP(2);
    {
      LAS float* sclf = (LAS float*)(SM);
      LAS float* epart = (LAS float*)(STG);
      float sc8[8];
      {
        const int soff = S3_OFF + (head * 32 + (lane & 31)) * S3R + 1024;
        if (wave == 7) {
          v2u stt; int spins = 0;
          for (;;) { CBAR(); stt = ld8(rs, soff); if (__all(max(stt.x, stt.y) != SENT) || ++spins > SPIN_CAP) break; if ((spins & 3) == 0) REPUB(); }
          const float m = __uint_as_float(stt.x), l = __uint_as_float(stt.y);
          const float mg = wave_max(m);
          const float sc = __builtin_amdgcn_exp2f(m - mg);
          const float il = 2.f * __builtin_amdgcn_rcpf(wave_sum(sc * l));
          if (lane < 32) sclf[lane] = sc * il;
        }
      }
      v4u p0, p1; int spins = 0;
      const int off = S3_OFF + (head * 32 + n) * S3R + half * 512 + wave * 64 + q * 16;
      for (;;) {
        CBAR();
        p0 = ld16(rs, off); p1 = ld16(rs, off + 16 * S3R);
        if (__all(max(mx4(p0), mx4(p1)) != SENT) || ++spins > SPIN_CAP) break;
        if ((spins & 15) == 0) REPUB();
      }
      STAMP(3);
      f32x4 y0[8], y1[8];
#pragma unroll
      for (int T = 0; T < 8; T++) {
        y0[T] = MFMA(__builtin_bit_cast(bf16x8, p0), wvf[T], ((f32x4){0, 0, 0, 0}));
        y1[T] = MFMA(__builtin_bit_cast(bf16x8, p1), wvf[T], ((f32x4){0, 0, 0, 0}));
      }
      bar();
      { const f32x4 a = *(const LAS f32x4*)(sclf + 4 * q), b = *(const LAS f32x4*)(sclf + 16 + 4 * q); sc8[0] = a[0]; sc8[1] = a[1]; sc8[2] = a[2]; sc8[3] = a[3]; sc8[4] = b[0]; sc8[5] = b[1]; sc8[6] = b[2]; sc8[7] = b[3]; }
      float z[8];
#pragma unroll
      for (int T = 0; T < 8; T++)
        z[T] = ((y0[T][0] * sc8[0] + y0[T][1] * sc8[1]) + (y0[T][2] * sc8[2] + y0[T][3] * sc8[3])) + ((y1[T][0] * sc8[4] + y1[T][1] * sc8[5]) + (y1[T][2] * sc8[6] + y1[T][3] * sc8[7]));
      const float r0 = red32(z[0], z[4]), r1 = red32(z[1], z[5]), r2 = red32(z[2], z[6]), r3 = red32(z[3], z[7]);
      const float u0 = red16(r0, r2), u1 = red16(r1, r3);
      const int T0 = 4 * (lane >> 5) + 2 * ((lane >> 4) & 1);
      epart[wave * 128 + T0 * 16 + n] = u0; epart[wave * 128 + T0 * 16 + 16 + n] = u1;
      bar();
      if (wave == 0) {
        f32x2 sacc = {0.f, 0.f};
#pragma unroll
        for (int w = 0; w < 8; w++) sacc += *(LAS f32x2*)(epart + w * 128 + lane * 2);
        const unsigned d = canon(pk2(sacc.x, sacc.y));
        v4u o4 = {dppu<0x00>(d), dppu<0x55>(d), dppu<0xAA>(d), dppu<0xFF>(d)};
        if ((lane & 3) == 0) st16(rs, S4_OFF + (head * 2 + half) * 256 + lane * 4, o4);
      }
    }
    STAMP(4);
    if (gi >= 43) {
      if (wave == 0) {
        int spins = 0;
        for (;;) {
          CBAR(); v4u v = ld16(rs, DN_OFF + xc * 128);
          if (v.x != SENT || ++spins > SPIN_CAP) break;
          if ((spins & 3) == 0) REPUB();
          __builtin_amdgcn_s_sleep(8);
        }
      }
      STAMP_FLUSH(); return;
    }
    {
      v4u v0, v1, v2; int spins = 0;
      const int off = SL_OFF + wave * 4096 + lane * 16;
      for (;;) { CBAR(); v0 = ld16(rs, off); v1 = ld16(rs, off + 1024); v2 = ld16(rs, off + 2048); if (__all(max(max(mx4(v0), mx4(v1)), mx4(v2)) != SENT) || ++spins > SPIN_CAP) break; if ((spins & 15) == 0) REPUB(); __builtin_amdgcn_s_sleep(1); }
      *(LAS v4u*)(L + wave * 3072 + lane * 16) = v0; *(LAS v4u*)(L + wave * 3072 + 1024 + lane * 16) = v1; *(LAS v4u*)(L + wave * 3072 + 2048 + lane * 16) = v2;
    }
    bar();
    float gam = 0.f, sbm = 0.f, cb1 = 0.f;
    {
      const LAS u16* mlh = (const LAS u16*)L;
#pragma unroll
      for (int k = 0; k < 8; k++) { gam += bflo(mlh[k * 1536 + mcol]); sbm += bflo(mlh[k * 1536 + 512 + mcol]); cb1 += bflo(mlh[k * 1536 + 1024 + mcol]); }
      gam = rbf(gam); sbm = rbf(sbm); cb1 = rbf(1.f + rbf(cb1));
    }
    LAS u16* cs16 = (LAS u16*)(SM + 10240);
    LAS float* gp64 = (LAS float*)(SM + 12288);
    cs16[mcol] = __builtin_bit_cast(u16, (__bf16)cb1); cs16[512 + mcol] = __builtin_bit_cast(u16, (__bf16)sbm);
    LGWAIT0();
    bf16x8 auxf[2];
    {
      const bf16x8 ones = {0x3F80, 0x3F80, 0x3F80, 0x3F80, 0x3F80, 0x3F80, 0x3F80, 0x3F80};
#pragma unroll
      for (int i = 0; i < 2; i++) { const bf16x8 v = *(const LAS bf16x8*)(cs16 + ((n - 1) & 1) * 512 + wave * 64 + i * 32 + q * 8); auxf[i] = n == 0 ? ones : v; }
      f32x4 k0 = MFMA(auxf[0], w1f[0], ((f32x4){0, 0, 0, 0})), k1 = MFMA(auxf[0], w1f[1], ((f32x4){0, 0, 0, 0})), kg = MFMA(auxf[0], auxf[0], ((f32x4){0, 0, 0, 0}));
      k0 = MFMA(auxf[1], w1f[2], k0); k1 = MFMA(auxf[1], w1f[3], k1); kg = MFMA(auxf[1], auxf[1], kg);
      if (lane < 16) {
        LAS float* g = (LAS float*)(L + 32768) + wave * 192;
        g[n] = k0[0]; g[16 + n] = k1[0]; g[32 + n] = kg[0]; g[64 + n] = k0[1]; g[80 + n] = k1[1]; g[96 + n] = kg[1]; g[128 + n] = k0[2]; g[144 + n] = k1[2]; g[160 + n] = kg[2];
      }
    }
    bar();
    float Ow = 0.f, Cw = 0.f, Sw = 0.f, Kc = 0.f, Ks = 0.f, Kc2 = 0.f, Kcs = 0.f, Ks2 = 0.f;
    if (wave == 0) {
      const LAS float* g = (const LAS float*)(L + 32768);
      float o = 0.f, c = 0.f, sg = 0.f;
#pragma unroll
      for (int w = 0; w < 8; w++) { o += g[w * 192 + (lane < 48 ? lane : 0)]; c += g[w * 192 + 64 + (lane < 48 ? lane : 0)]; sg += g[w * 192 + 128 + (lane < 48 ? lane : 0)]; }
      Ow = o; Cw = c; Sw = sg;
      Kc = RL(o, 33); Ks = RL(o, 34); Kc2 = RL(c, 33); Kcs = RL(c, 34); Ks2 = RL(sg, 34);
    }
    asm volatile("" : "+v"(gam), "+v"(cb1), "+v"(Ow), "+v"(Cw), "+v"(Sw));
    STAMP(5);
    {
      v4u v; int spins = 0;
      const int off = S5_OFF + xc * 8192 + (lane >> 3) * 1024 + wave * 128 + (lane & 7) * 16;
      for (;;) { CBAR(); v = ld16(rs, off); if (__all(mx4(v) != SENT) || ++spins > SPIN_CAP) break; if ((spins & 15) == 0) REPUB(); }
      STAMP(6);
      const float b0 = red32(bflo(v.x), bflo(v.z)), b1x = red32(bfhi(v.x), bfhi(v.z)), b2x = red32(bflo(v.y), bflo(v.w)), b3 = red32(bfhi(v.y), bfhi(v.w));
      const float c0 = red16(b0, b2x), c1 = red16(b1x, b3);
      const bool up = lane & 8;
      const float keep = up ? c1 : c0, send = up ? c0 : c1;
      const float ao = keep + dppf<0x128>(send);
      const __bf16 xb = (__bf16)(xme + rbf(gam * rbf(ao)));
      cs16[mcol] = __builtin_bit_cast(u16, (__bf16)((float)xb * cb1)); cs16[512 + mcol] = __builtin_bit_cast(u16, xb);
      LGWAIT0();
      const bf16x8 y0 = *(const LAS bf16x8*)(cs16 + (n & 1) * 512 + wave * 64 + q * 8), y1 = *(const LAS bf16x8*)(cs16 + (n & 1) * 512 + wave * 64 + 32 + q * 8);
      f32x4 z0 = MFMA(y0, w1f[0], ((f32x4){0, 0, 0, 0})), z1 = MFMA(y0, w1f[1], ((f32x4){0, 0, 0, 0})), ax = MFMA(y0, auxf[0], ((f32x4){0, 0, 0, 0})), gr = MFMA(y0, y0, ((f32x4){0, 0, 0, 0}));
      z0 = MFMA(y1, w1f[2], z0); z1 = MFMA(y1, w1f[3], z1); ax = MFMA(y1, auxf[1], ax); gr = MFMA(y1, y1, gr);
      const float lastq = n == 0 ? ax[1] : n == 1 ? gr[1] : gr[0];
      gp64[wave * 64 + lane] = q == 0 ? z0[0] : q == 1 ? z1[0] : q == 2 ? ax[0] : lastq;
    }
    bar();
    if (wave == 0) {
      float a = 0;
#pragma unroll
      for (int w = 0; w < 8; w++) a += gp64[w * 64 + lane];
      const float B1 = RL(a, 32), B3 = RL(a, 33), B4 = RL(a, 34), A1 = RL(a, 48), A2 = RL(a, 49), B2 = RL(a, 50);
      const float mu = A1 * (1.f / 512.f), rr = __builtin_amdgcn_rsqf(fmaxf(A2 * (1.f / 512.f) - mu * mu, 0.f) + 1e-5f);
      const float m2 = (rr * (B1 - mu * Kc) + Ks) * (1.f / 512.f);
      const float e2 = (rr * rr * (B2 - 2.f * mu * B3 + mu * mu * Kc2) + 2.f * rr * (B4 - mu * Kcs) + Ks2) * (1.f / 512.f);
      const float r2 = __builtin_amdgcn_rsqf(fmaxf(e2 - m2 * m2, 0.f) + 1e-5f);
      const float zz = r2 * (rr * (a - mu * Cw) + Sw - m2 * Ow);
      const float z = rbf(rbf(zz) + b1v);
      const float u = z * (0.7978845608028654f + 0.035677408136300125f * z * z);
      const float gl = z * __builtin_amdgcn_rcpf(1.f + __builtin_amdgcn_exp2f(-2.885390081777927f * u));
      const unsigned d = canon(pk2(gl, dppf<0x101>(gl)));
      if (lane < 32 && !(lane & 1)) ((LAS unsigned*)y16)[lane >> 1] = d;
      LGWAIT0();
      const v4u o = *(const LAS v4u*)((LAS unsigned*)y16 + (lane & 3) * 4);
      if (lane < 32) st16(rs, S6_OFF + (lane >> 2) * 6144 + gi * 128 + (lane & 3) * 16, o);
    }
    if (ti == 0 && wave == 1 && lane < 8) { v4u o = {1, 1, 1, 1}; st16(rs, DN_OFF + lane * 128, o); }
    STAMP(7);
    STAMP_FLUSH();
  } else {
    const int e = r - 16, split = 4 * xc + (e & 3), hg = e >> 2;
    const int sfoff = SF_OFF + (8 + xc) * 8192 + lane * 128;
    LAS unsigned char* qa = SM;
    LAS unsigned char* Pm = SM + 16896;
    LAS float* wmax = (LAS float*)(SM + 21504);
    LAS float* wsum = wmax + 128;
    unsigned snapf = 0;
    if (wave == 0) snapf = ld8(rs, sfoff).y;
    bar();
    CBAR();
#pragma unroll
    for (int i = 0; i < 16; i++) {
      const int key = ((i * 8 + wave) + hg * 64) & 127;
      dma16(ckv + (size_t)(split * 128 + key) * 512 + ((lane ^ swz(key)) * 8), L + key * 1024);
    }
    VMWAIT0();
    bar();
    STAMP(3);
    bf16x8 ka[16], va[16];
    {
      const int key = wave * 16 + n, f2 = swz(key);
#pragma unroll
      for (int s2 = 0; s2 < 16; s2++) ka[s2] = *(const LAS bf16x8*)(L + key * 1024 + (((4 * s2 + q) ^ f2) * 16));
#pragma unroll
      for (int i = 0; i < 16; i++) va[i] = trfrag<1024, true>(L, 32 * (i & 3), wave * 8 + 2 * (i >> 2), lane);
    }
    if (wave == 0) {
      int spins = 0; bool ok = false;
      for (;;) { CBAR(); if (!ok) ok = ld8(rs, sfoff).y != snapf; if (__all(ok) || ++spins > SPIN_CAP) break; __builtin_amdgcn_s_sleep(1); }
    }
    bar();
    STAMP(4);
    {
      const int off = S2_OFF + xc * 32768 + hg * 16384 + t * 16;
      v4u v0, v1; int spins = 0;
      for (;;) {
        CBAR();
        v0 = ld16(rs, off); v1 = ld16(rs, off + 8192);
        if (__all(max(mx4(v0), mx4(v1)) != SENT) || ++spins > SPIN_CAP) break;
      }
      *(LAS v4u*)(qa + wave * 1056 + lane * 16) = v0;
      *(LAS v4u*)(qa + (8 + wave) * 1056 + lane * 16) = v1;
    }
    bar();
    STAMP(5);
    f32x4 sc = {0, 0, 0, 0};
    {
      bf16x8 qf[16];
#pragma unroll
      for (int s2 = 0; s2 < 16; s2++) qf[s2] = *(const LAS bf16x8*)(qa + n * 1056 + s2 * 64 + q * 16);
      SB();
      f32x4 sc2 = {0, 0, 0, 0};
#pragma unroll
      for (int s2 = 0; s2 < 8; s2++) { sc = MFMA(ka[2 * s2], qf[2 * s2], sc); sc2 = MFMA(ka[2 * s2 + 1], qf[2 * s2 + 1], sc2); }
      sc += sc2;
    }
    float mx = fmaxf(fmaxf(sc[0], sc[1]), fmaxf(sc[2], sc[3]));
    mx = max16(mx); mx = max32(mx);
    if (lane < 16) wmax[lane * 8 + wave] = mx;
    bar();
    {
      f32x4 m0 = *(LAS f32x4*)(wmax + n * 8), m1 = *(LAS f32x4*)(wmax + n * 8 + 4);
      mx = fmaxf(fmaxf(fmaxf(m0[0], m0[1]), fmaxf(m0[2], m0[3])), fmaxf(fmaxf(m1[0], m1[1]), fmaxf(m1[2], m1[3])));
    }
    const float p0 = __builtin_amdgcn_exp2f(sc[0] - mx), p1 = __builtin_amdgcn_exp2f(sc[1] - mx), p2 = __builtin_amdgcn_exp2f(sc[2] - mx), p3 = __builtin_amdgcn_exp2f(sc[3] - mx);
    { v2u o = {pk2(p0, p1), pk2(p2, p3)}; *(LAS v2u*)(Pm + n * 288 + (wave * 16 + q * 4) * 2) = o; }
    float ps = (p0 + p1) + (p2 + p3);
    ps = red16(ps, ps); ps = red32(ps, ps);
    if (lane < 16) wsum[lane * 8 + wave] = ps;
    bar();
    {
      if (wave == 7) {
        f32x4 s0 = *(LAS f32x4*)(wsum + n * 8), s1 = *(LAS f32x4*)(wsum + n * 8 + 4);
        const float l = ((s0[0] + s0[1]) + (s0[2] + s0[3])) + ((s1[0] + s1[1]) + (s1[2] + s1[3]));
        v2u o2 = {canon(__float_as_uint(mx)), canon(__float_as_uint(l))};
        if (lane < 16) st8(rs, S3_OFF + ((hg * 16 + lane) * 32 + split) * S3R + 1024, o2);
      }
      bf16x8 bp[4];
#pragma unroll
      for (int ks = 0; ks < 4; ks++) bp[ks] = *(const LAS bf16x8*)(Pm + n * 288 + ks * 64 + q * 16);
      SB();
      f32x4 acc[4];
#pragma unroll
      for (int T = 0; T < 4; T++) {
        acc[T] = (f32x4){0, 0, 0, 0};
#pragma unroll
        for (int ks = 0; ks < 4; ks++) acc[T] = MFMA(va[T * 4 + ks], bp[ks], acc[T]);
      }
      unsigned d0[4], d1[4];
#pragma unroll
      for (int T = 0; T < 4; T++) { d0[T] = canon(pk2(acc[T][0], acc[T][1])); d1[T] = canon(pk2(acc[T][2], acc[T][3])); }
      auto r0 = __builtin_amdgcn_permlane16_swap(d0[0], d0[1], false, false);
      auto r1 = __builtin_amdgcn_permlane16_swap(d1[0], d1[1], false, false);
      auto r2 = __builtin_amdgcn_permlane16_swap(d0[2], d0[3], false, false);
      auto r3 = __builtin_amdgcn_permlane16_swap(d1[2], d1[3], false, false);
      v4u oa = {r0[0], r1[0], r0[1], r1[1]}, ob = {r2[0], r3[0], r2[1], r3[1]};
      const v4u obs = {dppu<0x128>(ob.x), dppu<0x128>(ob.y), dppu<0x128>(ob.z), dppu<0x128>(ob.w)};
      const bool lo = n < 8;
      const int o3b = S3_OFF + ((hg * 16 + (n & 7)) * 32 + split) * S3R + (wave * 64 + (q & 1) * 16 + (q & 2) * 4) * 2;
      st16(rs, o3b + (lo ? 0 : 64), lo ? oa : obs);
      st16(rs, o3b + 8 * 32 * S3R + (lo ? 64 : 0), lo ? obs : oa);
    }
    STAMP(6);
    STAMP_FLUSH();
  }
}

extern "C" void kernel_launch(void* const* d_in, const int* in_sizes, int n_in, void* d_out, int out_size, void* d_ws, size_t ws_size, hipStream_t stream) {
  (void)in_sizes; (void)n_in; (void)out_size; (void)ws_size;
  (void)hipFuncSetAttribute((const void*)fused_kernel, hipFuncAttributeMaxDynamicSharedMemorySize, L_TOTAL);
  fused_kernel<<<256, NT, L_TOTAL, stream>>>((const u16*)d_in[0], (const u16*)d_in[1], (const u16*)d_in[2], (const u16*)d_in[3], (const u16*)d_in[4], (const u16*)d_in[5],
                                             (const u16*)d_in[7], (const u16*)d_in[8], (const u16*)d_in[9], (const u16*)d_in[10], (const u16*)d_in[11], (const u16*)d_in[12],
                                             (u16*)d_out, (unsigned char*)d_ws);
}
```

```cpp
#include <hip/hip_runtime.h>
#include <stdint.h>

typedef unsigned short u16;
typedef unsigned v4u __attribute__((ext_vector_type(4)));
typedef unsigned v2u __attribute__((ext_vector_type(2)));
typedef float f32x4 __attribute__((ext_vector_type(4)));
typedef float f32x2 __attribute__((ext_vector_type(2)));
typedef short bf16x8 __attribute__((ext_vector_type(8)));
typedef short s16x4 __attribute__((ext_vector_type(4)));
typedef __bf16 bf2 __attribute__((ext_vector_type(2)));
#define LAS __attribute__((address_space(3)))

constexpr unsigned SENT = 0xFFFFFFFFu;
constexpr int NT = 512;
constexpr int SPIN_CAP = 1 << 20;
#define NREP 2
#define EGL_DELAY 400
#define ROFF 24
#define CLRAUX 16
#define NTAUX 2
#define NTS " nt"
#define LDNT(T, p) __builtin_nontemporal_load((const T*)(p))
constexpr int SU_OFF = 0;
constexpr int SF_OFF = 131072;
constexpr int SQ_OFF = 262144;
constexpr int CLR_OFF = 327680;
constexpr int S2_OFF = CLR_OFF;
constexpr int S5_OFF = S2_OFF + 8 * 32768;
constexpr int S6_OFF = S5_OFF + 65536;
constexpr int SL_OFF = S6_OFF + 49152;
constexpr int SLF_OFF = SL_OFF + 32768;
constexpr int DN_OFF = SLF_OFF + 32768;
constexpr int EGC_END = DN_OFF + 4096;
constexpr int EGC_PIECES = (EGC_END - S2_OFF) / 16 / 64;
constexpr int S4_OFF = EGC_END;
constexpr int S3R = 1152;
constexpr int S3_OFF = S4_OFF + 16384;
constexpr int CLR_END = S3_OFF + 1024 * S3R;
constexpr int DUMMY_OFF = CLR_END;
constexpr int WS_USED = CLR_END + 65536;
constexpr int L_SM = 131072;
constexpr int L_TOTAL = 131072 + 30720;

__device__ __forceinline__ float bflo(unsigned w) { return __uint_as_float(w << 16); }
__device__ __forceinline__ float bfhi(unsigned w) { return __uint_as_float(w & 0xffff0000u); }
__device__ __forceinline__ unsigned pk2(float a, float b) { bf2 p; p.x = (__bf16)a; p.y = (__bf16)b; return __builtin_bit_cast(unsigned, p); }
__device__ __forceinline__ float rbf(float a) { return (float)(__bf16)a; }
__device__ __forceinline__ unsigned canon(unsigned w) { return w == SENT ? 0x7FC07FC0u : w; }
__device__ __forceinline__ float dot2(unsigned a, unsigned b, float c) { return __builtin_amdgcn_fdot2_f32_bf16(__builtin_bit_cast(bf2, a), __builtin_bit_cast(bf2, b), c, false); }
__device__ __forceinline__ void bar() { __builtin_amdgcn_sched_barrier(0); asm volatile("s_waitcnt lgkmcnt(0)\n\ts_barrier" ::: "memory"); __builtin_amdgcn_sched_barrier(0); }
template <int CTRL> __device__ __forceinline__ float dppf(float v) { return __int_as_float(__builtin_amdgcn_update_dpp(0, __float_as_int(v), CTRL, 0xf, 0xf, false)); }
template <int CTRL> __device__ __forceinline__ unsigned dppu(unsigned v) { return (unsigned)__builtin_amdgcn_update_dpp(0, (int)v, CTRL, 0xf, 0xf, false); }
__device__ __forceinline__ float red32(float a, float b) {
  auto r = __builtin_amdgcn_permlane32_swap(__float_as_uint(a), __float_as_uint(b), false, false);
  return __uint_as_float(r[0]) + __uint_as_float(r[1]);
}
__device__ __forceinline__ float red16(float a, float b) {
  auto r = __builtin_amdgcn_permlane16_swap(__float_as_uint(a), __float_as_uint(b), false, false);
  return __uint_as_float(r[0]) + __uint_as_float(r[1]);
}
__device__ __forceinline__ float max32(float a) { auto r = __builtin_amdgcn_permlane32_swap(__float_as_uint(a), __float_as_uint(a), false, false); return fmaxf(__uint_as_float(r[0]), __uint_as_float(r[1])); }
__device__ __forceinline__ float max16(float a) { auto r = __builtin_amdgcn_permlane16_swap(__float_as_uint(a), __float_as_uint(a), false, false); return fmaxf(__uint_as_float(r[0]), __uint_as_float(r[1])); }
__device__ __forceinline__ float wave_sum(float v) {
  v += dppf<0x128>(v); v += dppf<0x124>(v); v += dppf<0x122>(v); v += dppf<0x121>(v);
  v = red16(v, v); v = red32(v, v); return v;
}
__device__ __forceinline__ float wave_max(float v) {
  v = fmaxf(v, dppf<0x128>(v)); v = fmaxf(v, dppf<0x124>(v)); v = fmaxf(v, dppf<0x122>(v)); v = fmaxf(v, dppf<0x121>(v));
  v = max16(v); v = max32(v); return v;
}
__device__ __forceinline__ unsigned ilo(unsigned a, unsigned b) { return __builtin_amdgcn_perm(b, a, 0x05040100u); }
__device__ __forceinline__ unsigned ihi(unsigned a, unsigned b) { return __builtin_amdgcn_perm(b, a, 0x07060302u); }
__device__ __forceinline__ void interleave(v4u& a, v4u& b) {
  v4u x, y;
  x.x = ilo(a.x, b.x); x.y = ihi(a.x, b.x); x.z = ilo(a.y, b.y); x.w = ihi(a.y, b.y);
  y.x = ilo(a.z, b.z); y.y = ihi(a.z, b.z); y.z = ilo(a.w, b.w); y.w = ihi(a.w, b.w);
  a = x; b = y;
}
typedef __amdgpu_buffer_rsrc_t rsrc_t;
__device__ __forceinline__ v4u ld16(rsrc_t r, int off) { return __builtin_amdgcn_raw_buffer_load_b128(r, off, 0, 16); }
__device__ __forceinline__ v2u ld8(rsrc_t r, int off) { return __builtin_amdgcn_raw_buffer_load_b64(r, off, 0, 16); }
__device__ __forceinline__ void st16(rsrc_t r, int off, v4u v) { __builtin_amdgcn_raw_buffer_store_b128(v, r, off, 0, 16); }
__device__ __forceinline__ void st8(rsrc_t r, int off, v2u v) { __builtin_amdgcn_raw_buffer_store_b64(v, r, off, 0, 16); }
__device__ __forceinline__ void glds16(const void* g, unsigned lds_base) {
  unsigned sv; asm volatile("s_mov_b32 %0, m0\n\ts_mov_b32 m0, %2\n\ts_nop 0\n\tglobal_load_lds_dwordx4 %1, off" NTS "\n\ts_mov_b32 m0, %0" : "=&s"(sv) : "v"(g), "s"(lds_base) : "memory");
}
#define RL(v, l) __int_as_float(__builtin_amdgcn_readlane(__float_as_int(v), (l)))
#define SB() __builtin_amdgcn_sched_barrier(0)
#define CBAR() do { asm volatile("" ::: "memory"); __builtin_amdgcn_sched_barrier(0); } while (0)

__device__ __forceinline__ int swz(int row) { return ((row & 3) | ((row >> 1) & 4)) << 1; }
template <int AUX> __device__ __forceinline__ void dma16a(const void* g, LAS unsigned char* l) { __builtin_amdgcn_global_load_lds((const __attribute__((address_space(1))) unsigned*)g, (LAS unsigned*)l, 16, 0, AUX); }
#define dma16 dma16a<NTAUX>
#define NARROW 2
#define dma16n dma16a<NARROW>
#define LDNAR(T, p) __builtin_nontemporal_load((const T*)(p))
template <int RB, bool SW>
__device__ __forceinline__ bf16x8 trfrag(const LAS unsigned char* img, int k0, int ch0, int lane) {
  const int g = lane >> 4, qq = (lane & 15) >> 2, pp = lane & 3;
  s16x4 h[2];
#pragma unroll
  for (int tt = 0; tt < 2; tt++) {
    const int row = k0 + 8 * g + 4 * tt + qq;
    const int ch = SW ? ((ch0 + (pp >> 1)) ^ swz(row)) : (ch0 + (pp >> 1));
    h[tt] = __builtin_amdgcn_ds_read_tr16_b64_v4i16((LAS s16x4*)(img + row * RB + ch * 16 + (pp & 1) * 8));
  }
  return (bf16x8){h[0].x, h[0].y, h[0].z, h[0].w, h[1].x, h[1].y, h[1].z, h[1].w};
}
#define MFMA(a, b, c) __builtin_amdgcn_mfma_f32_16x16x32_bf16(a, b, c, 0, 0, 0)
#define VMWAIT0() do { asm volatile("" ::: "memory"); __builtin_amdgcn_s_waitcnt(0x0F70); asm volatile("s_waitcnt vmcnt(0)" ::: "memory"); } while (0)
#define LGWAIT0() asm volatile("s_waitcnt lgkmcnt(0)" ::: "memory")
typedef unsigned long long u64;
__device__ __forceinline__ unsigned mx4(v4u v) { return max(max(v.x, v.y), max(v.z, v.w)); }
__device__ __forceinline__ void stclr(rsrc_t r, int off, v4u v) { __builtin_amdgcn_raw_buffer_store_b128(v, r, off, 0, CLRAUX); }

__global__ void __launch_bounds__(NT, 2) fused_kernel(
    const u16* __restrict__ x, const u16* __restrict__ ckv, const u16* __restrict__ w_q, const u16* __restrict__ w_uk,
    const u16* __restrict__ w_uv, const u16* __restrict__ w_o, const u16* __restrict__ w1, const u16* __restrict__ b1,
    const u16* __restrict__ w2, const u16* __restrict__ b2, const u16* __restrict__ cvec, const u16* __restrict__ w_mod,
    u16* __restrict__ out, unsigned char* ws) {
  extern __shared__ __attribute__((aligned(16))) unsigned char lds_raw[];
  LAS unsigned char* L = (LAS unsigned char*)lds_raw;
  const int t = threadIdx.x, lane = t & 63, wave = __builtin_amdgcn_readfirstlane(t >> 6);
  const int b = blockIdx.x, xc = b & 7, r = ((b >> 3) + ROFF) & 31;
  const int n = lane & 15, q = lane >> 4;
  const rsrc_t rs = __builtin_amdgcn_make_buffer_rsrc((void*)ws, 0, WS_USED, 0x00020000);
  LAS unsigned char* SM = L + L_SM;
  const v4u sv4 = {SENT, SENT, SENT, SENT};
  const u64 tst = __builtin_amdgcn_s_memrealtime();
  (void)tst;
#define DELAY(x) do { while (__builtin_amdgcn_s_memrealtime() - tst < (x)) __builtin_amdgcn_s_sleep(2); } while (0)

  if (r < 16) {
    const int head = 4 * xc + (r >> 2), cs = r & 3;
    LAS unsigned char* hp = SM;
    LAS unsigned* q16 = (LAS unsigned*)(SM + 2304);
    LAS unsigned char* qst = SM + 8192;
    LAS unsigned char* WKI = L + 98304;
    LAS unsigned char* WQI = L + 65536;
    const int sfoff = SF_OFF + xc * 8192 + lane * 128;
    LAS unsigned* lcnt = (LAS unsigned*)(SM + 9472);
    const unsigned short cvl0 = cvec[(r & 7) * 64 + (t >> 4)], cvl1 = cvec[(r & 7) * 64 + 32 + (t >> 4)];
    bf16x8 wk[4];
    if (wave < 4) {
      const int suoff = SU_OFF + xc * 16384 + cs * 4096 + t * 16;
      const int sqoff = SQ_OFF + head * 2048 + (t & 127) * 16;
      const v4u snapu = ld16(rs, suoff);
      const v4u snapq = ld16(rs, sqoff);
      const unsigned snapf = ld8(rs, sfoff).y;
      if (t == 0) *lcnt = 0u;
      bar();
      v4u v = snapu; int spins = 0; bool ok = false;
      for (;;) {
        CBAR();
        if (!ok) v = ld16(rs, suoff);
        ok = (v.y != snapu.y) & (v.w != snapu.w);
        if (__all(ok) || ++spins > SPIN_CAP) break;
      }
      v2u hh = {v.x, v.z};
      *(LAS v2u*)(hp + ((t >> 4) & 7) * 272 + (t >> 7) * 128 + (t & 15) * 8) = hh;
      bar();
#pragma unroll
      for (int i = 0; i < 4; i++) wk[i] = *(const LAS bf16x8*)(WKI + (wave * 16 + n) * 256 + (((4 * i + q) ^ swz(n)) * 16));
      if (wave < 2) {
        spins = 0; ok = false;
        for (;;) {
          CBAR();
          if (!ok) v = ld16(rs, sqoff);
          ok = (v.y != snapq.y) & (v.w != snapq.w);
          if (__all(ok) || ++spins > SPIN_CAP) break;
        }
        v2u o2 = {v.x, v.z};
        *(LAS v2u*)(qst + (t >> 5) * 272 + (t & 31) * 8) = o2;
      } else if (wave == 2) {
        spins = 0; ok = false;
        for (;;) { CBAR(); if (!ok) ok = ld8(rs, sfoff).y != snapf; if (__all(ok) || ++spins > SPIN_CAP) break; }
      }
      bar();
    } else {
      const int lw = wave - 4;
      const int sqo = SQ_OFF + head * 2048 + cs * 512 + lw * 128 + ((lane >> 2) & 7) * 16;
      const v4u oldq = ld16(rs, sqo);
      bar();
#pragma unroll
      for (int i = 0; i < 16; i++) {
        const int j = 4 * i + lw, rg = j & 31, row = 4 * rg + q;
        const u16* src = (i < 8 ? w_q : w_uk) + (size_t)(cs * 128 + row) * 4096 + head * 128 + ((n ^ swz(row)) * 8);
        dma16(src, L + 65536 + j * 1024);
      }
      VMWAIT0();
      if (lane == 0) __hip_atomic_fetch_add(lcnt, 1u, __ATOMIC_RELAXED, __HIP_MEMORY_SCOPE_WORKGROUP);
      { int spins = 0; while (__hip_atomic_load(lcnt, __ATOMIC_RELAXED, __HIP_MEMORY_SCOPE_WORKGROUP) < 4u && ++spins < SPIN_CAP) {} }
      CBAR();
      bf16x8 wqf[8];
#pragma unroll
      for (int i = 0; i < 8; i++) wqf[i] = trfrag<256, true>(WQI, 32 * (i & 3), 4 * lw + 2 * (i >> 2), lane);
#pragma unroll
      for (int i = 0; i < 4; i++) wk[i] = *(const LAS bf16x8*)(WKI + (wave * 16 + n) * 256 + (((4 * i + q) ^ swz(n)) * 16));
      bar();
      f32x4 a0 = {0, 0, 0, 0}, a1 = {0, 0, 0, 0}; bf16x8 hf[4];
#pragma unroll
      for (int i = 0; i < 4; i++) hf[i] = *(const LAS bf16x8*)(hp + (n & 7) * 272 + i * 64 + q * 16);
      SB();
#pragma unroll
      for (int i = 0; i < 4; i++) { a0 = MFMA(hf[i], wqf[i], a0); a1 = MFMA(hf[i], wqf[4 + i], a1); }
      const float sq = red16((a0[0] + a0[1]) + (a0[2] + a0[3]), (a1[0] + a1[1]) + (a1[2] + a1[3]));
      const unsigned d = pk2(sq, dppf<0x101>(sq));
      const unsigned d2 = dppu<0x102>(d);
      if (lane < 32 && (lane & 3) == 0) { v4u o = {d, oldq.y + 1u, d2, oldq.w + 1u}; st16(rs, sqo, o); }
      {
        const int cb = S3_OFF + (head * 32 + cs * 8) * S3R + (t - 256) * 16;
        stclr(rs, cb, sv4); stclr(rs, cb + 4096, sv4);
        if (wave == 4) stclr(rs, cb + 8192, sv4);
        else if (wave == 5 && lane < 8) stclr(rs, S4_OFF + head * 512 + cs * 128 + lane * 16, sv4);
      }
      VMWAIT0();
      bar();
      if (lane < 32 && (lane & 3) == 0) { v4u o = {d, oldq.y + 2u, d2, oldq.w + 2u}; st16(rs, sqo, o); }
    }
    {
      f32x4 a = {0, 0, 0, 0}; bf16x8 qf[4];
#pragma unroll
      for (int i = 0; i < 4; i++) qf[i] = *(const LAS bf16x8*)(qst + (n & 3) * 272 + i * 64 + q * 16);
      SB();
      f32x4 ab = {0, 0, 0, 0};
      a = MFMA(wk[0], qf[0], a); ab = MFMA(wk[1], qf[1], ab); a = MFMA(wk[2], qf[2], a); ab = MFMA(wk[3], qf[3], ab);
      a += ab;
#pragma unroll
      for (int i = 0; i < 4; i++) { a[i] += dppf<0xB1>(a[i]); a[i] += dppf<0x4E>(a[i]); }
      const float SC = 0.08838834764831845f * 1.4426950408889634f;
      v2u o = {canon(pk2(a[0] * SC, a[1] * SC)), canon(pk2(a[2] * SC, a[3] * SC))};
      if (n == 0) *(LAS v2u*)(q16 + 64 + wave * 8 + q * 2) = o;
    }
    bar();
    if (wave < 2) { const v4u o = *(const LAS v4u*)(q16 + 64 + n * 4); st16(rs, S2_OFF + (wave * 4 + q) * 32768 + head * 1024 + cs * 256 + n * 16, o); }
    bar();
    {
      const int kj = r & 7, cg = xc * 2 + (r >> 3);
      const u16* pl = w_mod + (size_t)(kj * 64 + (t >> 4)) * 3072 + 1024 + cg * 128 + n * 8;
      v4u w0 = LDNT(v4u, pl), w1r = LDNT(v4u, pl + (size_t)32 * 3072);
      CBAR();
      if (r < 8) {
#pragma unroll
        for (int i = 0; i < 8; i++) {
          const int rg = 8 * i + wave, row = 8 * rg + (lane >> 3);
          glds16(w_o + (size_t)(xc * 512 + row) * 512 + r * 64 + (((lane & 7) ^ ((row & 2) | ((row >> 1) & 4))) * 8), (unsigned)(rg * 1024));
        }
        asm volatile("s_waitcnt vmcnt(8)" ::: "memory");
      }
      const float c0 = bflo(cvl0), c1 = bflo(cvl1);
      const unsigned s01 = pk2(c0 * __builtin_amdgcn_rcpf(1.f + __expf(-c0)), c1 * __builtin_amdgcn_rcpf(1.f + __expf(-c1)));
      interleave(w0, w1r);
      float a[8] = {dot2(w0.x, s01, 0.f), dot2(w0.y, s01, 0.f), dot2(w0.z, s01, 0.f), dot2(w0.w, s01, 0.f),
                    dot2(w1r.x, s01, 0.f), dot2(w1r.y, s01, 0.f), dot2(w1r.z, s01, 0.f), dot2(w1r.w, s01, 0.f)};
      float b0 = red32(a[0], a[4]), b1x = red32(a[1], a[5]), b2x = red32(a[2], a[6]), b3 = red32(a[3], a[7]);
      f32x2 o = {red16(b0, b2x), red16(b1x, b3)};
      LAS float* apart = (LAS float*)(SM + 12288);
      *(LAS f32x2*)(apart + wave * 128 + n * 8 + 4 * (lane >> 5) + 2 * ((lane >> 4) & 1)) = o;
      bar();
      if (wave == 7) {
        f32x2 sacc = {0.f, 0.f};
#pragma unroll
        for (int w = 0; w < 8; w++) sacc += *(LAS f32x2*)(apart + w * 128 + lane * 2);
        const unsigned d = canon(pk2(sacc.x, sacc.y));
        v4u o4 = {dppu<0x00>(d), dppu<0x55>(d), dppu<0xAA>(d), dppu<0xFF>(d)};
        if ((lane & 3) == 0) st16(rs, SL_OFF + kj * 4096 + cg * 256 + lane * 4, o4);
        if (cg < 4 || cg >= 12) {
          const unsigned f0 = canon(__float_as_uint(sacc.x)), f1 = canon(__float_as_uint(sacc.y));
          v4u of = {f0, f1, dppu<0x101>(f0), dppu<0x101>(f1)};
          if (!(lane & 1)) st16(rs, SLF_OFF + kj * 4096 + (cg < 4 ? cg : cg - 8) * 512 + lane * 8, of);
        }
      }
      bar();
    }
    if (r < 8) {
      LAS float* fpart = (LAS float*)(SM + 3072);
      LAS unsigned char* cst = SM + 4096;
      LAS float* fp64 = (LAS float*)(SM + 16384);
      VMWAIT0();
      bar();
      bf16x8 wof[8];
      {
        const int g = lane >> 4, qq = (lane & 15) >> 2, pp = lane & 3;
#pragma unroll
        for (int i = 0; i < 8; i++) {
          s16x4 h[2];
#pragma unroll
          for (int tt = 0; tt < 2; tt++) {
            const int row = 32 * (2 * wave + (i >> 2)) + 8 * g + 4 * tt + qq;
            const int ch = (2 * (i & 3) + (pp >> 1)) ^ ((row & 2) | ((row >> 1) & 4));
            h[tt] = __builtin_amdgcn_ds_read_tr16_b64_v4i16((LAS s16x4*)(L + row * 128 + ch * 16 + (pp & 1) * 8));
          }
          wof[i] = (bf16x8){h[0].x, h[0].y, h[0].z, h[0].w, h[1].x, h[1].y, h[1].z, h[1].w};
        }
      }
      if (wave >= 4) {
        int spins = 0;
        for (;;) { CBAR(); v4u v = ld16(rs, S2_OFF + xc * 32768 + xc * 4096 + (t - 256) * 16); if (__all(mx4(v) != SENT) || ++spins > SPIN_CAP) break; __builtin_amdgcn_s_sleep(2); }
      }
      bar();
      {
        v4u av0, av1; int spins = 0;
        const int off = S4_OFF + ((4 * xc + (wave >> 1)) * 2 + (n & 1)) * 256 + (wave & 1) * 128 + q * 16;
        for (;;) { CBAR(); av0 = ld16(rs, off); av1 = ld16(rs, off + 64); if (__all(max(mx4(av0), mx4(av1)) != SENT) || ++spins > SPIN_CAP) break; }
        f32x4 a[4];
#pragma unroll
        for (int T = 0; T < 4; T++) { a[T] = MFMA(__builtin_bit_cast(bf16x8, av0), wof[T], ((f32x4){0, 0, 0, 0})); a[T] = MFMA(__builtin_bit_cast(bf16x8, av1), wof[4 + T], a[T]); }
        const f32x4 sel = q == 0 ? a[0] : q == 1 ? a[1] : q == 2 ? a[2] : a[3];
        fp64[wave * 64 + lane] = sel[0] + sel[1];
        bar();
        if (wave == 0) {
          float v = 0.f;
#pragma unroll
          for (int w = 0; w < 8; w++) v += fp64[w * 64 + lane];
          const unsigned d = canon(pk2(v, dppf<0x101>(v)));
          if (!(lane & 1)) ((LAS unsigned*)cst)[lane >> 1] = d;
          LGWAIT0();
          const v4u o = *(const LAS v4u*)(cst + (lane & 7) * 16);
          st16(rs, S5_OFF + (lane >> 3) * 8192 + xc * 1024 + r * 128 + (lane & 7) * 16, o);
        }
      }
    } else {
      const int ti = xc * 8 + (r - 8);
      LAS unsigned char* W2I = L;
      const float b2v = bflo(b2[ti * 8 + (lane & 7)]);
      const float x8 = bflo(x[ti * 8 + (lane & 7)]);
      CBAR();
#pragma unroll
      for (int i = 0; i < 3; i++) { const int rg = 8 * i + wave; if (rg < 22) { int row = rg * 64 + lane; row = row < 1376 ? row : 1375; dma16n(w2 + (size_t)row * 512 + ti * 8, W2I + rg * 1024); } }
      CBAR();
      LAS float* hpart = (LAS float*)(SM + 2048);
      LAS unsigned* sbuf = (LAS unsigned*)(SM + 2560);
      VMWAIT0();
      bar();
      bf16x8 w2f[6];
#pragma unroll
      for (int i = 0; i < 6; i++) {
        const int ks = wave + 8 * i < 43 ? wave + 8 * i : 0;
        const int g = lane >> 4, qq = (lane & 15) >> 2, pp = lane & 3;
        s16x4 h0 = __builtin_amdgcn_ds_read_tr16_b64_v4i16((LAS s16x4*)(W2I + (32 * ks + 8 * g + qq) * 16 + (pp & 1) * 8));
        s16x4 h1 = __builtin_amdgcn_ds_read_tr16_b64_v4i16((LAS s16x4*)(W2I + (32 * ks + 8 * g + 4 + qq) * 16 + (pp & 1) * 8));
        w2f[i] = (bf16x8){h0.x, h0.y, h0.z, h0.w, h1.x, h1.y, h1.z, h1.w};
      }
      float xn8 = 0.f, gbv = 0.f;
      if (wave == 0) {
        const int off = lane < 16 ? S5_OFF + xc * 8192 + (lane & 7) * 1024 + ti * 16 : SLF_OFF + ((lane >> 1) & 7) * 4096 + ((lane & 32) ? 2048 : 0) + ti * 32 + (lane & 1) * 16;
        v4u v; int spins = 0;
        for (;;) { CBAR(); v = ld16(rs, off); if (__all(mx4(v) != SENT) || ++spins > SPIN_CAP) break; __builtin_amdgcn_s_sleep(4); }
        if (lane < 48) *(LAS v4u*)(sbuf + lane * 4) = v;
        LGWAIT0();
        const LAS u16* sh = (const LAS u16*)sbuf; const LAS float* sf = (const LAS float*)sbuf;
        float ao = 0, ga8 = 0;
#pragma unroll
        for (int k = 0; k < 8; k++) { ao += bflo(sh[k * 8 + (lane & 7)]); ga8 += sf[64 + k * 8 + (lane & 7)]; gbv += sf[128 + k * 8 + (lane & 7)]; }
        xn8 = x8 + ga8 * ao;
      }
      bar();
      {
        v4u hv[6]; int spins = 0;
        const int off = S6_OFF + xc * 6144 + wave * 128 + q * 16, off5 = wave < 3 ? off + 5120 : off;
        for (;;) {
          CBAR();
#pragma unroll
          for (int i = 0; i < 5; i++) hv[i] = ld16(rs, off + i * 1024);
          hv[5] = ld16(rs, off5);
          const unsigned m = max(max(max(mx4(hv[0]), mx4(hv[1])), max(mx4(hv[2]), mx4(hv[3]))), max(mx4(hv[4]), mx4(hv[5])));
          if (__all(m != SENT) || ++spins > SPIN_CAP) break;
        }
        if (wave >= 3) hv[5] = (v4u){0, 0, 0, 0};
        f32x4 a = {0, 0, 0, 0}, a2 = {0, 0, 0, 0};
#pragma unroll
        for (int i = 0; i < 3; i++) { a = MFMA(__builtin_bit_cast(bf16x8, hv[2 * i]), w2f[2 * i], a); a2 = MFMA(__builtin_bit_cast(bf16x8, hv[2 * i + 1]), w2f[2 * i + 1], a2); }
        if (lane < 8) hpart[wave * 8 + lane] = a[0] + a2[0];
      }
      bar();
      if (wave == 0) {
        float a = hpart[lane];
        a += dppf<0x128>(a); a = red16(a, a); a = red32(a, a);
        const float res = xn8 + gbv * (a + b2v);
        if (lane < 8) out[ti * 8 + lane] = __builtin_bit_cast(u16, (__bf16)res);
      }
    }
  } else if (r >= 24) {
    const int e = r - 24, head = 4 * xc + (e >> 1), half = e & 1, ti = xc * 8 + e;
    const int gi = e * 8 + xc;
    const int wst = (gi < 43 ? gi : 0) * 4;
    const int mcol = wave * 64 + 8 * (lane & 7) + 4 * (lane >> 5) + 2 * ((lane >> 4) & 1) + ((lane >> 3) & 1);
    LAS unsigned char* W1I = L + 65536;
    LAS unsigned char* STG = L + 98304;
    LAS u16* scl = (LAS u16*)(SM);
    LAS unsigned* cc16 = (LAS unsigned*)(SM + 1024);
    LAS unsigned* ml = (LAS unsigned*)(SM + 2048);
    LAS float* stat = (LAS float*)(SM + 5120);
    LAS u16* y16 = (LAS u16*)(SM + 5376);
    LAS float* gpart = (LAS float*)(SM + 6400);
    LAS unsigned* pub = (LAS unsigned*)(SM + 7424);
    LAS unsigned* cnt = (LAS unsigned*)(SM + 7680);
    LAS unsigned* tbase = (LAS unsigned*)(SM + 8192);
    LAS float* apart = (LAS float*)(SM + 10240);
    LAS float* fpart = (LAS float*)(SM + 14336);
    LAS unsigned char* cst = SM + 15360;
    const int puoff = SU_OFF + q * 16384 + (xc * 8 + e) * 256 + n * 16;
    const int pfoff = SF_OFF + n * 8192 + ti * 128;
    const int sfoff = SF_OFF + (8 + xc) * 8192 + lane * 128;
#define LDSADD(p) __hip_atomic_fetch_add((p), 1u, __ATOMIC_RELAXED, __HIP_MEMORY_SCOPE_WORKGROUP)
#define REPUB() do { v2u p2_ = *(LAS v2u*)(pub + n * 2); v4u tb_ = *(LAS v4u*)(tbase + lane * 8); unsigned tf_ = tbase[lane * 8 + 4]; unsigned k_ = 0; if (lane == 0) k_ = LDSADD(cnt); k_ = __builtin_amdgcn_readfirstlane(k_); \
      v4u o_ = {p2_.x, tb_.x + k_, p2_.y, tb_.y + k_}; st16(rs, puoff, o_); v4u o2_ = {p2_.x, tb_.z + k_, p2_.y, tb_.w + k_}; st16(rs, puoff + 65536, o2_); \
      if (lane < 16) { v2u f_ = {0u, tf_ + k_}; st8(rs, pfoff, f_); } } while (0)
    v2u pd = {0, 0};
    v4u old0 = {0, 0, 0, 0}, old1 = {0, 0, 0, 0}, xv = {0, 0, 0, 0}; unsigned snapf = 0, xw = 0;
    v4u wm[2]; unsigned short cv[2];
    {
      const u16* p = w_mod + (size_t)(e * 64 + (t >> 4)) * 3072 + (n >> 3) * 512 + xc * 64 + (n & 7) * 8;
      wm[0] = LDNT(v4u, p); wm[1] = LDNT(v4u, p + (size_t)32 * 3072);
      cv[0] = cvec[e * 64 + (t >> 4)]; cv[1] = cvec[e * 64 + 32 + (t >> 4)];
    }
    CBAR();
    const unsigned oldf = ld8(rs, pfoff).y;
    if (wave == 0) { old0 = ld16(rs, puoff); old1 = ld16(rs, puoff + 65536); xv = *(const v4u*)(x + lane * 8); xw = ((const unsigned*)x)[xc * 32 + (lane & 31)]; }
    if (wave == 1) snapf = ld8(rs, sfoff).y;
    CBAR();
    {
      const int pc = t >= 64 ? t - 64 : t;
      st16(rs, wave == 0 ? DUMMY_OFF + ti * 1024 + lane * 16 : S2_OFF + ti * (EGC_PIECES * 16) + (pc < EGC_PIECES ? pc : pc - EGC_PIECES) * 16, sv4);
    }
    CBAR();
    float ln0 = 0.f, ln1 = 0.f;
    if (wave == 0) {
      float xs[8] = {bflo(xv.x), bfhi(xv.x), bflo(xv.y), bfhi(xv.y), bflo(xv.z), bfhi(xv.z), bflo(xv.w), bfhi(xv.w)};
      float s = 0;
#pragma unroll
      for (int i = 0; i < 8; i++) s += xs[i];
      const float mean = wave_sum(s) * (1.f / 512.f);
      float vs = 0;
#pragma unroll
      for (int i = 0; i < 8; i++) { float d = xs[i] - mean; vs += d * d; }
      const float rstd = rsqrtf(wave_sum(vs) * (1.f / 512.f) + 1e-5f);
      ln0 = (bflo(xw) - mean) * rstd; ln1 = (bfhi(xw) - mean) * rstd;
      v4u tb = {old0.y, old0.w, old1.y, old1.w};
      *(LAS v4u*)(tbase + lane * 8) = tb; tbase[lane * 8 + 4] = oldf;
      if (lane == 0) { cnt[0] = 2u + NREP; cnt[1] = 0u; }
    }
    CBAR();
    {
      const float c0 = bflo(cv[0]), c1 = bflo(cv[1]);
      const unsigned s01 = pk2(c0 * __builtin_amdgcn_rcpf(1.f + __expf(-c0)), c1 * __builtin_amdgcn_rcpf(1.f + __expf(-c1)));
      interleave(wm[0], wm[1]);
      float a[8] = {dot2(wm[0].x, s01, 0.f), dot2(wm[0].y, s01, 0.f), dot2(wm[0].z, s01, 0.f), dot2(wm[0].w, s01, 0.f),
                    dot2(wm[1].x, s01, 0.f), dot2(wm[1].y, s01, 0.f), dot2(wm[1].z, s01, 0.f), dot2(wm[1].w, s01, 0.f)};
      float b0 = red32(a[0], a[4]), b1x = red32(a[1], a[5]), b2x = red32(a[2], a[6]), b3 = red32(a[3], a[7]);
      f32x2 o = {red16(b0, b2x), red16(b1x, b3)};
      *(LAS f32x2*)(apart + wave * 128 + n * 8 + 4 * (lane >> 5) + 2 * ((lane >> 4) & 1)) = o;
    }
    bar();
    if (wave == 0) {
      f32x2 sacc = {0.f, 0.f};
#pragma unroll
      for (int w = 0; w < 8; w++) sacc += *(LAS f32x2*)(apart + w * 128 + lane * 2);
      auto r0 = __builtin_amdgcn_permlane32_swap(__float_as_uint(sacc.x), __float_as_uint(sacc.x), false, false);
      auto r1 = __builtin_amdgcn_permlane32_swap(__float_as_uint(sacc.y), __float_as_uint(sacc.y), false, false);
      const float one = e == 0 ? 1.f : 0.f;
      const float u0 = ln0 * (one + __uint_as_float(r0[1])) + __uint_as_float(r0[0]), u1 = ln1 * (one + __uint_as_float(r1[1])) + __uint_as_float(r1[0]);
      if (lane < 32) pub[lane] = pk2(u0, u1);
      LGWAIT0();
      pd = *(LAS v2u*)(pub + n * 2);
      v4u o = {pd.x, old0.y + 1u, pd.y, old0.w + 1u}; st16(rs, puoff, o);
      v4u o2 = {pd.x, old1.y + 1u, pd.y, old1.w + 1u}; st16(rs, puoff + 65536, o2);
    }
    unsigned karr = 0;
    if (wave != 0) { VMWAIT0(); if (lane == 0) karr = LDSADD(cnt + 1); }
    const bool lastw = __builtin_amdgcn_readfirstlane(karr) == 6u;
    if (lastw && lane < 16) { v2u f = {0u, oldf + 1u}; st8(rs, pfoff, f); }
#pragma unroll
    for (int i = 0; i < 4; i++) if (gi < 43) { const int rg = 8 * i + wave; dma16n(w1 + (size_t)(rg * 16 + (lane >> 2)) * 1376 + wst * 8 + (lane & 3) * 8, W1I + rg * 1024); }
    CBAR();
    DELAY(EGL_DELAY);
    const float xme = bflo(x[mcol]);
    const float b1v = bflo(b1[wst * 8 + (lane & 31)]);
    CBAR();
#pragma unroll
    for (int i = 0; i < 8; i++) {
      const int rg = 8 * i + wave, row = 4 * rg + q;
      dma16(w_uv + (size_t)(half * 256 + row) * 4096 + head * 128 + ((n ^ swz(row)) * 8), L + rg * 1024);
    }
    CBAR();
    if (wave == 0 || lastw) {
#pragma unroll 1
      for (int k = 0; k < NREP; k++) {
        __builtin_amdgcn_s_sleep(14);
        if (wave == 0) { v4u o_ = {pd.x, old0.y + 2u + k, pd.y, old0.w + 2u + k}; st16(rs, puoff, o_); v4u o2_ = {pd.x, old1.y + 2u + k, pd.y, old1.w + 2u + k}; st16(rs, puoff + 65536, o2_); }
        if (lastw && lane < 16) { v2u f_ = {0u, oldf + 2u + k}; st8(rs, pfoff, f_); }
      }
    }
    VMWAIT0();
    bar();
    bf16x8 wvf[16], w1f[4];
#pragma unroll
    for (int s2 = 0; s2 < 16; s2++) wvf[s2] = trfrag<256, true>(L, 64 * (wave & 3) + 32 * (s2 >> 3), 2 * (s2 & 7), lane);
#pragma unroll
    for (int i = 0; i < 4; i++) w1f[i] = trfrag<64, false>(W1I, 64 * wave + 32 * (i >> 1), 2 * (i & 1), lane);
    if (wave == 1) {
      int spins = 0; bool ok = false;
      for (;;) { CBAR(); if (!ok) ok = ld8(rs, sfoff).y != snapf; if (__all(ok) || ++spins > SPIN_CAP) break; if ((spins & 3) == 0) REPUB(); __builtin_amdgcn_s_sleep(2); }
      spins = 0;
      for (;;) { CBAR(); v4u v = ld16(rs, S2_OFF + xc * 32768 + head * 1024 + lane * 16); if (__all(mx4(v) != SENT) || ++spins > SPIN_CAP) break; if ((spins & 3) == 0) REPUB(); __builtin_amdgcn_s_sleep(2); }
    }
    bar();
    {
      LAS float* sclf = (LAS float*)(SM);
      LAS float* epart = (LAS float*)(STG);
      {
        const int soff = S3_OFF + (head * 32 + (lane & 31)) * S3R + 1024;
        if (wave == 7) {
          v2u stt; int spins = 0;
          for (;;) { CBAR(); stt = ld8(rs, soff); if (__all(max(stt.x, stt.y) != SENT) || ++spins > SPIN_CAP) break; if ((spins & 63) == 0) REPUB(); }
          const float m = __uint_as_float(stt.x), l = __uint_as_float(stt.y);
          const float mg = wave_max(m);
          const float sc = __builtin_amdgcn_exp2f(m - mg);
          const float il = 2.f * __builtin_amdgcn_rcpf(wave_sum(sc * l));
          if (lane < 32) sclf[lane] = sc * il;
        }
      }
      v4u p0, p1; int spins = 0;
      const int off = S3_OFF + (head * 32 + (wave >> 2) * 16 + n) * S3R + half * 512 + (wave & 3) * 128 + q * 16;
      for (;;) {
        CBAR();
        p0 = ld16(rs, off); p1 = ld16(rs, off + 64);
        if (__all(max(mx4(p0), mx4(p1)) != SENT) || ++spins > SPIN_CAP) break;
        if ((spins & 63) == 0) REPUB();
      }
      f32x4 y0[8];
#pragma unroll
      for (int T = 0; T < 8; T++) { y0[T] = MFMA(__builtin_bit_cast(bf16x8, p0), wvf[T], ((f32x4){0, 0, 0, 0})); y0[T] = MFMA(__builtin_bit_cast(bf16x8, p1), wvf[8 + T], y0[T]); }
      bar();
      const f32x4 s4 = *(const LAS f32x4*)(sclf + 16 * (wave >> 2) + 4 * q);
      float z[8];
#pragma unroll
      for (int T = 0; T < 8; T++) z[T] = (y0[T][0] * s4[0] + y0[T][1] * s4[1]) + (y0[T][2] * s4[2] + y0[T][3] * s4[3]);
      const float r0 = red32(z[0], z[4]), r1 = red32(z[1], z[5]), r2 = red32(z[2], z[6]), r3 = red32(z[3], z[7]);
      const float u0 = red16(r0, r2), u1 = red16(r1, r3);
      const int T0 = 4 * (lane >> 5) + 2 * ((lane >> 4) & 1);
      epart[wave * 128 + T0 * 16 + n] = u0; epart[wave * 128 + T0 * 16 + 16 + n] = u1;
      bar();
      if (wave == 0) {
        f32x2 sacc = {0.f, 0.f};
#pragma unroll
        for (int w = 0; w < 8; w++) sacc += *(LAS f32x2*)(epart + w * 128 + lane * 2);
        const unsigned d = canon(pk2(sacc.x, sacc.y));
        v4u o4 = {dppu<0x00>(d), dppu<0x55>(d), dppu<0xAA>(d), dppu<0xFF>(d)};
        if ((lane & 3) == 0) st16(rs, S4_OFF + (head * 2 + half) * 256 + lane * 4, o4);
      }
    }
    if (gi >= 43) {
      if (wave == 0) {
        int spins = 0;
        for (;;) {
          CBAR(); v4u v = ld16(rs, DN_OFF + xc * 128);
          if (v.x != SENT || ++spins > SPIN_CAP) break;
          if ((spins & 3) == 0) REPUB();
          __builtin_amdgcn_s_sleep(8);
        }
      } return;
    }
    {
      v4u v0, v1, v2; int spins = 0;
      const int off = SL_OFF + wave * 4096 + lane * 16;
      for (;;) { CBAR(); v0 = ld16(rs, off); v1 = ld16(rs, off + 1024); v2 = ld16(rs, off + 2048); if (__all(max(max(mx4(v0), mx4(v1)), mx4(v2)) != SENT) || ++spins > SPIN_CAP) break; if ((spins & 63) == 0) REPUB(); __builtin_amdgcn_s_sleep(1); }
      *(LAS v4u*)(L + wave * 3072 + lane * 16) = v0; *(LAS v4u*)(L + wave * 3072 + 1024 + lane * 16) = v1; *(LAS v4u*)(L + wave * 3072 + 2048 + lane * 16) = v2;
    }
    bar();
    float gam = 0.f, sbm = 0.f, cb1 = 0.f;
    {
      const LAS u16* mlh = (const LAS u16*)L;
#pragma unroll
      for (int k = 0; k < 8; k++) { gam += bflo(mlh[k * 1536 + mcol]); sbm += bflo(mlh[k * 1536 + 512 + mcol]); cb1 += bflo(mlh[k * 1536 + 1024 + mcol]); }
      sbm = rbf(sbm); cb1 = rbf(1.f + cb1);
    }
    LAS u16* cs16 = (LAS u16*)(SM + 10240);
    LAS float* gp64 = (LAS float*)(SM + 12288);
    cs16[mcol] = __builtin_bit_cast(u16, (__bf16)cb1); cs16[512 + mcol] = __builtin_bit_cast(u16, (__bf16)sbm);
    LGWAIT0();
    bf16x8 auxf[2];
    {
      const bf16x8 ones = {0x3F80, 0x3F80, 0x3F80, 0x3F80, 0x3F80, 0x3F80, 0x3F80, 0x3F80};
#pragma unroll
      for (int i = 0; i < 2; i++) { const bf16x8 v = *(const LAS bf16x8*)(cs16 + ((n - 1) & 1) * 512 + wave * 64 + i * 32 + q * 8); auxf[i] = n == 0 ? ones : v; }
      f32x4 k0 = MFMA(auxf[0], w1f[0], ((f32x4){0, 0, 0, 0})), k1 = MFMA(auxf[0], w1f[1], ((f32x4){0, 0, 0, 0})), kg = MFMA(auxf[0], auxf[0], ((f32x4){0, 0, 0, 0}));
      k0 = MFMA(auxf[1], w1f[2], k0); k1 = MFMA(auxf[1], w1f[3], k1); kg = MFMA(auxf[1], auxf[1], kg);
      if (lane < 16) {
        LAS float* g = (LAS float*)(L + 32768) + wave * 192;
        g[n] = k0[0]; g[16 + n] = k1[0]; g[32 + n] = kg[0]; g[64 + n] = k0[1]; g[80 + n] = k1[1]; g[96 + n] = kg[1]; g[128 + n] = k0[2]; g[144 + n] = k1[2]; g[160 + n] = kg[2];
      }
    }
    bar();
    float Ow = 0.f, Cw = 0.f, Sw = 0.f, Kc = 0.f, Ks = 0.f, Kc2 = 0.f, Kcs = 0.f, Ks2 = 0.f;
    if (wave == 0) {
      const LAS float* g = (const LAS float*)(L + 32768);
      float o = 0.f, c = 0.f, sg = 0.f;
#pragma unroll
      for (int w = 0; w < 8; w++) { o += g[w * 192 + (lane < 48 ? lane : 0)]; c += g[w * 192 + 64 + (lane < 48 ? lane : 0)]; sg += g[w * 192 + 128 + (lane < 48 ? lane : 0)]; }
      Ow = o; Cw = c; Sw = sg;
      Kc = RL(o, 33); Ks = RL(o, 34); Kc2 = RL(c, 33); Kcs = RL(c, 34); Ks2 = RL(sg, 34);
    }
    asm volatile("" : "+v"(gam), "+v"(cb1), "+v"(Ow), "+v"(Cw), "+v"(Sw));
    {
      v4u v; int spins = 0;
      const int off = S5_OFF + xc * 8192 + (lane >> 3) * 1024 + wave * 128 + (lane & 7) * 16;
      for (;;) { CBAR(); v = ld16(rs, off); if (__all(mx4(v) != SENT) || ++spins > SPIN_CAP) break; if ((spins & 63) == 0) REPUB(); }
      const float b0 = red32(bflo(v.x), bflo(v.z)), b1x = red32(bfhi(v.x), bfhi(v.z)), b2x = red32(bflo(v.y), bflo(v.w)), b3 = red32(bfhi(v.y), bfhi(v.w));
      const float c0 = red16(b0, b2x), c1 = red16(b1x, b3);
      const bool up = lane & 8;
      const float keep = up ? c1 : c0, send = up ? c0 : c1;
      const float ao = keep + dppf<0x128>(send);
      const __bf16 xb = (__bf16)(xme + gam * ao);
      cs16[mcol] = __builtin_bit_cast(u16, (__bf16)((float)xb * cb1)); cs16[512 + mcol] = __builtin_bit_cast(u16, xb);
      LGWAIT0();
      const bf16x8 y0 = *(const LAS bf16x8*)(cs16 + (n & 1) * 512 + wave * 64 + q * 8), y1 = *(const LAS bf16x8*)(cs16 + (n & 1) * 512 + wave * 64 + 32 + q * 8);
      f32x4 z0 = MFMA(y0, w1f[0], ((f32x4){0, 0, 0, 0})), z1 = MFMA(y0, w1f[1], ((f32x4){0, 0, 0, 0})), ax = MFMA(y0, auxf[0], ((f32x4){0, 0, 0, 0})), gr = MFMA(y0, y0, ((f32x4){0, 0, 0, 0}));
      z0 = MFMA(y1, w1f[2], z0); z1 = MFMA(y1, w1f[3], z1); ax = MFMA(y1, auxf[1], ax); gr = MFMA(y1, y1, gr);
      const float lastq = n == 0 ? ax[1] : n == 1 ? gr[1] : gr[0];
      gp64[wave * 64 + lane] = q == 0 ? z0[0] : q == 1 ? z1[0] : q == 2 ? ax[0] : lastq;
    }
    bar();
    if (wave == 0) {
      float a = 0;
#pragma unroll
      for (int w = 0; w < 8; w++) a += gp64[w * 64 + lane];
      const float B1 = RL(a, 32), B3 = RL(a, 33), B4 = RL(a, 34), A1 = RL(a, 48), A2 = RL(a, 49), B2 = RL(a, 50);
      const float mu = A1 * (1.f / 512.f), rr = __builtin_amdgcn_rsqf(fmaxf(A2 * (1.f / 512.f) - mu * mu, 0.f) + 1e-5f);
      const float m2 = (rr * (B1 - mu * Kc) + Ks) * (1.f / 512.f);
      const float e2 = (rr * rr * (B2 - 2.f * mu * B3 + mu * mu * Kc2) + 2.f * rr * (B4 - mu * Kcs) + Ks2) * (1.f / 512.f);
      const float r2 = __builtin_amdgcn_rsqf(fmaxf(e2 - m2 * m2, 0.f) + 1e-5f);
      const float zz = r2 * (rr * (a - mu * Cw) + Sw - m2 * Ow);
      const float z = zz + b1v;
      const float u = z * (0.7978845608028654f + 0.035677408136300125f * z * z);
      const float gl = z * __builtin_amdgcn_rcpf(1.f + __builtin_amdgcn_exp2f(-2.885390081777927f * u));
      const unsigned d = canon(pk2(gl, dppf<0x101>(gl)));
      if (lane < 32 && !(lane & 1)) ((LAS unsigned*)y16)[lane >> 1] = d;
      LGWAIT0();
      const v4u o = *(const LAS v4u*)((LAS unsigned*)y16 + (lane & 3) * 4);
      if (lane < 32) st16(rs, S6_OFF + (lane >> 2) * 6144 + gi * 128 + (lane & 3) * 16, o);
    }
    if (ti == 0 && wave == 1 && lane < 8) { v4u o = {1, 1, 1, 1}; st16(rs, DN_OFF + lane * 128, o); }
  } else {
    const int e = r - 16, split = 4 * xc + (e & 3), hg = e >> 2;
    const int sfoff = SF_OFF + (8 + xc) * 8192 + lane * 128;
    LAS unsigned char* qa = SM;
    LAS unsigned char* Pm = SM + 16896;
    LAS float* wmax = (LAS float*)(SM + 21504);
    LAS float* wsum = wmax + 128;
    unsigned snapf = 0;
    if (wave == 0) snapf = ld8(rs, sfoff).y;
    bar();
    CBAR();
#pragma unroll
    for (int i = 0; i < 16; i++) {
      const int key = ((i * 8 + wave) + hg * 64) & 127;
      dma16(ckv + (size_t)(split * 128 + key) * 512 + ((lane ^ swz(key)) * 8), L + key * 1024);
    }
    VMWAIT0();
    bar();
    bf16x8 ka[16], va[16];
    {
      const int key = wave * 16 + n, f2 = swz(key);
#pragma unroll
      for (int s2 = 0; s2 < 16; s2++) ka[s2] = *(const LAS bf16x8*)(L + key * 1024 + (((4 * s2 + q) ^ f2) * 16));
#pragma unroll
      for (int i = 0; i < 16; i++) va[i] = trfrag<1024, true>(L, 32 * (i & 3), wave * 8 + 2 * (i >> 2), lane);
    }
    if (wave == 0) {
      int spins = 0; bool ok = false;
      for (;;) { CBAR(); if (!ok) ok = ld8(rs, sfoff).y != snapf; if (__all(ok) || ++spins > SPIN_CAP) break; __builtin_amdgcn_s_sleep(1); }
    }
    bar();
    {
      const int off = S2_OFF + xc * 32768 + hg * 16384 + t * 16;
      v4u v0, v1; int spins = 0;
      for (;;) {
        CBAR();
        v0 = ld16(rs, off); v1 = ld16(rs, off + 8192);
        if (__all(max(mx4(v0), mx4(v1)) != SENT) || ++spins > SPIN_CAP) break;
      }
      *(LAS v4u*)(qa + wave * 1056 + lane * 16) = v0;
      *(LAS v4u*)(qa + (8 + wave) * 1056 + lane * 16) = v1;
    }
    bar();
    f32x4 sc = {0, 0, 0, 0};
    {
      bf16x8 qf[16];
#pragma unroll
      for (int s2 = 0; s2 < 16; s2++) qf[s2] = *(const LAS bf16x8*)(qa + n * 1056 + s2 * 64 + q * 16);
      SB();
      f32x4 sc2 = {0, 0, 0, 0};
#pragma unroll
      for (int s2 = 0; s2 < 8; s2++) { sc = MFMA(ka[2 * s2], qf[2 * s2], sc); sc2 = MFMA(ka[2 * s2 + 1], qf[2 * s2 + 1], sc2); }
      sc += sc2;
    }
    float mx = fmaxf(fmaxf(sc[0], sc[1]), fmaxf(sc[2], sc[3]));
    mx = max16(mx); mx = max32(mx);
    if (lane < 16) wmax[lane * 8 + wave] = mx;
    bar();
    {
      f32x4 m0 = *(LAS f32x4*)(wmax + n * 8), m1 = *(LAS f32x4*)(wmax + n * 8 + 4);
      mx = fmaxf(fmaxf(fmaxf(m0[0], m0[1]), fmaxf(m0[2], m0[3])), fmaxf(fmaxf(m1[0], m1[1]), fmaxf(m1[2], m1[3])));
    }
    const float p0 = __builtin_amdgcn_exp2f(sc[0] - mx), p1 = __builtin_amdgcn_exp2f(sc[1] - mx), p2 = __builtin_amdgcn_exp2f(sc[2] - mx), p3 = __builtin_amdgcn_exp2f(sc[3] - mx);
    { v2u o = {pk2(p0, p1), pk2(p2, p3)}; *(LAS v2u*)(Pm + n * 288 + (wave * 16 + q * 4) * 2) = o; }
    float ps = (p0 + p1) + (p2 + p3);
    ps = red16(ps, ps); ps = red32(ps, ps);
    if (lane < 16) wsum[lane * 8 + wave] = ps;
    bar();
    {
      if (wave == 7) {
        f32x4 s0 = *(LAS f32x4*)(wsum + n * 8), s1 = *(LAS f32x4*)(wsum + n * 8 + 4);
        const float l = ((s0[0] + s0[1]) + (s0[2] + s0[3])) + ((s1[0] + s1[1]) + (s1[2] + s1[3]));
        v2u o2 = {canon(__float_as_uint(mx)), canon(__float_as_uint(l))};
        if (lane < 16) st8(rs, S3_OFF + ((hg * 16 + lane) * 32 + split) * S3R + 1024, o2);
      }
      bf16x8 bp[4];
#pragma unroll
      for (int ks = 0; ks < 4; ks++) bp[ks] = *(const LAS bf16x8*)(Pm + n * 288 + ks * 64 + q * 16);
      SB();
      f32x4 acc[4];
#pragma unroll
      for (int T = 0; T < 4; T++) {
        acc[T] = (f32x4){0, 0, 0, 0};
#pragma unroll
        for (int ks = 0; ks < 4; ks++) acc[T] = MFMA(va[T * 4 + ks], bp[ks], acc[T]);
      }
      unsigned d0[4], d1[4];
#pragma unroll
      for (int T = 0; T < 4; T++) { d0[T] = canon(pk2(acc[T][0], acc[T][1])); d1[T] = canon(pk2(acc[T][2], acc[T][3])); }
      auto r0 = __builtin_amdgcn_permlane16_swap(d0[0], d0[1], false, false);
      auto r1 = __builtin_amdgcn_permlane16_swap(d1[0], d1[1], false, false);
      auto r2 = __builtin_amdgcn_permlane16_swap(d0[2], d0[3], false, false);
      auto r3 = __builtin_amdgcn_permlane16_swap(d1[2], d1[3], false, false);
      v4u oa = {r0[0], r1[0], r0[1], r1[1]}, ob = {r2[0], r3[0], r2[1], r3[1]};
      const v4u obs = {dppu<0x128>(ob.x), dppu<0x128>(ob.y), dppu<0x128>(ob.z), dppu<0x128>(ob.w)};
      const bool lo = n < 8;
      const int o3b = S3_OFF + ((hg * 16 + (n & 7)) * 32 + split) * S3R + (wave * 64 + (q & 1) * 16 + (q & 2) * 4) * 2;
      st16(rs, o3b + (lo ? 0 : 64), lo ? oa : obs);
      st16(rs, o3b + 8 * 32 * S3R + (lo ? 64 : 0), lo ? obs : oa);
    }
  }
}

extern "C" void kernel_launch(void* const* d_in, const int* in_sizes, int n_in, void* d_out, int out_size, void* d_ws, size_t ws_size, hipStream_t stream) {
  (void)in_sizes; (void)n_in; (void)out_size; (void)ws_size;
  (void)hipFuncSetAttribute((const void*)fused_kernel, hipFuncAttributeMaxDynamicSharedMemorySize, L_TOTAL);
  fused_kernel<<<256, NT, L_TOTAL, stream>>>((const u16*)d_in[0], (const u16*)d_in[1], (const u16*)d_in[2], (const u16*)d_in[3], (const u16*)d_in[4], (const u16*)d_in[5],
                                             (const u16*)d_in[7], (const u16*)d_in[8], (const u16*)d_in[9], (const u16*)d_in[10], (const u16*)d_in[11], (const u16*)d_in[12],
                                             (u16*)d_out, (unsigned char*)d_ws);
}
```

```cpp
#include <hip/hip_runtime.h>
#include <stdint.h>

typedef unsigned short u16;
typedef unsigned v4u __attribute__((ext_vector_type(4)));
typedef unsigned v2u __attribute__((ext_vector_type(2)));
typedef float f32x4 __attribute__((ext_vector_type(4)));
typedef float f32x2 __attribute__((ext_vector_type(2)));
typedef short bf16x8 __attribute__((ext_vector_type(8)));
typedef short s16x4 __attribute__((ext_vector_type(4)));
typedef __bf16 bf2 __attribute__((ext_vector_type(2)));
#define LAS __attribute__((address_space(3)))

constexpr unsigned SENT = 0xFFFFFFFFu;
constexpr int NT = 512;
constexpr int SPIN_CAP = 1 << 20;
#define NREP 2
#define EGL_DELAY 400
#define ROFF 24
#define CLRAUX 16
#define NTAUX 2
#define NTS " nt"
#define LDNT(T, p) __builtin_nontemporal_load((const T*)(p))
constexpr int SU_OFF = 0;
constexpr int SF_OFF = 131072;
constexpr int SQ_OFF = 262144;
constexpr int CLR_OFF = 327680;
constexpr int S2_OFF = CLR_OFF;
constexpr int S5_OFF = S2_OFF + 8 * 32768;
constexpr int S6_OFF = S5_OFF + 65536;
constexpr int SL_OFF = S6_OFF + 49152;
constexpr int SLF_OFF = SL_OFF + 32768;
constexpr int DN_OFF = SLF_OFF + 32768;
constexpr int EGC_END = DN_OFF + 4096;
constexpr int EGC_PIECES = (EGC_END - S2_OFF) / 16 / 64;
constexpr int S4_OFF = EGC_END;
constexpr int S3R = 1152;
constexpr int S3_OFF = S4_OFF + 16384;
constexpr int CLR_END = S3_OFF + 1024 * S3R;
constexpr int DUMMY_OFF = CLR_END;
constexpr int WS_USED = CLR_END + 65536;
constexpr int L_SM = 131072;
constexpr int L_TOTAL = 131072 + 30720;

__device__ __forceinline__ float bflo(unsigned w) { return __uint_as_float(w << 16); }
__device__ __forceinline__ float bfhi(unsigned w) { return __uint_as_float(w & 0xffff0000u); }
__device__ __forceinline__ unsigned pk2(float a, float b) { bf2 p; p.x = (__bf16)a; p.y = (__bf16)b; return __builtin_bit_cast(unsigned, p); }
__device__ __forceinline__ float rbf(float a) { return (float)(__bf16)a; }
__device__ __forceinline__ unsigned canon(unsigned w) { return w == SENT ? 0x7FC07FC0u : w; }
__device__ __forceinline__ float dot2(unsigned a, unsigned b, float c) { return __builtin_amdgcn_fdot2_f32_bf16(__builtin_bit_cast(bf2, a), __builtin_bit_cast(bf2, b), c, false); }
__device__ __forceinline__ void bar() { __builtin_amdgcn_sched_barrier(0); asm volatile("s_waitcnt lgkmcnt(0)\n\ts_barrier" ::: "memory"); __builtin_amdgcn_sched_barrier(0); }
template <int CTRL> __device__ __forceinline__ float dppf(float v) { return __int_as_float(__builtin_amdgcn_update_dpp(0, __float_as_int(v), CTRL, 0xf, 0xf, false)); }
template <int CTRL> __device__ __forceinline__ unsigned dppu(unsigned v) { return (unsigned)__builtin_amdgcn_update_dpp(0, (int)v, CTRL, 0xf, 0xf, false); }
__device__ __forceinline__ float red32(float a, float b) {
  auto r = __builtin_amdgcn_permlane32_swap(__float_as_uint(a), __float_as_uint(b), false, false);
  return __uint_as_float(r[0]) + __uint_as_float(r[1]);
}
__device__ __forceinline__ float red16(float a, float b) {
  auto r = __builtin_amdgcn_permlane16_swap(__float_as_uint(a), __float_as_uint(b), false, false);
  return __uint_as_float(r[0]) + __uint_as_float(r[1]);
}
__device__ __forceinline__ float max32(float a) { auto r = __builtin_amdgcn_permlane32_swap(__float_as_uint(a), __float_as_uint(a), false, false); return fmaxf(__uint_as_float(r[0]), __uint_as_float(r[1])); }
__device__ __forceinline__ float max16(float a) { auto r = __builtin_amdgcn_permlane16_swap(__float_as_uint(a), __float_as_uint(a), false, false); return fmaxf(__uint_as_float(r[0]), __uint_as_float(r[1])); }
__device__ __forceinline__ float wave_sum(float v) {
  v += dppf<0x128>(v); v += dppf<0x124>(v); v += dppf<0x122>(v); v += dppf<0x121>(v);
  v = red16(v, v); v = red32(v, v); return v;
}
__device__ __forceinline__ float wave_max(float v) {
  v = fmaxf(v, dppf<0x128>(v)); v = fmaxf(v, dppf<0x124>(v)); v = fmaxf(v, dppf<0x122>(v)); v = fmaxf(v, dppf<0x121>(v));
  v = max16(v); v = max32(v); return v;
}
__device__ __forceinline__ unsigned ilo(unsigned a, unsigned b) { return __builtin_amdgcn_perm(b, a, 0x05040100u); }
__device__ __forceinline__ unsigned ihi(unsigned a, unsigned b) { return __builtin_amdgcn_perm(b, a, 0x07060302u); }
__device__ __forceinline__ void interleave(v4u& a, v4u& b) {
  v4u x, y;
  x.x = ilo(a.x, b.x); x.y = ihi(a.x, b.x); x.z = ilo(a.y, b.y); x.w = ihi(a.y, b.y);
  y.x = ilo(a.z, b.z); y.y = ihi(a.z, b.z); y.z = ilo(a.w, b.w); y.w = ihi(a.w, b.w);
  a = x; b = y;
}
typedef __amdgpu_buffer_rsrc_t rsrc_t;
__device__ __forceinline__ v4u ld16(rsrc_t r, int off) { return __builtin_amdgcn_raw_buffer_load_b128(r, off, 0, 16); }
__device__ __forceinline__ v2u ld8(rsrc_t r, int off) { return __builtin_amdgcn_raw_buffer_load_b64(r, off, 0, 16); }
__device__ __forceinline__ void st16(rsrc_t r, int off, v4u v) { __builtin_amdgcn_raw_buffer_store_b128(v, r, off, 0, 16); }
__device__ __forceinline__ void st8(rsrc_t r, int off, v2u v) { __builtin_amdgcn_raw_buffer_store_b64(v, r, off, 0, 16); }
__device__ __forceinline__ void glds16(const void* g, unsigned lds_base) {
  unsigned sv; asm volatile("s_mov_b32 %0, m0\n\ts_mov_b32 m0, %2\n\ts_nop 0\n\tglobal_load_lds_dwordx4 %1, off" NTS "\n\ts_mov_b32 m0, %0" : "=&s"(sv) : "v"(g), "s"(lds_base) : "memory");
}
#define RL(v, l) __int_as_float(__builtin_amdgcn_readlane(__float_as_int(v), (l)))
#define SB() __builtin_amdgcn_sched_barrier(0)
#define CBAR() do { asm volatile("" ::: "memory"); __builtin_amdgcn_sched_barrier(0); } while (0)

__device__ __forceinline__ int swz(int row) { return ((row & 3) | ((row >> 1) & 4)) << 1; }
template <int AUX> __device__ __forceinline__ void dma16a(const void* g, LAS unsigned char* l) { __builtin_amdgcn_global_load_lds((const __attribute__((address_space(1))) unsigned*)g, (LAS unsigned*)l, 16, 0, AUX); }
#define dma16 dma16a<NTAUX>
#define NARROW 2
#define dma16n dma16a<NARROW>
#define LDNAR(T, p) __builtin_nontemporal_load((const T*)(p))
template <int RB, bool SW>
__device__ __forceinline__ bf16x8 trfrag(const LAS unsigned char* img, int k0, int ch0, int lane) {
  const int g = lane >> 4, qq = (lane & 15) >> 2, pp = lane & 3;
  s16x4 h[2];
#pragma unroll
  for (int tt = 0; tt < 2; tt++) {
    const int row = k0 + 8 * g + 4 * tt + qq;
    const int ch = SW ? ((ch0 + (pp >> 1)) ^ swz(row)) : (ch0 + (pp >> 1));
    h[tt] = __builtin_amdgcn_ds_read_tr16_b64_v4i16((LAS s16x4*)(img + row * RB + ch * 16 + (pp & 1) * 8));
  }
  return (bf16x8){h[0].x, h[0].y, h[0].z, h[0].w, h[1].x, h[1].y, h[1].z, h[1].w};
}
#define MFMA(a, b, c) __builtin_amdgcn_mfma_f32_16x16x32_bf16(a, b, c, 0, 0, 0)
#define VMWAIT0() do { asm volatile("" ::: "memory"); __builtin_amdgcn_s_waitcnt(0x0F70); asm volatile("s_waitcnt vmcnt(0)" ::: "memory"); } while (0)
#define LGWAIT0() asm volatile("s_waitcnt lgkmcnt(0)" ::: "memory")
typedef unsigned long long u64;
__device__ __forceinline__ unsigned mx4(v4u v) { return max(max(v.x, v.y), max(v.z, v.w)); }
__device__ __forceinline__ void stclr(rsrc_t r, int off, v4u v) { __builtin_amdgcn_raw_buffer_store_b128(v, r, off, 0, CLRAUX); }

__global__ void __launch_bounds__(NT, 2) fused_kernel(
    const u16* __restrict__ x, const u16* __restrict__ ckv, const u16* __restrict__ w_q, const u16* __restrict__ w_uk,
    const u16* __restrict__ w_uv, const u16* __restrict__ w_o, const u16* __restrict__ w1, const u16* __restrict__ b1,
    const u16* __restrict__ w2, const u16* __restrict__ b2, const u16* __restrict__ cvec, const u16* __restrict__ w_mod,
    u16* __restrict__ out, unsigned char* ws) {
  extern __shared__ __attribute__((aligned(16))) unsigned char lds_raw[];
  LAS unsigned char* L = (LAS unsigned char*)lds_raw;
  const int t = threadIdx.x, lane = t & 63, wave = __builtin_amdgcn_readfirstlane(t >> 6);
  const int b = blockIdx.x, xc = b & 7, r = ((b >> 3) + ROFF) & 31;
  const int n = lane & 15, q = lane >> 4;
  const rsrc_t rs = __builtin_amdgcn_make_buffer_rsrc((void*)ws, 0, WS_USED, 0x00020000);
  LAS unsigned char* SM = L + L_SM;
  const v4u sv4 = {SENT, SENT, SENT, SENT};
  const u64 tst = __builtin_amdgcn_s_memrealtime();
  (void)tst;
#define DELAY(x) do { while (__builtin_amdgcn_s_memrealtime() - tst < (x)) __builtin_amdgcn_s_sleep(2); } while (0)

  if (r < 16) {
    const int head = 4 * xc + (r >> 2), cs = r & 3;
    LAS unsigned char* hp = SM;
    LAS unsigned* q16 = (LAS unsigned*)(SM + 2304);
    LAS unsigned char* qst = SM + 8192;
    LAS unsigned char* WKI = L + 98304;
    LAS unsigned char* WQI = L + 65536;
    const int sfoff = SF_OFF + xc * 8192 + lane * 128;
    LAS unsigned* lcnt = (LAS unsigned*)(SM + 9472);
    const unsigned short cvl0 = cvec[(r & 7) * 64 + (t >> 4)], cvl1 = cvec[(r & 7) * 64 + 32 + (t >> 4)];
    bf16x8 wk[4];
    if (wave < 4) {
      const int suoff = SU_OFF + xc * 16384 + cs * 4096 + t * 16;
      const int sqoff = SQ_OFF + head * 2048 + (t & 127) * 16;
      const v4u snapu = ld16(rs, suoff);
      const v4u snapq = ld16(rs, sqoff);
      const unsigned snapf = ld8(rs, sfoff).y;
      if (t == 0) *lcnt = 0u;
      bar();
      v4u v = snapu; int spins = 0; bool ok = false;
      for (;;) {
        CBAR();
        if (!ok) v = ld16(rs, suoff);
        ok = (v.y != snapu.y) & (v.w != snapu.w);
        if (__all(ok) || ++spins > SPIN_CAP) break;
      }
      v2u hh = {v.x, v.z};
      *(LAS v2u*)(hp + ((t >> 4) & 7) * 272 + (t >> 7) * 128 + (t & 15) * 8) = hh;
      bar();
#pragma unroll
      for (int i = 0; i < 4; i++) wk[i] = *(const LAS bf16x8*)(WKI + (wave * 16 + n) * 256 + (((4 * i + q) ^ swz(n)) * 16));
      if (wave < 2) {
        spins = 0; ok = false;
        for (;;) {
          CBAR();
          if (!ok) v = ld16(rs, sqoff);
          ok = (v.y != snapq.y) & (v.w != snapq.w);
          if (__all(ok) || ++spins > SPIN_CAP) break;
        }
        v2u o2 = {v.x, v.z};
        *(LAS v2u*)(qst + (t >> 5) * 272 + (t & 31) * 8) = o2;
      } else if (wave == 2) {
        spins = 0; ok = false;
        for (;;) { CBAR(); if (!ok) ok = ld8(rs, sfoff).y != snapf; if (__all(ok) || ++spins > SPIN_CAP) break; }
      }
      bar();
    } else {
      const int lw = wave - 4;
      const int sqo = SQ_OFF + head * 2048 + cs * 512 + lw * 128 + ((lane >> 2) & 7) * 16;
      const v4u oldq = ld16(rs, sqo);
      bar();
#pragma unroll
      for (int i = 0; i < 16; i++) {
        const int j = 4 * i + lw, rg = j & 31, row = 4 * rg + q;
        const u16* src = (i < 8 ? w_q : w_uk) + (size_t)(cs * 128 + row) * 4096 + head * 128 + ((n ^ swz(row)) * 8);
        dma16(src, L + 65536 + j * 1024);
      }
      VMWAIT0();
      if (lane == 0) __hip_atomic_fetch_add(lcnt, 1u, __ATOMIC_RELAXED, __HIP_MEMORY_SCOPE_WORKGROUP);
      { int spins = 0; while (__hip_atomic_load(lcnt, __ATOMIC_RELAXED, __HIP_MEMORY_SCOPE_WORKGROUP) < 4u && ++spins < SPIN_CAP) {} }
      CBAR();
      bf16x8 wqf[8];
#pragma unroll
      for (int i = 0; i < 8; i++) wqf[i] = trfrag<256, true>(WQI, 32 * (i & 3), 4 * lw + 2 * (i >> 2), lane);
#pragma unroll
      for (int i = 0; i < 4; i++) wk[i] = *(const LAS bf16x8*)(WKI + (wave * 16 + n) * 256 + (((4 * i + q) ^ swz(n)) * 16));
      bar();
      f32x4 a0 = {0, 0, 0, 0}, a1 = {0, 0, 0, 0}; bf16x8 hf[4];
#pragma unroll
      for (int i = 0; i < 4; i++) hf[i] = *(const LAS bf16x8*)(hp + (n & 7) * 272 + i * 64 + q * 16);
      SB();
#pragma unroll
      for (int i = 0; i < 4; i++) { a0 = MFMA(hf[i], wqf[i], a0); a1 = MFMA(hf[i], wqf[4 + i], a1); }
      const float sq = red16((a0[0] + a0[1]) + (a0[2] + a0[3]), (a1[0] + a1[1]) + (a1[2] + a1[3]));
      const unsigned d = pk2(sq, dppf<0x101>(sq));
      const unsigned d2 = dppu<0x102>(d);
      if (lane < 32 && (lane & 3) == 0) { v4u o = {d, oldq.y + 1u, d2, oldq.w + 1u}; st16(rs, sqo, o); }
      {
        const int cb = S3_OFF + (head * 32 + cs * 8) * S3R + (t - 256) * 16;
        stclr(rs, cb, sv4); stclr(rs, cb + 4096, sv4);
        if (wave == 4) stclr(rs, cb + 8192, sv4);
        else if (wave == 5 && lane < 8) stclr(rs, S4_OFF + head * 512 + cs * 128 + lane * 16, sv4);
      }
      VMWAIT0();
      bar();
      if (lane < 32 && (lane & 3) == 0) { v4u o = {d, oldq.y + 2u, d2, oldq.w + 2u}; st16(rs, sqo, o); }
    }
    {
      f32x4 a = {0, 0, 0, 0}; bf16x8 qf[4];
#pragma unroll
      for (int i = 0; i < 4; i++) qf[i] = *(const LAS bf16x8*)(qst + (n & 3) * 272 + i * 64 + q * 16);
      SB();
      f32x4 ab = {0, 0, 0, 0};
      a = MFMA(wk[0], qf[0], a); ab = MFMA(wk[1], qf[1], ab); a = MFMA(wk[2], qf[2], a); ab = MFMA(wk[3], qf[3], ab);
      a += ab;
#pragma unroll
      for (int i = 0; i < 4; i++) { a[i] += dppf<0xB1>(a[i]); a[i] += dppf<0x4E>(a[i]); }
      const float SC = 0.08838834764831845f * 1.4426950408889634f;
      v2u o = {canon(pk2(a[0] * SC, a[1] * SC)), canon(pk2(a[2] * SC, a[3] * SC))};
      if (n == 0) *(LAS v2u*)(q16 + 64 + wave * 8 + q * 2) = o;
    }
    bar();
    if (wave < 2) { const v4u o = *(const LAS v4u*)(q16 + 64 + n * 4); st16(rs, S2_OFF + (wave * 4 + q) * 32768 + head * 1024 + cs * 256 + n * 16, o); }
    bar();
    {
      const int kj = r & 7, cg = xc * 2 + (r >> 3);
      const u16* pl = w_mod + (size_t)(kj * 64 + (t >> 4)) * 3072 + 1024 + cg * 128 + n * 8;
      v4u w0 = LDNT(v4u, pl), w1r = LDNT(v4u, pl + (size_t)32 * 3072);
      CBAR();
      if (r < 8) {
#pragma unroll
        for (int i = 0; i < 8; i++) {
          const int rg = 8 * i + wave, row = 8 * rg + (lane >> 3);
          glds16(w_o + (size_t)(xc * 512 + row) * 512 + r * 64 + (((lane & 7) ^ ((row & 2) | ((row >> 1) & 4))) * 8), (unsigned)(rg * 1024));
        }
        asm volatile("s_waitcnt vmcnt(8)" ::: "memory");
      }
      const float c0 = bflo(cvl0), c1 = bflo(cvl1);
      const unsigned s01 = pk2(c0 * __builtin_amdgcn_rcpf(1.f + __expf(-c0)), c1 * __builtin_amdgcn_rcpf(1.f + __expf(-c1)));
      interleave(w0, w1r);
      float a[8] = {dot2(w0.x, s01, 0.f), dot2(w0.y, s01, 0.f), dot2(w0.z, s01, 0.f), dot2(w0.w, s01, 0.f),
                    dot2(w1r.x, s01, 0.f), dot2(w1r.y, s01, 0.f), dot2(w1r.z, s01, 0.f), dot2(w1r.w, s01, 0.f)};
      float b0 = red32(a[0], a[4]), b1x = red32(a[1], a[5]), b2x = red32(a[2], a[6]), b3 = red32(a[3], a[7]);
      f32x2 o = {red16(b0, b2x), red16(b1x, b3)};
      LAS float* apart = (LAS float*)(SM + 12288);
      *(LAS f32x2*)(apart + wave * 128 + n * 8 + 4 * (lane >> 5) + 2 * ((lane >> 4) & 1)) = o;
      bar();
      if (wave == 7) {
        f32x2 sacc = {0.f, 0.f};
#pragma unroll
        for (int w = 0; w < 8; w++) sacc += *(LAS f32x2*)(apart + w * 128 + lane * 2);
        const unsigned d = canon(pk2(sacc.x, sacc.y));
        v4u o4 = {dppu<0x00>(d), dppu<0x55>(d), dppu<0xAA>(d), dppu<0xFF>(d)};
        if ((lane & 3) == 0) st16(rs, SL_OFF + kj * 4096 + cg * 256 + lane * 4, o4);
        if (cg < 4 || cg >= 12) {
          const unsigned f0 = canon(__float_as_uint(sacc.x)), f1 = canon(__float_as_uint(sacc.y));
          v4u of = {f0, f1, dppu<0x101>(f0), dppu<0x101>(f1)};
          if (!(lane & 1)) st16(rs, SLF_OFF + kj * 4096 + (cg < 4 ? cg : cg - 8) * 512 + lane * 8, of);
        }
      }
      bar();
    }
    if (r < 8) {
      LAS float* fpart = (LAS float*)(SM + 3072);
      LAS unsigned char* cst = SM + 4096;
      LAS float* fp64 = (LAS float*)(SM + 16384);
      VMWAIT0();
      bar();
      bf16x8 wof[8];
      {
        const int g = lane >> 4, qq = (lane & 15) >> 2, pp = lane & 3;
#pragma unroll
        for (int i = 0; i < 8; i++) {
          s16x4 h[2];
#pragma unroll
          for (int tt = 0; tt < 2; tt++) {
            const int row = 32 * (2 * wave + (i >> 2)) + 8 * g + 4 * tt + qq;
            const int ch = (2 * (i & 3) + (pp >> 1)) ^ ((row & 2) | ((row >> 1) & 4));
            h[tt] = __builtin_amdgcn_ds_read_tr16_b64_v4i16((LAS s16x4*)(L + row * 128 + ch * 16 + (pp & 1) * 8));
          }
          wof[i] = (bf16x8){h[0].x, h[0].y, h[0].z, h[0].w, h[1].x, h[1].y, h[1].z, h[1].w};
        }
      }
      if (wave >= 4) {
        int spins = 0;
        for (;;) { CBAR(); v4u v = ld16(rs, S2_OFF + xc * 32768 + xc * 4096 + (t - 256) * 16); if (__all(mx4(v) != SENT) || ++spins > SPIN_CAP) break; __builtin_amdgcn_s_sleep(2); }
      }
      bar();
      {
        v4u av0, av1; int spins = 0;
        const int off = S4_OFF + ((4 * xc + (wave >> 1)) * 2 + (n & 1)) * 256 + (wave & 1) * 128 + q * 16;
        for (;;) { CBAR(); av0 = ld16(rs, off); av1 = ld16(rs, off + 64); if (__all(max(mx4(av0), mx4(av1)) != SENT) || ++spins > SPIN_CAP) break; }
        f32x4 a[4];
#pragma unroll
        for (int T = 0; T < 4; T++) { a[T] = MFMA(__builtin_bit_cast(bf16x8, av0), wof[T], ((f32x4){0, 0, 0, 0})); a[T] = MFMA(__builtin_bit_cast(bf16x8, av1), wof[4 + T], a[T]); }
        const f32x4 sel = q == 0 ? a[0] : q == 1 ? a[1] : q == 2 ? a[2] : a[3];
        fp64[wave * 64 + lane] = sel[0] + sel[1];
        bar();
        if (wave == 0) {
          float v = 0.f;
#pragma unroll
          for (int w = 0; w < 8; w++) v += fp64[w * 64 + lane];
          const unsigned d = canon(pk2(v, dppf<0x101>(v)));
          if (!(lane & 1)) ((LAS unsigned*)cst)[lane >> 1] = d;
          LGWAIT0();
          const v4u o = *(const LAS v4u*)(cst + (lane & 7) * 16);
          st16(rs, S5_OFF + (lane >> 3) * 8192 + xc * 1024 + r * 128 + (lane & 7) * 16, o);
        }
      }
    } else {
      const int ti = xc * 8 + (r - 8);
      LAS unsigned char* W2I = L;
      const float b2v = bflo(b2[ti * 8 + (lane & 7)]);
      const float x8 = bflo(x[ti * 8 + (lane & 7)]);
      CBAR();
#pragma unroll
      for (int i = 0; i < 3; i++) { const int rg = 8 * i + wave; if (rg < 22) { int row = rg * 64 + lane; row = row < 1376 ? row : 1375; dma16n(w2 + (size_t)row * 512 + ti * 8, W2I + rg * 1024); } }
      CBAR();
      LAS float* hpart = (LAS float*)(SM + 2048);
      LAS unsigned* sbuf = (LAS unsigned*)(SM + 2560);
      VMWAIT0();
      bar();
      bf16x8 w2f[6];
#pragma unroll
      for (int i = 0; i < 6; i++) {
        const int ks = wave + 8 * i < 43 ? wave + 8 * i : 0;
        const int g = lane >> 4, qq = (lane & 15) >> 2, pp = lane & 3;
        s16x4 h0 = __builtin_amdgcn_ds_read_tr16_b64_v4i16((LAS s16x4*)(W2I + (32 * ks + 8 * g + qq) * 16 + (pp & 1) * 8));
        s16x4 h1 = __builtin_amdgcn_ds_read_tr16_b64_v4i16((LAS s16x4*)(W2I + (32 * ks + 8 * g + 4 + qq) * 16 + (pp & 1) * 8));
        w2f[i] = (bf16x8){h0.x, h0.y, h0.z, h0.w, h1.x, h1.y, h1.z, h1.w};
      }
      float xn8 = 0.f, gbv = 0.f;
      if (wave == 0) {
        const int off = lane < 16 ? S5_OFF + xc * 8192 + (lane & 7) * 1024 + ti * 16 : SLF_OFF + ((lane >> 1) & 7) * 4096 + ((lane & 32) ? 2048 : 0) + ti * 32 + (lane & 1) * 16;
        v4u v; int spins = 0;
        for (;;) { CBAR(); v = ld16(rs, off); if (__all(mx4(v) != SENT) || ++spins > SPIN_CAP) break; __builtin_amdgcn_s_sleep(4); }
        if (lane < 48) *(LAS v4u*)(sbuf + lane * 4) = v;
        LGWAIT0();
        const LAS u16* sh = (const LAS u16*)sbuf; const LAS float* sf = (const LAS float*)sbuf;
        float ao = 0, ga8 = 0;
#pragma unroll
        for (int k = 0; k < 8; k++) { ao += bflo(sh[k * 8 + (lane & 7)]); ga8 += sf[64 + k * 8 + (lane & 7)]; gbv += sf[128 + k * 8 + (lane & 7)]; }
        xn8 = x8 + ga8 * ao;
      }
      bar();
      {
        v4u hv[6]; int spins = 0;
        const int off = S6_OFF + xc * 6144 + wave * 128 + q * 16, off5 = wave < 3 ? off + 5120 : off;
        for (;;) {
          CBAR();
#pragma unroll
          for (int i = 0; i < 5; i++) hv[i] = ld16(rs, off + i * 1024);
          hv[5] = ld16(rs, off5);
          const unsigned m = max(max(max(mx4(hv[0]), mx4(hv[1])), max(mx4(hv[2]), mx4(hv[3]))), max(mx4(hv[4]), mx4(hv[5])));
          if (__all(m != SENT) || ++spins > SPIN_CAP) break;
        }
        if (wave >= 3) hv[5] = (v4u){0, 0, 0, 0};
        f32x4 a = {0, 0, 0, 0}, a2 = {0, 0, 0, 0};
#pragma unroll
        for (int i = 0; i < 3; i++) { a = MFMA(__builtin_bit_cast(bf16x8, hv[2 * i]), w2f[2 * i], a); a2 = MFMA(__builtin_bit_cast(bf16x8, hv[2 * i + 1]), w2f[2 * i + 1], a2); }
        if (lane < 8) hpart[wave * 8 + lane] = a[0] + a2[0];
      }
      bar();
      if (wave == 0) {
        float a = hpart[lane];
        a += dppf<0x128>(a); a = red16(a, a); a = red32(a, a);
        const float res = xn8 + gbv * (a + b2v);
        if (lane < 8) out[ti * 8 + lane] = __builtin_bit_cast(u16, (__bf16)res);
      }
    }
  } else if (r >= 24) {
    const int e = r - 24, head = 4 * xc + (e >> 1), half = e & 1, ti = xc * 8 + e;
    const int gi = e * 8 + xc;
    const int wst = (gi < 43 ? gi : 0) * 4;
    const int mcol = wave * 64 + 8 * (lane & 7) + 4 * (lane >> 5) + 2 * ((lane >> 4) & 1) + ((lane >> 3) & 1);
    LAS unsigned char* W1I = L + 65536;
    LAS unsigned char* STG = L + 98304;
    LAS u16* scl = (LAS u16*)(SM);
    LAS unsigned* cc16 = (LAS unsigned*)(SM + 1024);
    LAS unsigned* ml = (LAS unsigned*)(SM + 2048);
    LAS float* stat = (LAS float*)(SM + 5120);
    LAS u16* y16 = (LAS u16*)(SM + 5376);
    LAS float* gpart = (LAS float*)(SM + 6400);
    LAS unsigned* pub = (LAS unsigned*)(SM + 7424);
    LAS unsigned* cnt = (LAS unsigned*)(SM + 7680);
    LAS unsigned* tbase = (LAS unsigned*)(SM + 8192);
    LAS float* apart = (LAS float*)(SM + 10240);
    LAS float* fpart = (LAS float*)(SM + 14336);
    LAS unsigned char* cst = SM + 15360;
    const int puoff = SU_OFF + q * 16384 + (xc * 8 + e) * 256 + n * 16;
    const int pfoff = SF_OFF + n * 8192 + ti * 128;
    const int sfoff = SF_OFF + (8 + xc) * 8192 + lane * 128;
#define LDSADD(p) __hip_atomic_fetch_add((p), 1u, __ATOMIC_RELAXED, __HIP_MEMORY_SCOPE_WORKGROUP)
#define REPUB() do { v2u p2_ = *(LAS v2u*)(pub + n * 2); v4u tb_ = *(LAS v4u*)(tbase + lane * 8); unsigned tf_ = tbase[lane * 8 + 4]; unsigned k_ = 0; if (lane == 0) k_ = LDSADD(cnt); k_ = __builtin_amdgcn_readfirstlane(k_); \
      v4u o_ = {p2_.x, tb_.x + k_, p2_.y, tb_.y + k_}; st16(rs, puoff, o_); v4u o2_ = {p2_.x, tb_.z + k_, p2_.y, tb_.w + k_}; st16(rs, puoff + 65536, o2_); \
      if (lane < 16) { v2u f_ = {0u, tf_ + k_}; st8(rs, pfoff, f_); } } while (0)
    v2u pd = {0, 0};
    v4u old0 = {0, 0, 0, 0}, old1 = {0, 0, 0, 0}, xv = {0, 0, 0, 0}; unsigned snapf = 0, xw = 0;
    v4u wm[2]; unsigned short cv[2];
    {
      const u16* p = w_mod + (size_t)(e * 64 + (t >> 4)) * 3072 + (n >> 3) * 512 + xc * 64 + (n & 7) * 8;
      wm[0] = LDNT(v4u, p); wm[1] = LDNT(v4u, p + (size_t)32 * 3072);
      cv[0] = cvec[e * 64 + (t >> 4)]; cv[1] = cvec[e * 64 + 32 + (t >> 4)];
    }
    CBAR();
    const unsigned oldf = ld8(rs, pfoff).y;
    if (wave == 0) { old0 = ld16(rs, puoff); old1 = ld16(rs, puoff + 65536); xv = *(const v4u*)(x + lane * 8); xw = ((const unsigned*)x)[xc * 32 + (lane & 31)]; }
    if (wave == 1) snapf = ld8(rs, sfoff).y;
    CBAR();
    {
      const int pc = t >= 64 ? t - 64 : t;
      st16(rs, wave == 0 ? DUMMY_OFF + ti * 1024 + lane * 16 : S2_OFF + ti * (EGC_PIECES * 16) + (pc < EGC_PIECES ? pc : pc - EGC_PIECES) * 16, sv4);
    }
    CBAR();
    float ln0 = 0.f, ln1 = 0.f;
    if (wave == 0) {
      float xs[8] = {bflo(xv.x), bfhi(xv.x), bflo(xv.y), bfhi(xv.y), bflo(xv.z), bfhi(xv.z), bflo(xv.w), bfhi(xv.w)};
      float s = 0;
#pragma unroll
      for (int i = 0; i < 8; i++) s += xs[i];
      const float mean = wave_sum(s) * (1.f / 512.f);
      float vs = 0;
#pragma unroll
      for (int i = 0; i < 8; i++) { float d = xs[i] - mean; vs += d * d; }
      const float rstd = rsqrtf(wave_sum(vs) * (1.f / 512.f) + 1e-5f);
      ln0 = (bflo(xw) - mean) * rstd; ln1 = (bfhi(xw) - mean) * rstd;
      v4u tb = {old0.y, old0.w, old1.y, old1.w};
      *(LAS v4u*)(tbase + lane * 8) = tb; tbase[lane * 8 + 4] = oldf;
      if (lane == 0) { cnt[0] = 2u + NREP; cnt[1] = 0u; }
    }
    CBAR();
    {
      const float c0 = bflo(cv[0]), c1 = bflo(cv[1]);
      const unsigned s01 = pk2(c0 * __builtin_amdgcn_rcpf(1.f + __expf(-c0)), c1 * __builtin_amdgcn_rcpf(1.f + __expf(-c1)));
      interleave(wm[0], wm[1]);
      float a[8] = {dot2(wm[0].x, s01, 0.f), dot2(wm[0].y, s01, 0.f), dot2(wm[0].z, s01, 0.f), dot2(wm[0].w, s01, 0.f),
                    dot2(wm[1].x, s01, 0.f), dot2(wm[1].y, s01, 0.f), dot2(wm[1].z, s01, 0.f), dot2(wm[1].w, s01, 0.f)};
      float b0 = red32(a[0], a[4]), b1x = red32(a[1], a[5]), b2x = red32(a[2], a[6]), b3 = red32(a[3], a[7]);
      f32x2 o = {red16(b0, b2x), red16(b1x, b3)};
      *(LAS f32x2*)(apart + wave * 128 + n * 8 + 4 * (lane >> 5) + 2 * ((lane >> 4) & 1)) = o;
    }
    bar();
    if (wave == 0) {
      f32x2 sacc = {0.f, 0.f};
#pragma unroll
      for (int w = 0; w < 8; w++) sacc += *(LAS f32x2*)(apart + w * 128 + lane * 2);
      auto r0 = __builtin_amdgcn_permlane32_swap(__float_as_uint(sacc.x), __float_as_uint(sacc.x), false, false);
      auto r1 = __builtin_amdgcn_permlane32_swap(__float_as_uint(sacc.y), __float_as_uint(sacc.y), false, false);
      const float one = e == 0 ? 1.f : 0.f;
      const float u0 = ln0 * (one + __uint_as_float(r0[1])) + __uint_as_float(r0[0]), u1 = ln1 * (one + __uint_as_float(r1[1])) + __uint_as_float(r1[0]);
      if (lane < 32) pub[lane] = pk2(u0, u1);
      LGWAIT0();
      pd = *(LAS v2u*)(pub + n * 2);
      v4u o = {pd.x, old0.y + 1u, pd.y, old0.w + 1u}; st16(rs, puoff, o);
      v4u o2 = {pd.x, old1.y + 1u, pd.y, old1.w + 1u}; st16(rs, puoff + 65536, o2);
    }
    unsigned karr = 0;
    if (wave != 0) { VMWAIT0(); if (lane == 0) karr = LDSADD(cnt + 1); }
    const bool lastw = __builtin_amdgcn_readfirstlane(karr) == 6u;
    if (lastw && lane < 16) { v2u f = {0u, oldf + 1u}; st8(rs, pfoff, f); }
#pragma unroll
    for (int i = 0; i < 4; i++) if (gi < 43) { const int rg = 8 * i + wave; dma16n(w1 + (size_t)(rg * 16 + (lane >> 2)) * 1376 + wst * 8 + (lane & 3) * 8, W1I + rg * 1024); }
    CBAR();
    DELAY(EGL_DELAY);
    const float xme = bflo(x[mcol]);
    const float b1v = bflo(b1[wst * 8 + (lane & 31)]);
    CBAR();
#pragma unroll
    for (int i = 0; i < 8; i++) {
      const int rg = 8 * i + wave, row = 4 * rg + q;
      dma16(w_uv + (size_t)(half * 256 + row) * 4096 + head * 128 + ((n ^ swz(row)) * 8), L + rg * 1024);
    }
    CBAR();
    if (wave == 0 || lastw) {
#pragma unroll 1
      for (int k = 0; k < NREP; k++) {
        __builtin_amdgcn_s_sleep(14);
        if (wave == 0) { v4u o_ = {pd.x, old0.y + 2u + k, pd.y, old0.w + 2u + k}; st16(rs, puoff, o_); v4u o2_ = {pd.x, old1.y + 2u + k, pd.y, old1.w + 2u + k}; st16(rs, puoff + 65536, o2_); }
        if (lastw && lane < 16) { v2u f_ = {0u, oldf + 2u + k}; st8(rs, pfoff, f_); }
      }
    }
    VMWAIT0();
    bar();
    bf16x8 wvf[8], w1f[4];
#pragma unroll
    for (int s2 = 0; s2 < 8; s2++) wvf[s2] = trfrag<256, true>(L, 32 * wave, 2 * s2, lane);
#pragma unroll
    for (int i = 0; i < 4; i++) w1f[i] = trfrag<64, false>(W1I, 64 * wave + 32 * (i >> 1), 2 * (i & 1), lane);
    if (wave == 1) {
      int spins = 0; bool ok = false;
      for (;;) { CBAR(); if (!ok) ok = ld8(rs, sfoff).y != snapf; if (__all(ok) || ++spins > SPIN_CAP) break; if ((spins & 3) == 0) REPUB(); __builtin_amdgcn_s_sleep(2); }
      spins = 0;
      for (;;) { CBAR(); v4u v = ld16(rs, S2_OFF + xc * 32768 + head * 1024 + lane * 16); if (__all(mx4(v) != SENT) || ++spins > SPIN_CAP) break; if ((spins & 3) == 0) REPUB(); __builtin_amdgcn_s_sleep(2); }
    }
    bar();
    {
      LAS float* sclf = (LAS float*)(SM);
      LAS float* epart = (LAS float*)(STG);
      float sc8[8];
      {
        const int soff = S3_OFF + (head * 32 + (lane & 31)) * S3R + 1024;
        if (wave == 7) {
          v2u stt; int spins = 0;
          for (;;) { CBAR(); stt = ld8(rs, soff); if (__all(max(stt.x, stt.y) != SENT) || ++spins > SPIN_CAP) break; if ((spins & 3) == 0) REPUB(); }
          const float m = __uint_as_float(stt.x), l = __uint_as_float(stt.y);
          const float mg = wave_max(m);
          const float sc = __builtin_amdgcn_exp2f(m - mg);
          const float il = 2.f * __builtin_amdgcn_rcpf(wave_sum(sc * l));
          if (lane < 32) sclf[lane] = sc * il;
        }
      }
      v4u p0, p1; int spins = 0;
      const int off = S3_OFF + (head * 32 + n) * S3R + half * 512 + wave * 64 + q * 16;
      for (;;) {
        CBAR();
        p0 = ld16(rs, off); p1 = ld16(rs, off + 16 * S3R);
        if (__all(max(mx4(p0), mx4(p1)) != SENT) || ++spins > SPIN_CAP) break;
        if ((spins & 15) == 0) REPUB();
      }
      f32x4 y0[8], y1[8];
#pragma unroll
      for (int T = 0; T < 8; T++) {
        y0[T] = MFMA(__builtin_bit_cast(bf16x8, p0), wvf[T], ((f32x4){0, 0, 0, 0}));
        y1[T] = MFMA(__builtin_bit_cast(bf16x8, p1), wvf[T], ((f32x4){0, 0, 0, 0}));
      }
      bar();
      { const f32x4 a = *(const LAS f32x4*)(sclf + 4 * q), b = *(const LAS f32x4*)(sclf + 16 + 4 * q); sc8[0] = a[0]; sc8[1] = a[1]; sc8[2] = a[2]; sc8[3] = a[3]; sc8[4] = b[0]; sc8[5] = b[1]; sc8[6] = b[2]; sc8[7] = b[3]; }
      float z[8];
#pragma unroll
      for (int T = 0; T < 8; T++)
        z[T] = ((y0[T][0] * sc8[0] + y0[T][1] * sc8[1]) + (y0[T][2] * sc8[2] + y0[T][3] * sc8[3])) + ((y1[T][0] * sc8[4] + y1[T][1] * sc8[5]) + (y1[T][2] * sc8[6] + y1[T][3] * sc8[7]));
      const float r0 = red32(z[0], z[4]), r1 = red32(z[1], z[5]), r2 = red32(z[2], z[6]), r3 = red32(z[3], z[7]);
      const float u0 = red16(r0, r2), u1 = red16(r1, r3);
      const int T0 = 4 * (lane >> 5) + 2 * ((lane >> 4) & 1);
      epart[wave * 128 + T0 * 16 + n] = u0; epart[wave * 128 + T0 * 16 + 16 + n] = u1;
      bar();
      if (wave == 0) {
        f32x2 sacc = {0.f, 0.f};
#pragma unroll
        for (int w = 0; w < 8; w++) sacc += *(LAS f32x2*)(epart + w * 128 + lane * 2);
        const unsigned d = canon(pk2(sacc.x, sacc.y));
        v4u o4 = {dppu<0x00>(d), dppu<0x55>(d), dppu<0xAA>(d), dppu<0xFF>(d)};
        if ((lane & 3) == 0) st16(rs, S4_OFF + (head * 2 + half) * 256 + lane * 4, o4);
      }
    }
    if (gi >= 43) {
      if (wave == 0) {
        int spins = 0;
        for (;;) {
          CBAR(); v4u v = ld16(rs, DN_OFF + xc * 128);
          if (v.x != SENT || ++spins > SPIN_CAP) break;
          if ((spins & 3) == 0) REPUB();
          __builtin_amdgcn_s_sleep(8);
        }
      } return;
    }
    {
      v4u v0, v1, v2; int spins = 0;
      const int off = SL_OFF + wave * 4096 + lane * 16;
      for (;;) { CBAR(); v0 = ld16(rs, off); v1 = ld16(rs, off + 1024); v2 = ld16(rs, off + 2048); if (__all(max(max(mx4(v0), mx4(v1)), mx4(v2)) != SENT) || ++spins > SPIN_CAP) break; if ((spins & 15) == 0) REPUB(); __builtin_amdgcn_s_sleep(1); }
      *(LAS v4u*)(L + wave * 3072 + lane * 16) = v0; *(LAS v4u*)(L + wave * 3072 + 1024 + lane * 16) = v1; *(LAS v4u*)(L + wave * 3072 + 2048 + lane * 16) = v2;
    }
    bar();
    float gam = 0.f, sbm = 0.f, cb1 = 0.f;
    {
      const LAS u16* mlh = (const LAS u16*)L;
#pragma unroll
      for (int k = 0; k < 8; k++) { gam += bflo(mlh[k * 1536 + mcol]); sbm += bflo(mlh[k * 1536 + 512 + mcol]); cb1 += bflo(mlh[k * 1536 + 1024 + mcol]); }
      sbm = rbf(sbm); cb1 = rbf(1.f + cb1);
    }
    LAS u16* cs16 = (LAS u16*)(SM + 10240);
    LAS float* gp64 = (LAS float*)(SM + 12288);
    cs16[mcol] = __builtin_bit_cast(u16, (__bf16)cb1); cs16[512 + mcol] = __builtin_bit_cast(u16, (__bf16)sbm);
    LGWAIT0();
    bf16x8 auxf[2];
    {
      const bf16x8 ones = {0x3F80, 0x3F80, 0x3F80, 0x3F80, 0x3F80, 0x3F80, 0x3F80, 0x3F80};
#pragma unroll
      for (int i = 0; i < 2; i++) { const bf16x8 v = *(const LAS bf16x8*)(cs16 + ((n - 1) & 1) * 512 + wave * 64 + i * 32 + q * 8); auxf[i] = n == 0 ? ones : v; }
      f32x4 k0 = MFMA(auxf[0], w1f[0], ((f32x4){0, 0, 0, 0})), k1 = MFMA(auxf[0], w1f[1], ((f32x4){0, 0, 0, 0})), kg = MFMA(auxf[0], auxf[0], ((f32x4){0, 0, 0, 0}));
      k0 = MFMA(auxf[1], w1f[2], k0); k1 = MFMA(auxf[1], w1f[3], k1); kg = MFMA(auxf[1], auxf[1], kg);
      if (lane < 16) {
        LAS float* g = (LAS float*)(L + 32768) + wave * 192;
        g[n] = k0[0]; g[16 + n] = k1[0]; g[32 + n] = kg[0]; g[64 + n] = k0[1]; g[80 + n] = k1[1]; g[96 + n] = kg[1]; g[128 + n] = k0[2]; g[144 + n] = k1[2]; g[160 + n] = kg[2];
      }
    }
    bar();
    float Ow = 0.f, Cw = 0.f, Sw = 0.f, Kc = 0.f, Ks = 0.f, Kc2 = 0.f, Kcs = 0.f, Ks2 = 0.f;
    if (wave == 0) {
      const LAS float* g = (const LAS float*)(L + 32768);
      float o = 0.f, c = 0.f, sg = 0.f;
#pragma unroll
      for (int w = 0; w < 8; w++) { o += g[w * 192 + (lane < 48 ? lane : 0)]; c += g[w * 192 + 64 + (lane < 48 ? lane : 0)]; sg += g[w * 192 + 128 + (lane < 48 ? lane : 0)]; }
      Ow = o; Cw = c; Sw = sg;
      Kc = RL(o, 33); Ks = RL(o, 34); Kc2 = RL(c, 33); Kcs = RL(c, 34); Ks2 = RL(sg, 34);
    }
    asm volatile("" : "+v"(gam), "+v"(cb1), "+v"(Ow), "+v"(Cw), "+v"(Sw));
    {
      v4u v; int spins = 0;
      const int off = S5_OFF + xc * 8192 + (lane >> 3) * 1024 + wave * 128 + (lane & 7) * 16;
      for (;;) { CBAR(); v = ld16(rs, off); if (__all(mx4(v) != SENT) || ++spins > SPIN_CAP) break; if ((spins & 15) == 0) REPUB(); }
      const float b0 = red32(bflo(v.x), bflo(v.z)), b1x = red32(bfhi(v.x), bfhi(v.z)), b2x = red32(bflo(v.y), bflo(v.w)), b3 = red32(bfhi(v.y), bfhi(v.w));
      const float c0 = red16(b0, b2x), c1 = red16(b1x, b3);
      const bool up = lane & 8;
      const float keep = up ? c1 : c0, send = up ? c0 : c1;
      const float ao = keep + dppf<0x128>(send);
      const __bf16 xb = (__bf16)(xme + gam * ao);
      cs16[mcol] = __builtin_bit_cast(u16, (__bf16)((float)xb * cb1)); cs16[512 + mcol] = __builtin_bit_cast(u16, xb);
      LGWAIT0();
      const bf16x8 y0 = *(const LAS bf16x8*)(cs16 + (n & 1) * 512 + wave * 64 + q * 8), y1 = *(const LAS bf16x8*)(cs16 + (n & 1) * 512 + wave * 64 + 32 + q * 8);
      f32x4 z0 = MFMA(y0, w1f[0], ((f32x4){0, 0, 0, 0})), z1 = MFMA(y0, w1f[1], ((f32x4){0, 0, 0, 0})), ax = MFMA(y0, auxf[0], ((f32x4){0, 0, 0, 0})), gr = MFMA(y0, y0, ((f32x4){0, 0, 0, 0}));
      z0 = MFMA(y1, w1f[2], z0); z1 = MFMA(y1, w1f[3], z1); ax = MFMA(y1, auxf[1], ax); gr = MFMA(y1, y1, gr);
      const float lastq = n == 0 ? ax[1] : n == 1 ? gr[1] : gr[0];
      gp64[wave * 64 + lane] = q == 0 ? z0[0] : q == 1 ? z1[0] : q == 2 ? ax[0] : lastq;
    }
    bar();
    if (wave == 0) {
      float a = 0;
#pragma unroll
      for (int w = 0; w < 8; w++) a += gp64[w * 64 + lane];
      const float B1 = RL(a, 32), B3 = RL(a, 33), B4 = RL(a, 34), A1 = RL(a, 48), A2 = RL(a, 49), B2 = RL(a, 50);
      const float mu = A1 * (1.f / 512.f), rr = __builtin_amdgcn_rsqf(fmaxf(A2 * (1.f / 512.f) - mu * mu, 0.f) + 1e-5f);
      const float m2 = (rr * (B1 - mu * Kc) + Ks) * (1.f / 512.f);
      const float e2 = (rr * rr * (B2 - 2.f * mu * B3 + mu * mu * Kc2) + 2.f * rr * (B4 - mu * Kcs) + Ks2) * (1.f / 512.f);
      const float r2 = __builtin_amdgcn_rsqf(fmaxf(e2 - m2 * m2, 0.f) + 1e-5f);
      const float zz = r2 * (rr * (a - mu * Cw) + Sw - m2 * Ow);
      const float z = zz + b1v;
      const float u = z * (0.7978845608028654f + 0.035677408136300125f * z * z);
      const float gl = z * __builtin_amdgcn_rcpf(1.f + __builtin_amdgcn_exp2f(-2.885390081777927f * u));
      const unsigned d = canon(pk2(gl, dppf<0x101>(gl)));
      if (lane < 32 && !(lane & 1)) ((LAS unsigned*)y16)[lane >> 1] = d;
      LGWAIT0();
      const v4u o = *(const LAS v4u*)((LAS unsigned*)y16 + (lane & 3) * 4);
      if (lane < 32) st16(rs, S6_OFF + (lane >> 2) * 6144 + gi * 128 + (lane & 3) * 16, o);
    }
    if (ti == 0 && wave == 1 && lane < 8) { v4u o = {1, 1, 1, 1}; st16(rs, DN_OFF + lane * 128, o); }
  } else {
    const int e = r - 16, split = 4 * xc + (e & 3), hg = e >> 2;
    const int sfoff = SF_OFF + (8 + xc) * 8192 + lane * 128;
    LAS unsigned char* qa = SM;
    LAS unsigned char* Pm = SM + 16896;
    LAS float* wmax = (LAS float*)(SM + 21504);
    LAS float* wsum = wmax + 128;
    unsigned snapf = 0;
    if (wave == 0) snapf = ld8(rs, sfoff).y;
    bar();
    CBAR();
#pragma unroll
    for (int i = 0; i < 16; i++) {
      const int key = ((i * 8 + wave) + hg * 64) & 127;
      dma16(ckv + (size_t)(split * 128 + key) * 512 + ((lane ^ swz(key)) * 8), L + key * 1024);
    }
    VMWAIT0();
    bar();
    bf16x8 ka[16], va[16];
    {
      const int key = wave * 16 + n, f2 = swz(key);
#pragma unroll
      for (int s2 = 0; s2 < 16; s2++) ka[s2] = *(const LAS bf16x8*)(L + key * 1024 + (((4 * s2 + q) ^ f2) * 16));
#pragma unroll
      for (int i = 0; i < 16; i++) va[i] = trfrag<1024, true>(L, 32 * (i & 3), wave * 8 + 2 * (i >> 2), lane);
    }
    if (wave == 0) {
      int spins = 0; bool ok = false;
      for (;;) { CBAR(); if (!ok) ok = ld8(rs, sfoff).y != snapf; if (__all(ok) || ++spins > SPIN_CAP) break; __builtin_amdgcn_s_sleep(1); }
    }
    bar();
    {
      const int off = S2_OFF + xc * 32768 + hg * 16384 + t * 16;
      v4u v0, v1; int spins = 0;
      for (;;) {
        CBAR();
        v0 = ld16(rs, off); v1 = ld16(rs, off + 8192);
        if (__all(max(mx4(v0), mx4(v1)) != SENT) || ++spins > SPIN_CAP) break;
      }
      *(LAS v4u*)(qa + wave * 1056 + lane * 16) = v0;
      *(LAS v4u*)(qa + (8 + wave) * 1056 + lane * 16) = v1;
    }
    bar();
    f32x4 sc = {0, 0, 0, 0};
    {
      bf16x8 qf[16];
#pragma unroll
      for (int s2 = 0; s2 < 16; s2++) qf[s2] = *(const LAS bf16x8*)(qa + n * 1056 + s2 * 64 + q * 16);
      SB();
      f32x4 sc2 = {0, 0, 0, 0};
#pragma unroll
      for (int s2 = 0; s2 < 8; s2++) { sc = MFMA(ka[2 * s2], qf[2 * s2], sc); sc2 = MFMA(ka[2 * s2 + 1], qf[2 * s2 + 1], sc2); }
      sc += sc2;
    }
    float mx = fmaxf(fmaxf(sc[0], sc[1]), fmaxf(sc[2], sc[3]));
    mx = max16(mx); mx = max32(mx);
    if (lane < 16) wmax[lane * 8 + wave] = mx;
    bar();
    {
      f32x4 m0 = *(LAS f32x4*)(wmax + n * 8), m1 = *(LAS f32x4*)(wmax + n * 8 + 4);
      mx = fmaxf(fmaxf(fmaxf(m0[0], m0[1]), fmaxf(m0[2], m0[3])), fmaxf(fmaxf(m1[0], m1[1]), fmaxf(m1[2], m1[3])));
    }
    const float p0 = __builtin_amdgcn_exp2f(sc[0] - mx), p1 = __builtin_amdgcn_exp2f(sc[1] - mx), p2 = __builtin_amdgcn_exp2f(sc[2] - mx), p3 = __builtin_amdgcn_exp2f(sc[3] - mx);
    { v2u o = {pk2(p0, p1), pk2(p2, p3)}; *(LAS v2u*)(Pm + n * 288 + (wave * 16 + q * 4) * 2) = o; }
    float ps = (p0 + p1) + (p2 + p3);
    ps = red16(ps, ps); ps = red32(ps, ps);
    if (lane < 16) wsum[lane * 8 + wave] = ps;
    bar();
    {
      if (wave == 7) {
        f32x4 s0 = *(LAS f32x4*)(wsum + n * 8), s1 = *(LAS f32x4*)(wsum + n * 8 + 4);
        const float l = ((s0[0] + s0[1]) + (s0[2] + s0[3])) + ((s1[0] + s1[1]) + (s1[2] + s1[3]));
        v2u o2 = {canon(__float_as_uint(mx)), canon(__float_as_uint(l))};
        if (lane < 16) st8(rs, S3_OFF + ((hg * 16 + lane) * 32 + split) * S3R + 1024, o2);
      }
      bf16x8 bp[4];
#pragma unroll
      for (int ks = 0; ks < 4; ks++) bp[ks] = *(const LAS bf16x8*)(Pm + n * 288 + ks * 64 + q * 16);
      SB();
      f32x4 acc[4];
#pragma unroll
      for (int T = 0; T < 4; T++) {
        acc[T] = (f32x4){0, 0, 0, 0};
#pragma unroll
        for (int ks = 0; ks < 4; ks++) acc[T] = MFMA(va[T * 4 + ks], bp[ks], acc[T]);
      }
      unsigned d0[4], d1[4];
#pragma unroll
      for (int T = 0; T < 4; T++) { d0[T] = canon(pk2(acc[T][0], acc[T][1])); d1[T] = canon(pk2(acc[T][2], acc[T][3])); }
      auto r0 = __builtin_amdgcn_permlane16_swap(d0[0], d0[1], false, false);
      auto r1 = __builtin_amdgcn_permlane16_swap(d1[0], d1[1], false, false);
      auto r2 = __builtin_amdgcn_permlane16_swap(d0[2], d0[3], false, false);
      auto r3 = __builtin_amdgcn_permlane16_swap(d1[2], d1[3], false, false);
      v4u oa = {r0[0], r1[0], r0[1], r1[1]}, ob = {r2[0], r3[0], r2[1], r3[1]};
      const v4u obs = {dppu<0x128>(ob.x), dppu<0x128>(ob.y), dppu<0x128>(ob.z), dppu<0x128>(ob.w)};
      const bool lo = n < 8;
      const int o3b = S3_OFF + ((hg * 16 + (n & 7)) * 32 + split) * S3R + (wave * 64 + (q & 1) * 16 + (q & 2) * 4) * 2;
      st16(rs, o3b + (lo ? 0 : 64), lo ? oa : obs);
      st16(rs, o3b + 8 * 32 * S3R + (lo ? 64 : 0), lo ? obs : oa);
    }
  }
}

extern "C" void kernel_launch(void* const* d_in, const int* in_sizes, int n_in, void* d_out, int out_size, void* d_ws, size_t ws_size, hipStream_t stream) {
  (void)in_sizes; (void)n_in; (void)out_size; (void)ws_size;
  (void)hipFuncSetAttribute((const void*)fused_kernel, hipFuncAttributeMaxDynamicSharedMemorySize, L_TOTAL);
  fused_kernel<<<256, NT, L_TOTAL, stream>>>((const u16*)d_in[0], (const u16*)d_in[1], (const u16*)d_in[2], (const u16*)d_in[3], (const u16*)d_in[4], (const u16*)d_in[5],
                                             (const u16*)d_in[7], (const u16*)d_in[8], (const u16*)d_in[9], (const u16*)d_in[10], (const u16*)d_in[11], (const u16*)d_in[12],
                                             (u16*)d_out, (unsigned char*)d_ws);
}
```

```cpp
#include <hip/hip_runtime.h>
#include <stdint.h>

typedef unsigned short u16;
typedef unsigned v4u __attribute__((ext_vector_type(4)));
typedef unsigned v2u __attribute__((ext_vector_type(2)));
typedef float f32x4 __attribute__((ext_vector_type(4)));
typedef float f32x2 __attribute__((ext_vector_type(2)));
typedef short bf16x8 __attribute__((ext_vector_type(8)));
typedef short s16x4 __attribute__((ext_vector_type(4)));
typedef __bf16 bf2 __attribute__((ext_vector_type(2)));
#define LAS __attribute__((address_space(3)))

constexpr unsigned SENT = 0xFFFFFFFFu;
constexpr int NT = 512;
constexpr int SPIN_CAP = 1 << 20;
#define NREP 2
#define EGL_DELAY 400
#define ROFF 24
#define CLRAUX 16
#define NTAUX 0
#define NTS ""
#define LDNT(T, p) (*(const T*)(p))
constexpr int SU_OFF = 0;
constexpr int SF_OFF = 131072;
constexpr int SQ_OFF = 262144;
constexpr int CLR_OFF = 327680;
constexpr int S2_OFF = CLR_OFF;
constexpr int S5_OFF = S2_OFF + 8 * 32768;
constexpr int S6_OFF = S5_OFF + 65536;
constexpr int SL_OFF = S6_OFF + 49152;
constexpr int SLF_OFF = SL_OFF + 32768;
constexpr int DN_OFF = SLF_OFF + 32768;
constexpr int EGC_END = DN_OFF + 4096;
constexpr int EGC_PIECES = (EGC_END - S2_OFF) / 16 / 64;
constexpr int S4_OFF = EGC_END;
constexpr int S3R = 1152;
constexpr int S3_OFF = S4_OFF + 16384;
constexpr int CLR_END = S3_OFF + 1024 * S3R;
constexpr int DUMMY_OFF = CLR_END;
constexpr int WS_USED = CLR_END + 65536;
constexpr int L_SM = 131072;
constexpr int L_TOTAL = 131072 + 30720;

__device__ __forceinline__ float bflo(unsigned w) { return __uint_as_float(w << 16); }
__device__ __forceinline__ float bfhi(unsigned w) { return __uint_as_float(w & 0xffff0000u); }
__device__ __forceinline__ unsigned pk2(float a, float b) { bf2 p; p.x = (__bf16)a; p.y = (__bf16)b; return __builtin_bit_cast(unsigned, p); }
__device__ __forceinline__ float rbf(float a) { return (float)(__bf16)a; }
__device__ __forceinline__ unsigned canon(unsigned w) { return w == SENT ? 0x7FC07FC0u : w; }
__device__ __forceinline__ float dot2(unsigned a, unsigned b, float c) { return __builtin_amdgcn_fdot2_f32_bf16(__builtin_bit_cast(bf2, a), __builtin_bit_cast(bf2, b), c, false); }
__device__ __forceinline__ void bar() { __builtin_amdgcn_sched_barrier(0); asm volatile("s_waitcnt lgkmcnt(0)\n\ts_barrier" ::: "memory"); __builtin_amdgcn_sched_barrier(0); }
template <int CTRL> __device__ __forceinline__ float dppf(float v) { return __int_as_float(__builtin_amdgcn_update_dpp(0, __float_as_int(v), CTRL, 0xf, 0xf, false)); }
template <int CTRL> __device__ __forceinline__ unsigned dppu(unsigned v) { return (unsigned)__builtin_amdgcn_update_dpp(0, (int)v, CTRL, 0xf, 0xf, false); }
__device__ __forceinline__ float red32(float a, float b) {
  auto r = __builtin_amdgcn_permlane32_swap(__float_as_uint(a), __float_as_uint(b), false, false);
  return __uint_as_float(r[0]) + __uint_as_float(r[1]);
}
__device__ __forceinline__ float red16(float a, float b) {
  auto r = __builtin_amdgcn_permlane16_swap(__float_as_uint(a), __float_as_uint(b), false, false);
  return __uint_as_float(r[0]) + __uint_as_float(r[1]);
}
__device__ __forceinline__ float max32(float a) { auto r = __builtin_amdgcn_permlane32_swap(__float_as_uint(a), __float_as_uint(a), false, false); return fmaxf(__uint_as_float(r[0]), __uint_as_float(r[1])); }
__device__ __forceinline__ float max16(float a) { auto r = __builtin_amdgcn_permlane16_swap(__float_as_uint(a), __float_as_uint(a), false, false); return fmaxf(__uint_as_float(r[0]), __uint_as_float(r[1])); }
__device__ __forceinline__ float wave_sum(float v) {
  v += dppf<0x128>(v); v += dppf<0x124>(v); v += dppf<0x122>(v); v += dppf<0x121>(v);
  v = red16(v, v); v = red32(v, v); return v;
}
__device__ __forceinline__ float wave_max(float v) {
  v = fmaxf(v, dppf<0x128>(v)); v = fmaxf(v, dppf<0x124>(v)); v = fmaxf(v, dppf<0x122>(v)); v = fmaxf(v, dppf<0x121>(v));
  v = max16(v); v = max32(v); return v;
}
__device__ __forceinline__ unsigned ilo(unsigned a, unsigned b) { return __builtin_amdgcn_perm(b, a, 0x05040100u); }
__device__ __forceinline__ unsigned ihi(unsigned a, unsigned b) { return __builtin_amdgcn_perm(b, a, 0x07060302u); }
__device__ __forceinline__ void interleave(v4u& a, v4u& b) {
  v4u x, y;
  x.x = ilo(a.x, b.x); x.y = ihi(a.x, b.x); x.z = ilo(a.y, b.y); x.w = ihi(a.y, b.y);
  y.x = ilo(a.z, b.z); y.y = ihi(a.z, b.z); y.z = ilo(a.w, b.w); y.w = ihi(a.w, b.w);
  a = x; b = y;
}
typedef __amdgpu_buffer_rsrc_t rsrc_t;
__device__ __forceinline__ v4u ld16(rsrc_t r, int off) { return __builtin_amdgcn_raw_buffer_load_b128(r, off, 0, 16); }
__device__ __forceinline__ v2u ld8(rsrc_t r, int off) { return __builtin_amdgcn_raw_buffer_load_b64(r, off, 0, 16); }
__device__ __forceinline__ void st16(rsrc_t r, int off, v4u v) { __builtin_amdgcn_raw_buffer_store_b128(v, r, off, 0, 16); }
__device__ __forceinline__ void st8(rsrc_t r, int off, v2u v) { __builtin_amdgcn_raw_buffer_store_b64(v, r, off, 0, 16); }
__device__ __forceinline__ void glds16(const void* g, unsigned lds_base) {
  unsigned sv; asm volatile("s_mov_b32 %0, m0\n\ts_mov_b32 m0, %2\n\ts_nop 0\n\tglobal_load_lds_dwordx4 %1, off" NTS "\n\ts_mov_b32 m0, %0" : "=&s"(sv) : "v"(g), "s"(lds_base) : "memory");
}
#define RL(v, l) __int_as_float(__builtin_amdgcn_readlane(__float_as_int(v), (l)))
#define SB() __builtin_amdgcn_sched_barrier(0)
#define CBAR() do { asm volatile("" ::: "memory"); __builtin_amdgcn_sched_barrier(0); } while (0)

__device__ __forceinline__ int swz(int row) { return ((row & 3) | ((row >> 1) & 4)) << 1; }
template <int AUX> __device__ __forceinline__ void dma16a(const void* g, LAS unsigned char* l) { __builtin_amdgcn_global_load_lds((const __attribute__((address_space(1))) unsigned*)g, (LAS unsigned*)l, 16, 0, AUX); }
#define dma16 dma16a<NTAUX>
#define NARROW 0
#define dma16n dma16a<NARROW>
#define LDNAR(T, p) (*(const T*)(p))
template <int RB, bool SW>
__device__ __forceinline__ bf16x8 trfrag(const LAS unsigned char* img, int k0, int ch0, int lane) {
  const int g = lane >> 4, qq = (lane & 15) >> 2, pp = lane & 3;
  s16x4 h[2];
#pragma unroll
  for (int tt = 0; tt < 2; tt++) {
    const int row = k0 + 8 * g + 4 * tt + qq;
    const int ch = SW ? ((ch0 + (pp >> 1)) ^ swz(row)) : (ch0 + (pp >> 1));
    h[tt] = __builtin_amdgcn_ds_read_tr16_b64_v4i16((LAS s16x4*)(img + row * RB + ch * 16 + (pp & 1) * 8));
  }
  return (bf16x8){h[0].x, h[0].y, h[0].z, h[0].w, h[1].x, h[1].y, h[1].z, h[1].w};
}
#define MFMA(a, b, c) __builtin_amdgcn_mfma_f32_16x16x32_bf16(a, b, c, 0, 0, 0)
#define VMWAIT0() do { asm volatile("" ::: "memory"); __builtin_amdgcn_s_waitcnt(0x0F70); asm volatile("s_waitcnt vmcnt(0)" ::: "memory"); } while (0)
#define LGWAIT0() asm volatile("s_waitcnt lgkmcnt(0)" ::: "memory")
typedef unsigned long long u64;
__device__ __forceinline__ unsigned mx4(v4u v) { return max(max(v.x, v.y), max(v.z, v.w)); }
__device__ __forceinline__ void stclr(rsrc_t r, int off, v4u v) { __builtin_amdgcn_raw_buffer_store_b128(v, r, off, 0, CLRAUX); }

__global__ void __launch_bounds__(NT, 2) fused_kernel(
    const u16* __restrict__ x, const u16* __restrict__ ckv, const u16* __restrict__ w_q, const u16* __restrict__ w_uk,
    const u16* __restrict__ w_uv, const u16* __restrict__ w_o, const u16* __restrict__ w1, const u16* __restrict__ b1,
    const u16* __restrict__ w2, const u16* __restrict__ b2, const u16* __restrict__ cvec, const u16* __restrict__ w_mod,
    u16* __restrict__ out, unsigned char* ws) {
  extern __shared__ __attribute__((aligned(16))) unsigned char lds_raw[];
  LAS unsigned char* L = (LAS unsigned char*)lds_raw;
  const int t = threadIdx.x, lane = t & 63, wave = __builtin_amdgcn_readfirstlane(t >> 6);
  const int b = blockIdx.x, xc = b & 7, r = ((b >> 3) + ROFF) & 31;
  const int n = lane & 15, q = lane >> 4;
  const rsrc_t rs = __builtin_amdgcn_make_buffer_rsrc((void*)ws, 0, WS_USED, 0x00020000);
  LAS unsigned char* SM = L + L_SM;
  const v4u sv4 = {SENT, SENT, SENT, SENT};
  const u64 tst = __builtin_amdgcn_s_memrealtime();
  (void)tst;
#define DELAY(x) do { while (__builtin_amdgcn_s_memrealtime() - tst < (x)) __builtin_amdgcn_s_sleep(2); } while (0)

  if (r < 16) {
    const int head = 4 * xc + (r >> 2), cs = r & 3;
    LAS unsigned char* hp = SM;
    LAS unsigned* q16 = (LAS unsigned*)(SM + 2304);
    LAS unsigned char* qst = SM + 8192;
    LAS unsigned char* WKI = L + 98304;
    LAS unsigned char* WQI = L + 65536;
    const int sfoff = SF_OFF + xc * 8192 + lane * 128;
    LAS unsigned* lcnt = (LAS unsigned*)(SM + 9472);
    const unsigned short cvl0 = cvec[(r & 7) * 64 + (t >> 4)], cvl1 = cvec[(r & 7) * 64 + 32 + (t >> 4)];
    bf16x8 wk[4];
    if (wave < 4) {
      const int suoff = SU_OFF + xc * 16384 + cs * 4096 + t * 16;
      const int sqoff = SQ_OFF + head * 2048 + (t & 127) * 16;
      const v4u snapu = ld16(rs, suoff);
      const v4u snapq = ld16(rs, sqoff);
      const unsigned snapf = ld8(rs, sfoff).y;
      if (t == 0) *lcnt = 0u;
      bar();
      v4u v = snapu; int spins = 0; bool ok = false;
      for (;;) {
        CBAR();
        if (!ok) v = ld16(rs, suoff);
        ok = (v.y != snapu.y) & (v.w != snapu.w);
        if (__all(ok) || ++spins > SPIN_CAP) break;
      }
      v2u hh = {v.x, v.z};
      *(LAS v2u*)(hp + ((t >> 4) & 7) * 272 + (t >> 7) * 128 + (t & 15) * 8) = hh;
      bar();
#pragma unroll
      for (int i = 0; i < 4; i++) wk[i] = *(const LAS bf16x8*)(WKI + (wave * 16 + n) * 256 + (((4 * i + q) ^ swz(n)) * 16));
      if (wave < 2) {
        spins = 0; ok = false;
        for (;;) {
          CBAR();
          if (!ok) v = ld16(rs, sqoff);
          ok = (v.y != snapq.y) & (v.w != snapq.w);
          if (__all(ok) || ++spins > SPIN_CAP) break;
        }
        v2u o2 = {v.x, v.z};
        *(LAS v2u*)(qst + (t >> 5) * 272 + (t & 31) * 8) = o2;
      } else if (wave == 2) {
        spins = 0; ok = false;
        for (;;) { CBAR(); if (!ok) ok = ld8(rs, sfoff).y != snapf; if (__all(ok) || ++spins > SPIN_CAP) break; }
      }
      bar();
    } else {
      const int lw = wave - 4;
      const int sqo = SQ_OFF + head * 2048 + cs * 512 + lw * 128 + ((lane >> 2) & 7) * 16;
      const v4u oldq = ld16(rs, sqo);
      bar();
#pragma unroll
      for (int i = 0; i < 16; i++) {
        const int j = 4 * i + lw, rg = j & 31, row = 4 * rg + q;
        const u16* src = (i < 8 ? w_q : w_uk) + (size_t)(cs * 128 + row) * 4096 + head * 128 + ((n ^ swz(row)) * 8);
        dma16(src, L + 65536 + j * 1024);
      }
      VMWAIT0();
      if (lane == 0) __hip_atomic_fetch_add(lcnt, 1u, __ATOMIC_RELAXED, __HIP_MEMORY_SCOPE_WORKGROUP);
      { int spins = 0; while (__hip_atomic_load(lcnt, __ATOMIC_RELAXED, __HIP_MEMORY_SCOPE_WORKGROUP) < 4u && ++spins < SPIN_CAP) {} }
      CBAR();
      bf16x8 wqf[8];
#pragma unroll
      for (int i = 0; i < 8; i++) wqf[i] = trfrag<256, true>(WQI, 32 * (i & 3), 4 * lw + 2 * (i >> 2), lane);
#pragma unroll
      for (int i = 0; i < 4; i++) wk[i] = *(const LAS bf16x8*)(WKI + (wave * 16 + n) * 256 + (((4 * i + q) ^ swz(n)) * 16));
      bar();
      f32x4 a0 = {0, 0, 0, 0}, a1 = {0, 0, 0, 0}; bf16x8 hf[4];
#pragma unroll
      for (int i = 0; i < 4; i++) hf[i] = *(const LAS bf16x8*)(hp + (n & 7) * 272 + i * 64 + q * 16);
      SB();
#pragma unroll
      for (int i = 0; i < 4; i++) { a0 = MFMA(hf[i], wqf[i], a0); a1 = MFMA(hf[i], wqf[4 + i], a1); }
      const float sq = red16((a0[0] + a0[1]) + (a0[2] + a0[3]), (a1[0] + a1[1]) + (a1[2] + a1[3]));
      const unsigned d = pk2(sq, dppf<0x101>(sq));
      const unsigned d2 = dppu<0x102>(d);
      if (lane < 32 && (lane & 3) == 0) { v4u o = {d, oldq.y + 1u, d2, oldq.w + 1u}; st16(rs, sqo, o); }
      {
        const int cb = S3_OFF + (head * 32 + cs * 8) * S3R + (t - 256) * 16;
        stclr(rs, cb, sv4); stclr(rs, cb + 4096, sv4);
        if (wave == 4) stclr(rs, cb + 8192, sv4);
        else if (wave == 5 && lane < 8) stclr(rs, S4_OFF + head * 512 + cs * 128 + lane * 16, sv4);
      }
      VMWAIT0();
      bar();
      if (lane < 32 && (lane & 3) == 0) { v4u o = {d, oldq.y + 2u, d2, oldq.w + 2u}; st16(rs, sqo, o); }
    }
    {
      f32x4 a = {0, 0, 0, 0}; bf16x8 qf[4];
#pragma unroll
      for (int i = 0; i < 4; i++) qf[i] = *(const LAS bf16x8*)(qst + (n & 3) * 272 + i * 64 + q * 16);
      SB();
      f32x4 ab = {0, 0, 0, 0};
      a = MFMA(wk[0], qf[0], a); ab = MFMA(wk[1], qf[1], ab); a = MFMA(wk[2], qf[2], a); ab = MFMA(wk[3], qf[3], ab);
      a += ab;
#pragma unroll
      for (int i = 0; i < 4; i++) { a[i] += dppf<0xB1>(a[i]); a[i] += dppf<0x4E>(a[i]); }
      const float SC = 0.08838834764831845f * 1.4426950408889634f;
      v2u o = {canon(pk2(a[0] * SC, a[1] * SC)), canon(pk2(a[2] * SC, a[3] * SC))};
      if (n == 0) *(LAS v2u*)(q16 + 64 + wave * 8 + q * 2) = o;
    }
    bar();
    if (wave < 2) { const v4u o = *(const LAS v4u*)(q16 + 64 + n * 4); st16(rs, S2_OFF + (wave * 4 + q) * 32768 + head * 1024 + cs * 256 + n * 16, o); }
    bar();
    {
      const int kj = r & 7, cg = xc * 2 + (r >> 3);
      const u16* pl = w_mod + (size_t)(kj * 64 + (t >> 4)) * 3072 + 1024 + cg * 128 + n * 8;
      v4u w0 = LDNT(v4u, pl), w1r = LDNT(v4u, pl + (size_t)32 * 3072);
      CBAR();
      if (r < 8) {
#pragma unroll
        for (int i = 0; i < 8; i++) {
          const int rg = 8 * i + wave, row = 8 * rg + (lane >> 3);
          glds16(w_o + (size_t)(xc * 512 + row) * 512 + r * 64 + (((lane & 7) ^ ((row & 2) | ((row >> 1) & 4))) * 8), (unsigned)(rg * 1024));
        }
        asm volatile("s_waitcnt vmcnt(8)" ::: "memory");
      }
      const float c0 = bflo(cvl0), c1 = bflo(cvl1);
      const unsigned s01 = pk2(c0 * __builtin_amdgcn_rcpf(1.f + __expf(-c0)), c1 * __builtin_amdgcn_rcpf(1.f + __expf(-c1)));
      interleave(w0, w1r);
      float a[8] = {dot2(w0.x, s01, 0.f), dot2(w0.y, s01, 0.f), dot2(w0.z, s01, 0.f), dot2(w0.w, s01, 0.f),
                    dot2(w1r.x, s01, 0.f), dot2(w1r.y, s01, 0.f), dot2(w1r.z, s01, 0.f), dot2(w1r.w, s01, 0.f)};
      float b0 = red32(a[0], a[4]), b1x = red32(a[1], a[5]), b2x = red32(a[2], a[6]), b3 = red32(a[3], a[7]);
      f32x2 o = {red16(b0, b2x), red16(b1x, b3)};
      LAS float* apart = (LAS float*)(SM + 12288);
      *(LAS f32x2*)(apart + wave * 128 + n * 8 + 4 * (lane >> 5) + 2 * ((lane >> 4) & 1)) = o;
      bar();
      if (wave == 7) {
        f32x2 sacc = {0.f, 0.f};
#pragma unroll
        for (int w = 0; w < 8; w++) sacc += *(LAS f32x2*)(apart + w * 128 + lane * 2);
        const unsigned d = canon(pk2(sacc.x, sacc.y));
        v4u o4 = {dppu<0x00>(d), dppu<0x55>(d), dppu<0xAA>(d), dppu<0xFF>(d)};
        if ((lane & 3) == 0) st16(rs, SL_OFF + kj * 4096 + cg * 256 + lane * 4, o4);
        if (cg < 4 || cg >= 12) {
          const unsigned f0 = canon(__float_as_uint(sacc.x)), f1 = canon(__float_as_uint(sacc.y));
          v4u of = {f0, f1, dppu<0x101>(f0), dppu<0x101>(f1)};
          if (!(lane & 1)) st16(rs, SLF_OFF + kj * 4096 + (cg < 4 ? cg : cg - 8) * 512 + lane * 8, of);
        }
      }
      bar();
    }
    if (r < 8) {
      LAS float* fpart = (LAS float*)(SM + 3072);
      LAS unsigned char* cst = SM + 4096;
      LAS float* fp64 = (LAS float*)(SM + 16384);
      VMWAIT0();
      bar();
      bf16x8 wof[8];
      {
        const int g = lane >> 4, qq = (lane & 15) >> 2, pp = lane & 3;
#pragma unroll
        for (int i = 0; i < 8; i++) {
          s16x4 h[2];
#pragma unroll
          for (int tt = 0; tt < 2; tt++) {
            const int row = 32 * (2 * wave + (i >> 2)) + 8 * g + 4 * tt + qq;
            const int ch = (2 * (i & 3) + (pp >> 1)) ^ ((row & 2) | ((row >> 1) & 4));
            h[tt] = __builtin_amdgcn_ds_read_tr16_b64_v4i16((LAS s16x4*)(L + row * 128 + ch * 16 + (pp & 1) * 8));
          }
          wof[i] = (bf16x8){h[0].x, h[0].y, h[0].z, h[0].w, h[1].x, h[1].y, h[1].z, h[1].w};
        }
      }
      if (wave >= 4) {
        int spins = 0;
        for (;;) { CBAR(); v4u v = ld16(rs, S2_OFF + xc * 32768 + xc * 4096 + (t - 256) * 16); if (__all(mx4(v) != SENT) || ++spins > SPIN_CAP) break; __builtin_amdgcn_s_sleep(2); }
      }
      bar();
      {
        v4u av0, av1; int spins = 0;
        const int off = S4_OFF + ((4 * xc + (wave >> 1)) * 2 + (n & 1)) * 256 + (wave & 1) * 128 + q * 16;
        for (;;) { CBAR(); av0 = ld16(rs, off); av1 = ld16(rs, off + 64); if (__all(max(mx4(av0), mx4(av1)) != SENT) || ++spins > SPIN_CAP) break; }
        f32x4 a[4];
#pragma unroll
        for (int T = 0; T < 4; T++) { a[T] = MFMA(__builtin_bit_cast(bf16x8, av0), wof[T], ((f32x4){0, 0, 0, 0})); a[T] = MFMA(__builtin_bit_cast(bf16x8, av1), wof[4 + T], a[T]); }
        const f32x4 sel = q == 0 ? a[0] : q == 1 ? a[1] : q == 2 ? a[2] : a[3];
        fp64[wave * 64 + lane] = sel[0] + sel[1];
        bar();
        if (wave == 0) {
          float v = 0.f;
#pragma unroll
          for (int w = 0; w < 8; w++) v += fp64[w * 64 + lane];
          const unsigned d = canon(pk2(v, dppf<0x101>(v)));
          if (!(lane & 1)) ((LAS unsigned*)cst)[lane >> 1] = d;
          LGWAIT0();
          const v4u o = *(const LAS v4u*)(cst + (lane & 7) * 16);
          st16(rs, S5_OFF + (lane >> 3) * 8192 + xc * 1024 + r * 128 + (lane & 7) * 16, o);
        }
      }
    } else {
      const int ti = xc * 8 + (r - 8);
      LAS unsigned char* W2I = L;
      const float b2v = bflo(b2[ti * 8 + (lane & 7)]);
      const float x8 = bflo(x[ti * 8 + (lane & 7)]);
      CBAR();
#pragma unroll
      for (int i = 0; i < 3; i++) { const int rg = 8 * i + wave; if (rg < 22) { int row = rg * 64 + lane; row = row < 1376 ? row : 1375; dma16n(w2 + (size_t)row * 512 + ti * 8, W2I + rg * 1024); } }
      CBAR();
      LAS float* hpart = (LAS float*)(SM + 2048);
      LAS unsigned* sbuf = (LAS unsigned*)(SM + 2560);
      VMWAIT0();
      bar();
      bf16x8 w2f[6];
#pragma unroll
      for (int i = 0; i < 6; i++) {
        const int ks = wave + 8 * i < 43 ? wave + 8 * i : 0;
        const int g = lane >> 4, qq = (lane & 15) >> 2, pp = lane & 3;
        s16x4 h0 = __builtin_amdgcn_ds_read_tr16_b64_v4i16((LAS s16x4*)(W2I + (32 * ks + 8 * g + qq) * 16 + (pp & 1) * 8));
        s16x4 h1 = __builtin_amdgcn_ds_read_tr16_b64_v4i16((LAS s16x4*)(W2I + (32 * ks + 8 * g + 4 + qq) * 16 + (pp & 1) * 8));
        w2f[i] = (bf16x8){h0.x, h0.y, h0.z, h0.w, h1.x, h1.y, h1.z, h1.w};
      }
      float xn8 = 0.f, gbv = 0.f;
      if (wave == 0) {
        const int off = lane < 16 ? S5_OFF + xc * 8192 + (lane & 7) * 1024 + ti * 16 : SLF_OFF + ((lane >> 1) & 7) * 4096 + ((lane & 32) ? 2048 : 0) + ti * 32 + (lane & 1) * 16;
        v4u v; int spins = 0;
        for (;;) { CBAR(); v = ld16(rs, off); if (__all(mx4(v) != SENT) || ++spins > SPIN_CAP) break; __builtin_amdgcn_s_sleep(4); }
        if (lane < 48) *(LAS v4u*)(sbuf + lane * 4) = v;
        LGWAIT0();
        const LAS u16* sh = (const LAS u16*)sbuf; const LAS float* sf = (const LAS float*)sbuf;
        float ao = 0, ga8 = 0;
#pragma unroll
        for (int k = 0; k < 8; k++) { ao += bflo(sh[k * 8 + (lane & 7)]); ga8 += sf[64 + k * 8 + (lane & 7)]; gbv += sf[128 + k * 8 + (lane & 7)]; }
        xn8 = x8 + ga8 * ao;
      }
      bar();
      {
        v4u hv[6]; int spins = 0;
        const int off = S6_OFF + xc * 6144 + wave * 128 + q * 16, off5 = wave < 3 ? off + 5120 : off;
        for (;;) {
          CBAR();
#pragma unroll
          for (int i = 0; i < 5; i++) hv[i] = ld16(rs, off + i * 1024);
          hv[5] = ld16(rs, off5);
          const unsigned m = max(max(max(mx4(hv[0]), mx4(hv[1])), max(mx4(hv[2]), mx4(hv[3]))), max(mx4(hv[4]), mx4(hv[5])));
          if (__all(m != SENT) || ++spins > SPIN_CAP) break;
        }
        if (wave >= 3) hv[5] = (v4u){0, 0, 0, 0};
        f32x4 a = {0, 0, 0, 0}, a2 = {0, 0, 0, 0};
#pragma unroll
        for (int i = 0; i < 3; i++) { a = MFMA(__builtin_bit_cast(bf16x8, hv[2 * i]), w2f[2 * i], a); a2 = MFMA(__builtin_bit_cast(bf16x8, hv[2 * i + 1]), w2f[2 * i + 1], a2); }
        if (lane < 8) hpart[wave * 8 + lane] = a[0] + a2[0];
      }
      bar();
      if (wave == 0) {
        float a = hpart[lane];
        a += dppf<0x128>(a); a = red16(a, a); a = red32(a, a);
        const float res = xn8 + gbv * (a + b2v);
        if (lane < 8) out[ti * 8 + lane] = __builtin_bit_cast(u16, (__bf16)res);
      }
    }
  } else if (r >= 24) {
    const int e = r - 24, head = 4 * xc + (e >> 1), half = e & 1, ti = xc * 8 + e;
    const int gi = e * 8 + xc;
    const int wst = (gi < 43 ? gi : 0) * 4;
    const int mcol = wave * 64 + 8 * (lane & 7) + 4 * (lane >> 5) + 2 * ((lane >> 4) & 1) + ((lane >> 3) & 1);
    LAS unsigned char* W1I = L + 65536;
    LAS unsigned char* STG = L + 98304;
    LAS u16* scl = (LAS u16*)(SM);
    LAS unsigned* cc16 = (LAS unsigned*)(SM + 1024);
    LAS unsigned* ml = (LAS unsigned*)(SM + 2048);
    LAS float* stat = (LAS float*)(SM + 5120);
    LAS u16* y16 = (LAS u16*)(SM + 5376);
    LAS float* gpart = (LAS float*)(SM + 6400);
    LAS unsigned* pub = (LAS unsigned*)(SM + 7424);
    LAS unsigned* cnt = (LAS unsigned*)(SM + 7680);
    LAS unsigned* tbase = (LAS unsigned*)(SM + 8192);
    LAS float* apart = (LAS float*)(SM + 10240);
    LAS float* fpart = (LAS float*)(SM + 14336);
    LAS unsigned char* cst = SM + 15360;
    const int puoff = SU_OFF + q * 16384 + (xc * 8 + e) * 256 + n * 16;
    const int pfoff = SF_OFF + n * 8192 + ti * 128;
    const int sfoff = SF_OFF + (8 + xc) * 8192 + lane * 128;
#define LDSADD(p) __hip_atomic_fetch_add((p), 1u, __ATOMIC_RELAXED, __HIP_MEMORY_SCOPE_WORKGROUP)
#define REPUB() do { v2u p2_ = *(LAS v2u*)(pub + n * 2); v4u tb_ = *(LAS v4u*)(tbase + lane * 8); unsigned tf_ = tbase[lane * 8 + 4]; unsigned k_ = 0; if (lane == 0) k_ = LDSADD(cnt); k_ = __builtin_amdgcn_readfirstlane(k_); \
      v4u o_ = {p2_.x, tb_.x + k_, p2_.y, tb_.y + k_}; st16(rs, puoff, o_); v4u o2_ = {p2_.x, tb_.z + k_, p2_.y, tb_.w + k_}; st16(rs, puoff + 65536, o2_); \
      if (lane < 16) { v2u f_ = {0u, tf_ + k_}; st8(rs, pfoff, f_); } } while (0)
    v2u pd = {0, 0};
    v4u old0 = {0, 0, 0, 0}, old1 = {0, 0, 0, 0}, xv = {0, 0, 0, 0}; unsigned snapf = 0, xw = 0;
    v4u wm[2]; unsigned short cv[2];
    {
      const u16* p = w_mod + (size_t)(e * 64 + (t >> 4)) * 3072 + (n >> 3) * 512 + xc * 64 + (n & 7) * 8;
      wm[0] = LDNT(v4u, p); wm[1] = LDNT(v4u, p + (size_t)32 * 3072);
      cv[0] = cvec[e * 64 + (t >> 4)]; cv[1] = cvec[e * 64 + 32 + (t >> 4)];
    }
    CBAR();
    const unsigned oldf = ld8(rs, pfoff).y;
    if (wave == 0) { old0 = ld16(rs, puoff); old1 = ld16(rs, puoff + 65536); xv = *(const v4u*)(x + lane * 8); xw = ((const unsigned*)x)[xc * 32 + (lane & 31)]; }
    if (wave == 1) snapf = ld8(rs, sfoff).y;
    CBAR();
    {
      const int pc = t >= 64 ? t - 64 : t;
      st16(rs, wave == 0 ? DUMMY_OFF + ti * 1024 + lane * 16 : S2_OFF + ti * (EGC_PIECES * 16) + (pc < EGC_PIECES ? pc : pc - EGC_PIECES) * 16, sv4);
    }
    CBAR();
    float ln0 = 0.f, ln1 = 0.f;
    if (wave == 0) {
      float xs[8] = {bflo(xv.x), bfhi(xv.x), bflo(xv.y), bfhi(xv.y), bflo(xv.z), bfhi(xv.z), bflo(xv.w), bfhi(xv.w)};
      float s = 0;
#pragma unroll
      for (int i = 0; i < 8; i++) s += xs[i];
      const float mean = wave_sum(s) * (1.f / 512.f);
      float vs = 0;
#pragma unroll
      for (int i = 0; i < 8; i++) { float d = xs[i] - mean; vs += d * d; }
      const float rstd = rsqrtf(wave_sum(vs) * (1.f / 512.f) + 1e-5f);
      ln0 = (bflo(xw) - mean) * rstd; ln1 = (bfhi(xw) - mean) * rstd;
      v4u tb = {old0.y, old0.w, old1.y, old1.w};
      *(LAS v4u*)(tbase + lane * 8) = tb; tbase[lane * 8 + 4] = oldf;
      if (lane == 0) { cnt[0] = 2u + NREP; cnt[1] = 0u; }
    }
    CBAR();
    {
      const float c0 = bflo(cv[0]), c1 = bflo(cv[1]);
      const unsigned s01 = pk2(c0 * __builtin_amdgcn_rcpf(1.f + __expf(-c0)), c1 * __builtin_amdgcn_rcpf(1.f + __expf(-c1)));
      interleave(wm[0], wm[1]);
      float a[8] = {dot2(wm[0].x, s01, 0.f), dot2(wm[0].y, s01, 0.f), dot2(wm[0].z, s01, 0.f), dot2(wm[0].w, s01, 0.f),
                    dot2(wm[1].x, s01, 0.f), dot2(wm[1].y, s01, 0.f), dot2(wm[1].z, s01, 0.f), dot2(wm[1].w, s01, 0.f)};
      float b0 = red32(a[0], a[4]), b1x = red32(a[1], a[5]), b2x = red32(a[2], a[6]), b3 = red32(a[3], a[7]);
      f32x2 o = {red16(b0, b2x), red16(b1x, b3)};
      *(LAS f32x2*)(apart + wave * 128 + n * 8 + 4 * (lane >> 5) + 2 * ((lane >> 4) & 1)) = o;
    }
    bar();
    if (wave == 0) {
      f32x2 sacc = {0.f, 0.f};
#pragma unroll
      for (int w = 0; w < 8; w++) sacc += *(LAS f32x2*)(apart + w * 128 + lane * 2);
      auto r0 = __builtin_amdgcn_permlane32_swap(__float_as_uint(sacc.x), __float_as_uint(sacc.x), false, false);
      auto r1 = __builtin_amdgcn_permlane32_swap(__float_as_uint(sacc.y), __float_as_uint(sacc.y), false, false);
      const float one = e == 0 ? 1.f : 0.f;
      const float u0 = ln0 * (one + __uint_as_float(r0[1])) + __uint_as_float(r0[0]), u1 = ln1 * (one + __uint_as_float(r1[1])) + __uint_as_float(r1[0]);
      if (lane < 32) pub[lane] = pk2(u0, u1);
      LGWAIT0();
      pd = *(LAS v2u*)(pub + n * 2);
      v4u o = {pd.x, old0.y + 1u, pd.y, old0.w + 1u}; st16(rs, puoff, o);
      v4u o2 = {pd.x, old1.y + 1u, pd.y, old1.w + 1u}; st16(rs, puoff + 65536, o2);
    }
    unsigned karr = 0;
    if (wave != 0) { VMWAIT0(); if (lane == 0) karr = LDSADD(cnt + 1); }
    const bool lastw = __builtin_amdgcn_readfirstlane(karr) == 6u;
    if (lastw && lane < 16) { v2u f = {0u, oldf + 1u}; st8(rs, pfoff, f); }
#pragma unroll
    for (int i = 0; i < 4; i++) if (gi < 43) { const int rg = 8 * i + wave; dma16n(w1 + (size_t)(rg * 16 + (lane >> 2)) * 1376 + wst * 8 + (lane & 3) * 8, W1I + rg * 1024); }
    CBAR();
    DELAY(EGL_DELAY);
    const float xme = bflo(x[mcol]);
    const float b1v = bflo(b1[wst * 8 + (lane & 31)]);
    CBAR();
#pragma unroll
    for (int i = 0; i < 8; i++) {
      const int rg = 8 * i + wave, row = 4 * rg + q;
      dma16(w_uv + (size_t)(half * 256 + row) * 4096 + head * 128 + ((n ^ swz(row)) * 8), L + rg * 1024);
    }
    CBAR();
    if (wave == 0 || lastw) {
#pragma unroll 1
      for (int k = 0; k < NREP; k++) {
        __builtin_amdgcn_s_sleep(14);
        if (wave == 0) { v4u o_ = {pd.x, old0.y + 2u + k, pd.y, old0.w + 2u + k}; st16(rs, puoff, o_); v4u o2_ = {pd.x, old1.y + 2u + k, pd.y, old1.w + 2u + k}; st16(rs, puoff + 65536, o2_); }
        if (lastw && lane < 16) { v2u f_ = {0u, oldf + 2u + k}; st8(rs, pfoff, f_); }
      }
    }
    VMWAIT0();
    bar();
    bf16x8 wvf[16], w1f[4];
#pragma unroll
    for (int s2 = 0; s2 < 16; s2++) wvf[s2] = trfrag<256, true>(L, 64 * (wave & 3) + 32 * (s2 >> 3), 2 * (s2 & 7), lane);
#pragma unroll
    for (int i = 0; i < 4; i++) w1f[i] = trfrag<64, false>(W1I, 64 * wave + 32 * (i >> 1), 2 * (i & 1), lane);
    if (wave == 1) {
      int spins = 0; bool ok = false;
      for (;;) { CBAR(); if (!ok) ok = ld8(rs, sfoff).y != snapf; if (__all(ok) || ++spins > SPIN_CAP) break; if ((spins & 3) == 0) REPUB(); __builtin_amdgcn_s_sleep(2); }
      spins = 0;
      for (;;) { CBAR(); v4u v = ld16(rs, S2_OFF + xc * 32768 + head * 1024 + lane * 16); if (__all(mx4(v) != SENT) || ++spins > SPIN_CAP) break; if ((spins & 3) == 0) REPUB(); __builtin_amdgcn_s_sleep(2); }
    }
    bar();
    {
      LAS float* sclf = (LAS float*)(SM);
      LAS float* epart = (LAS float*)(STG);
      {
        const int soff = S3_OFF + (head * 32 + (lane & 31)) * S3R + 1024;
        if (wave == 7) {
          v2u stt; int spins = 0;
          for (;;) { CBAR(); stt = ld8(rs, soff); if (__all(max(stt.x, stt.y) != SENT) || ++spins > SPIN_CAP) break; if ((spins & 3) == 0) REPUB(); }
          const float m = __uint_as_float(stt.x), l = __uint_as_float(stt.y);
          const float mg = wave_max(m);
          const float sc = __builtin_amdgcn_exp2f(m - mg);
          const float il = 2.f * __builtin_amdgcn_rcpf(wave_sum(sc * l));
          if (lane < 32) sclf[lane] = sc * il;
        }
      }
      v4u p0, p1; int spins = 0;
      const int off = S3_OFF + (head * 32 + (wave >> 2) * 16 + n) * S3R + half * 512 + (wave & 3) * 128 + q * 16;
      for (;;) {
        CBAR();
        p0 = ld16(rs, off); p1 = ld16(rs, off + 64);
        if (__all(max(mx4(p0), mx4(p1)) != SENT) || ++spins > SPIN_CAP) break;
        if ((spins & 15) == 0) REPUB();
      }
      f32x4 y0[8];
#pragma unroll
      for (int T = 0; T < 8; T++) { y0[T] = MFMA(__builtin_bit_cast(bf16x8, p0), wvf[T], ((f32x4){0, 0, 0, 0})); y0[T] = MFMA(__builtin_bit_cast(bf16x8, p1), wvf[8 + T], y0[T]); }
      bar();
      const f32x4 s4 = *(const LAS f32x4*)(sclf + 16 * (wave >> 2) + 4 * q);
      float z[8];
#pragma unroll
      for (int T = 0; T < 8; T++) z[T] = (y0[T][0] * s4[0] + y0[T][1] * s4[1]) + (y0[T][2] * s4[2] + y0[T][3] * s4[3]);
      const float r0 = red32(z[0], z[4]), r1 = red32(z[1], z[5]), r2 = red32(z[2], z[6]), r3 = red32(z[3], z[7]);
      const float u0 = red16(r0, r2), u1 = red16(r1, r3);
      const int T0 = 4 * (lane >> 5) + 2 * ((lane >> 4) & 1);
      epart[wave * 128 + T0 * 16 + n] = u0; epart[wave * 128 + T0 * 16 + 16 + n] = u1;
      bar();
      if (wave == 0) {
        f32x2 sacc = {0.f, 0.f};
#pragma unroll
        for (int w = 0; w < 8; w++) sacc += *(LAS f32x2*)(epart + w * 128 + lane * 2);
        const unsigned d = canon(pk2(sacc.x, sacc.y));
        v4u o4 = {dppu<0x00>(d), dppu<0x55>(d), dppu<0xAA>(d), dppu<0xFF>(d)};
        if ((lane & 3) == 0) st16(rs, S4_OFF + (head * 2 + half) * 256 + lane * 4, o4);
      }
    }
    if (gi >= 43) {
      if (wave == 0) {
        int spins = 0;
        for (;;) {
          CBAR(); v4u v = ld16(rs, DN_OFF + xc * 128);
          if (v.x != SENT || ++spins > SPIN_CAP) break;
          if ((spins & 3) == 0) REPUB();
          __builtin_amdgcn_s_sleep(8);
        }
      } return;
    }
    {
      v4u v0, v1, v2; int spins = 0;
      const int off = SL_OFF + wave * 4096 + lane * 16;
      for (;;) { CBAR(); v0 = ld16(rs, off); v1 = ld16(rs, off + 1024); v2 = ld16(rs, off + 2048); if (__all(max(max(mx4(v0), mx4(v1)), mx4(v2)) != SENT) || ++spins > SPIN_CAP) break; if ((spins & 15) == 0) REPUB(); __builtin_amdgcn_s_sleep(1); }
      *(LAS v4u*)(L + wave * 3072 + lane * 16) = v0; *(LAS v4u*)(L + wave * 3072 + 1024 + lane * 16) = v1; *(LAS v4u*)(L + wave * 3072 + 2048 + lane * 16) = v2;
    }
    bar();
    float gam = 0.f, sbm = 0.f, cb1 = 0.f;
    {
      const LAS u16* mlh = (const LAS u16*)L;
#pragma unroll
      for (int k = 0; k < 8; k++) { gam += bflo(mlh[k * 1536 + mcol]); sbm += bflo(mlh[k * 1536 + 512 + mcol]); cb1 += bflo(mlh[k * 1536 + 1024 + mcol]); }
      sbm = rbf(sbm); cb1 = rbf(1.f + cb1);
    }
    LAS u16* cs16 = (LAS u16*)(SM + 10240);
    LAS float* gp64 = (LAS float*)(SM + 12288);
    cs16[mcol] = __builtin_bit_cast(u16, (__bf16)cb1); cs16[512 + mcol] = __builtin_bit_cast(u16, (__bf16)sbm);
    LGWAIT0();
    bf16x8 auxf[2];
    {
      const bf16x8 ones = {0x3F80, 0x3F80, 0x3F80, 0x3F80, 0x3F80, 0x3F80, 0x3F80, 0x3F80};
#pragma unroll
      for (int i = 0; i < 2; i++) { const bf16x8 v = *(const LAS bf16x8*)(cs16 + ((n - 1) & 1) * 512 + wave * 64 + i * 32 + q * 8); auxf[i] = n == 0 ? ones : v; }
      f32x4 k0 = MFMA(auxf[0], w1f[0], ((f32x4){0, 0, 0, 0})), k1 = MFMA(auxf[0], w1f[1], ((f32x4){0, 0, 0, 0})), kg = MFMA(auxf[0], auxf[0], ((f32x4){0, 0, 0, 0}));
      k0 = MFMA(auxf[1], w1f[2], k0); k1 = MFMA(auxf[1], w1f[3], k1); kg = MFMA(auxf[1], auxf[1], kg);
      if (lane < 16) {
        LAS float* g = (LAS float*)(L + 32768) + wave * 192;
        g[n] = k0[0]; g[16 + n] = k1[0]; g[32 + n] = kg[0]; g[64 + n] = k0[1]; g[80 + n] = k1[1]; g[96 + n] = kg[1]; g[128 + n] = k0[2]; g[144 + n] = k1[2]; g[160 + n] = kg[2];
      }
    }
    bar();
    float Ow = 0.f, Cw = 0.f, Sw = 0.f, Kc = 0.f, Ks = 0.f, Kc2 = 0.f, Kcs = 0.f, Ks2 = 0.f;
    if (wave == 0) {
      const LAS float* g = (const LAS float*)(L + 32768);
      float o = 0.f, c = 0.f, sg = 0.f;
#pragma unroll
      for (int w = 0; w < 8; w++) { o += g[w * 192 + (lane < 48 ? lane : 0)]; c += g[w * 192 + 64 + (lane < 48 ? lane : 0)]; sg += g[w * 192 + 128 + (lane < 48 ? lane : 0)]; }
      Ow = o; Cw = c; Sw = sg;
      Kc = RL(o, 33); Ks = RL(o, 34); Kc2 = RL(c, 33); Kcs = RL(c, 34); Ks2 = RL(sg, 34);
    }
    asm volatile("" : "+v"(gam), "+v"(cb1), "+v"(Ow), "+v"(Cw), "+v"(Sw));
    {
      v4u v; int spins = 0;
      const int off = S5_OFF + xc * 8192 + (lane >> 3) * 1024 + wave * 128 + (lane & 7) * 16;
      for (;;) { CBAR(); v = ld16(rs, off); if (__all(mx4(v) != SENT) || ++spins > SPIN_CAP) break; if ((spins & 15) == 0) REPUB(); }
      const float b0 = red32(bflo(v.x), bflo(v.z)), b1x = red32(bfhi(v.x), bfhi(v.z)), b2x = red32(bflo(v.y), bflo(v.w)), b3 = red32(bfhi(v.y), bfhi(v.w));
      const float c0 = red16(b0, b2x), c1 = red16(b1x, b3);
      const bool up = lane & 8;
      const float keep = up ? c1 : c0, send = up ? c0 : c1;
      const float ao = keep + dppf<0x128>(send);
      const __bf16 xb = (__bf16)(xme + gam * ao);
      cs16[mcol] = __builtin_bit_cast(u16, (__bf16)((float)xb * cb1)); cs16[512 + mcol] = __builtin_bit_cast(u16, xb);
      LGWAIT0();
      const bf16x8 y0 = *(const LAS bf16x8*)(cs16 + (n & 1) * 512 + wave * 64 + q * 8), y1 = *(const LAS bf16x8*)(cs16 + (n & 1) * 512 + wave * 64 + 32 + q * 8);
      f32x4 z0 = MFMA(y0, w1f[0], ((f32x4){0, 0, 0, 0})), z1 = MFMA(y0, w1f[1], ((f32x4){0, 0, 0, 0})), ax = MFMA(y0, auxf[0], ((f32x4){0, 0, 0, 0})), gr = MFMA(y0, y0, ((f32x4){0, 0, 0, 0}));
      z0 = MFMA(y1, w1f[2], z0); z1 = MFMA(y1, w1f[3], z1); ax = MFMA(y1, auxf[1], ax); gr = MFMA(y1, y1, gr);
      const float lastq = n == 0 ? ax[1] : n == 1 ? gr[1] : gr[0];
      gp64[wave * 64 + lane] = q == 0 ? z0[0] : q == 1 ? z1[0] : q == 2 ? ax[0] : lastq;
    }
    bar();
    if (wave == 0) {
      float a = 0;
#pragma unroll
      for (int w = 0; w < 8; w++) a += gp64[w * 64 + lane];
      const float B1 = RL(a, 32), B3 = RL(a, 33), B4 = RL(a, 34), A1 = RL(a, 48), A2 = RL(a, 49), B2 = RL(a, 50);
      const float mu = A1 * (1.f / 512.f), rr = __builtin_amdgcn_rsqf(fmaxf(A2 * (1.f / 512.f) - mu * mu, 0.f) + 1e-5f);
      const float m2 = (rr * (B1 - mu * Kc) + Ks) * (1.f / 512.f);
      const float e2 = (rr * rr * (B2 - 2.f * mu * B3 + mu * mu * Kc2) + 2.f * rr * (B4 - mu * Kcs) + Ks2) * (1.f / 512.f);
      const float r2 = __builtin_amdgcn_rsqf(fmaxf(e2 - m2 * m2, 0.f) + 1e-5f);
      const float zz = r2 * (rr * (a - mu * Cw) + Sw - m2 * Ow);
      const float z = zz + b1v;
      const float u = z * (0.7978845608028654f + 0.035677408136300125f * z * z);
      const float gl = z * __builtin_amdgcn_rcpf(1.f + __builtin_amdgcn_exp2f(-2.885390081777927f * u));
      const unsigned d = canon(pk2(gl, dppf<0x101>(gl)));
      if (lane < 32 && !(lane & 1)) ((LAS unsigned*)y16)[lane >> 1] = d;
      LGWAIT0();
      const v4u o = *(const LAS v4u*)((LAS unsigned*)y16 + (lane & 3) * 4);
      if (lane < 32) st16(rs, S6_OFF + (lane >> 2) * 6144 + gi * 128 + (lane & 3) * 16, o);
    }
    if (ti == 0 && wave == 1 && lane < 8) { v4u o = {1, 1, 1, 1}; st16(rs, DN_OFF + lane * 128, o); }
  } else {
    const int e = r - 16, split = 4 * xc + (e & 3), hg = e >> 2;
    const int sfoff = SF_OFF + (8 + xc) * 8192 + lane * 128;
    LAS unsigned char* qa = SM;
    LAS unsigned char* Pm = SM + 16896;
    LAS float* wmax = (LAS float*)(SM + 21504);
    LAS float* wsum = wmax + 128;
    unsigned snapf = 0;
    if (wave == 0) snapf = ld8(rs, sfoff).y;
    bar();
    CBAR();
#pragma unroll
    for (int i = 0; i < 16; i++) {
      const int key = ((i * 8 + wave) + hg * 64) & 127;
      dma16(ckv + (size_t)(split * 128 + key) * 512 + ((lane ^ swz(key)) * 8), L + key * 1024);
    }
    VMWAIT0();
    bar();
    bf16x8 ka[16], va[16];
    {
      const int key = wave * 16 + n, f2 = swz(key);
#pragma unroll
      for (int s2 = 0; s2 < 16; s2++) ka[s2] = *(const LAS bf16x8*)(L + key * 1024 + (((4 * s2 + q) ^ f2) * 16));
#pragma unroll
      for (int i = 0; i < 16; i++) va[i] = trfrag<1024, true>(L, 32 * (i & 3), wave * 8 + 2 * (i >> 2), lane);
    }
    if (wave == 0) {
      int spins = 0; bool ok = false;
      for (;;) { CBAR(); if (!ok) ok = ld8(rs, sfoff).y != snapf; if (__all(ok) || ++spins > SPIN_CAP) break; __builtin_amdgcn_s_sleep(1); }
    }
    bar();
    {
      const int off = S2_OFF + xc * 32768 + hg * 16384 + t * 16;
      v4u v0, v1; int spins = 0;
      for (;;) {
        CBAR();
        v0 = ld16(rs, off); v1 = ld16(rs, off + 8192);
        if (__all(max(mx4(v0), mx4(v1)) != SENT) || ++spins > SPIN_CAP) break;
      }
      *(LAS v4u*)(qa + wave * 1056 + lane * 16) = v0;
      *(LAS v4u*)(qa + (8 + wave) * 1056 + lane * 16) = v1;
    }
    bar();
    f32x4 sc = {0, 0, 0, 0};
    {
      bf16x8 qf[16];
#pragma unroll
      for (int s2 = 0; s2 < 16; s2++) qf[s2] = *(const LAS bf16x8*)(qa + n * 1056 + s2 * 64 + q * 16);
      SB();
      f32x4 sc2 = {0, 0, 0, 0};
#pragma unroll
      for (int s2 = 0; s2 < 8; s2++) { sc = MFMA(ka[2 * s2], qf[2 * s2], sc); sc2 = MFMA(ka[2 * s2 + 1], qf[2 * s2 + 1], sc2); }
      sc += sc2;
    }
    float mx = fmaxf(fmaxf(sc[0], sc[1]), fmaxf(sc[2], sc[3]));
    mx = max16(mx); mx = max32(mx);
    if (lane < 16) wmax[lane * 8 + wave] = mx;
    bar();
    {
      f32x4 m0 = *(LAS f32x4*)(wmax + n * 8), m1 = *(LAS f32x4*)(wmax + n * 8 + 4);
      mx = fmaxf(fmaxf(fmaxf(m0[0], m0[1]), fmaxf(m0[2], m0[3])), fmaxf(fmaxf(m1[0], m1[1]), fmaxf(m1[2], m1[3])));
    }
    const float p0 = __builtin_amdgcn_exp2f(sc[0] - mx), p1 = __builtin_amdgcn_exp2f(sc[1] - mx), p2 = __builtin_amdgcn_exp2f(sc[2] - mx), p3 = __builtin_amdgcn_exp2f(sc[3] - mx);
    { v2u o = {pk2(p0, p1), pk2(p2, p3)}; *(LAS v2u*)(Pm + n * 288 + (wave * 16 + q * 4) * 2) = o; }
    float ps = (p0 + p1) + (p2 + p3);
    ps = red16(ps, ps); ps = red32(ps, ps);
    if (lane < 16) wsum[lane * 8 + wave] = ps;
    bar();
    {
      if (wave == 7) {
        f32x4 s0 = *(LAS f32x4*)(wsum + n * 8), s1 = *(LAS f32x4*)(wsum + n * 8 + 4);
        const float l = ((s0[0] + s0[1]) + (s0[2] + s0[3])) + ((s1[0] + s1[1]) + (s1[2] + s1[3]));
        v2u o2 = {canon(__float_as_uint(mx)), canon(__float_as_uint(l))};
        if (lane < 16) st8(rs, S3_OFF + ((hg * 16 + lane) * 32 + split) * S3R + 1024, o2);
      }
      bf16x8 bp[4];
#pragma unroll
      for (int ks = 0; ks < 4; ks++) bp[ks] = *(const LAS bf16x8*)(Pm + n * 288 + ks * 64 + q * 16);
      SB();
      f32x4 acc[4];
#pragma unroll
      for (int T = 0; T < 4; T++) {
        acc[T] = (f32x4){0, 0, 0, 0};
#pragma unroll
        for (int ks = 0; ks < 4; ks++) acc[T] = MFMA(va[T * 4 + ks], bp[ks], acc[T]);
      }
      unsigned d0[4], d1[4];
#pragma unroll
      for (int T = 0; T < 4; T++) { d0[T] = canon(pk2(acc[T][0], acc[T][1])); d1[T] = canon(pk2(acc[T][2], acc[T][3])); }
      auto r0 = __builtin_amdgcn_permlane16_swap(d0[0], d0[1], false, false);
      auto r1 = __builtin_amdgcn_permlane16_swap(d1[0], d1[1], false, false);
      auto r2 = __builtin_amdgcn_permlane16_swap(d0[2], d0[3], false, false);
      auto r3 = __builtin_amdgcn_permlane16_swap(d1[2], d1[3], false, false);
      v4u oa = {r0[0], r1[0], r0[1], r1[1]}, ob = {r2[0], r3[0], r2[1], r3[1]};
      const v4u obs = {dppu<0x128>(ob.x), dppu<0x128>(ob.y), dppu<0x128>(ob.z), dppu<0x128>(ob.w)};
      const bool lo = n < 8;
      const int o3b = S3_OFF + ((hg * 16 + (n & 7)) * 32 + split) * S3R + (wave * 64 + (q & 1) * 16 + (q & 2) * 4) * 2;
      st16(rs, o3b + (lo ? 0 : 64), lo ? oa : obs);
      st16(rs, o3b + 8 * 32 * S3R + (lo ? 64 : 0), lo ? obs : oa);
    }
  }
}

extern "C" void kernel_launch(void* const* d_in, const int* in_sizes, int n_in, void* d_out, int out_size, void* d_ws, size_t ws_size, hipStream_t stream) {
  (void)in_sizes; (void)n_in; (void)out_size; (void)ws_size;
  (void)hipFuncSetAttribute((const void*)fused_kernel, hipFuncAttributeMaxDynamicSharedMemorySize, L_TOTAL);
  fused_kernel<<<256, NT, L_TOTAL, stream>>>((const u16*)d_in[0], (const u16*)d_in[1], (const u16*)d_in[2], (const u16*)d_in[3], (const u16*)d_in[4], (const u16*)d_in[5],
                                             (const u16*)d_in[7], (const u16*)d_in[8], (const u16*)d_in[9], (const u16*)d_in[10], (const u16*)d_in[11], (const u16*)d_in[12],
                                             (u16*)d_out, (unsigned char*)d_ws);
}
```

```cpp
#include <hip/hip_runtime.h>
#include <stdint.h>

typedef unsigned short u16;
typedef unsigned v4u __attribute__((ext_vector_type(4)));
typedef unsigned v2u __attribute__((ext_vector_type(2)));
typedef float f32x4 __attribute__((ext_vector_type(4)));
typedef float f32x2 __attribute__((ext_vector_type(2)));
typedef short bf16x8 __attribute__((ext_vector_type(8)));
typedef short s16x4 __attribute__((ext_vector_type(4)));
typedef __bf16 bf2 __attribute__((ext_vector_type(2)));
#define LAS __attribute__((address_space(3)))

constexpr unsigned SENT = 0xFFFFFFFFu;
constexpr int NT = 512;
constexpr int SPIN_CAP = 1 << 20;
#define NREP 2
#define EGL_DELAY 400
#define ROFF 24
#define CLRAUX 16
#define NTAUX 2
#define NTS " nt"
#define LDNT(T, p) __builtin_nontemporal_load((const T*)(p))
constexpr int SU_OFF = 0;
constexpr int SF_OFF = 131072;
constexpr int SQ_OFF = 262144;
constexpr int CLR_OFF = 327680;
constexpr int S2_OFF = CLR_OFF;
constexpr int S5_OFF = S2_OFF + 8 * 32768;
constexpr int S6_OFF = S5_OFF + 65536;
constexpr int SL_OFF = S6_OFF + 49152;
constexpr int SLF_OFF = SL_OFF + 32768;
constexpr int DN_OFF = SLF_OFF + 32768;
constexpr int EGC_END = DN_OFF + 4096;
constexpr int EGC_PIECES = (EGC_END - S2_OFF) / 16 / 64;
static_assert(EGC_PIECES <= 448 && EGC_PIECES * 1024 == EGC_END - S2_OFF, "clear share");
constexpr int S4_OFF = EGC_END;
constexpr int S3R = 1152;
constexpr int S3_OFF = S4_OFF + 16384;
constexpr int CLR_END = S3_OFF + 1024 * S3R;
constexpr int DUMMY_OFF = CLR_END;
constexpr int WS_USED = CLR_END + 65536;
constexpr int L_SM = 131072;
constexpr int L_TOTAL = 131072 + 30720;

__device__ __forceinline__ float bflo(unsigned w) { return __uint_as_float(w << 16); }
__device__ __forceinline__ float bfhi(unsigned w) { return __uint_as_float(w & 0xffff0000u); }
__device__ __forceinline__ unsigned pk2(float a, float b) { bf2 p; p.x = (__bf16)a; p.y = (__bf16)b; return __builtin_bit_cast(unsigned, p); }
__device__ __forceinline__ float rbf(float a) { return (float)(__bf16)a; }
__device__ __forceinline__ unsigned canon(unsigned w) { return w == SENT ? 0x7FC07FC0u : w; }
__device__ __forceinline__ float dot2(unsigned a, unsigned b, float c) { return __builtin_amdgcn_fdot2_f32_bf16(__builtin_bit_cast(bf2, a), __builtin_bit_cast(bf2, b), c, false); }
__device__ __forceinline__ void bar() { __builtin_amdgcn_sched_barrier(0); asm volatile("s_waitcnt lgkmcnt(0)\n\ts_barrier" ::: "memory"); __builtin_amdgcn_sched_barrier(0); }
template <int CTRL> __device__ __forceinline__ float dppf(float v) { return __int_as_float(__builtin_amdgcn_update_dpp(0, __float_as_int(v), CTRL, 0xf, 0xf, false)); }
template <int CTRL> __device__ __forceinline__ unsigned dppu(unsigned v) { return (unsigned)__builtin_amdgcn_update_dpp(0, (int)v, CTRL, 0xf, 0xf, false); }
__device__ __forceinline__ float red32(float a, float b) {
  auto r = __builtin_amdgcn_permlane32_swap(__float_as_uint(a), __float_as_uint(b), false, false);
  return __uint_as_float(r[0]) + __uint_as_float(r[1]);
}
__device__ __forceinline__ float red16(float a, float b) {
  auto r = __builtin_amdgcn_permlane16_swap(__float_as_uint(a), __float_as_uint(b), false, false);
  return __uint_as_float(r[0]) + __uint_as_float(r[1]);
}
__device__ __forceinline__ float max32(float a) { auto r = __builtin_amdgcn_permlane32_swap(__float_as_uint(a), __float_as_uint(a), false, false); return fmaxf(__uint_as_float(r[0]), __uint_as_float(r[1])); }
__device__ __forceinline__ float max16(float a) { auto r = __builtin_amdgcn_permlane16_swap(__float_as_uint(a), __float_as_uint(a), false, false); return fmaxf(__uint_as_float(r[0]), __uint_as_float(r[1])); }
__device__ __forceinline__ float wave_sum(float v) {
  v += dppf<0x128>(v); v += dppf<0x124>(v); v += dppf<0x122>(v); v += dppf<0x121>(v);
  v = red16(v, v); v = red32(v, v); return v;
}
__device__ __forceinline__ float wave_max(float v) {
  v = fmaxf(v, dppf<0x128>(v)); v = fmaxf(v, dppf<0x124>(v)); v = fmaxf(v, dppf<0x122>(v)); v = fmaxf(v, dppf<0x121>(v));
  v = max16(v); v = max32(v); return v;
}
__device__ __forceinline__ unsigned ilo(unsigned a, unsigned b) { return __builtin_amdgcn_perm(b, a, 0x05040100u); }
__device__ __forceinline__ unsigned ihi(unsigned a, unsigned b) { return __builtin_amdgcn_perm(b, a, 0x07060302u); }
__device__ __forceinline__ void interleave(v4u& a, v4u& b) {
  v4u x, y;
  x.x = ilo(a.x, b.x); x.y = ihi(a.x, b.x); x.z = ilo(a.y, b.y); x.w = ihi(a.y, b.y);
  y.x = ilo(a.z, b.z); y.y = ihi(a.z, b.z); y.z = ilo(a.w, b.w); y.w = ihi(a.w, b.w);
  a = x; b = y;
}
typedef __amdgpu_buffer_rsrc_t rsrc_t;
__device__ __forceinline__ v4u ld16(rsrc_t r, int off) { return __builtin_amdgcn_raw_buffer_load_b128(r, off, 0, 16); }
__device__ __forceinline__ v2u ld8(rsrc_t r, int off) { return __builtin_amdgcn_raw_buffer_load_b64(r, off, 0, 16); }
__device__ __forceinline__ void st16(rsrc_t r, int off, v4u v) { __builtin_amdgcn_raw_buffer_store_b128(v, r, off, 0, 16); }
__device__ __forceinline__ void st8(rsrc_t r, int off, v2u v) { __builtin_amdgcn_raw_buffer_store_b64(v, r, off, 0, 16); }
__device__ __forceinline__ void glds16(const void* g, unsigned lds_base) {
  unsigned sv; asm volatile("s_mov_b32 %0, m0\n\ts_mov_b32 m0, %2\n\ts_nop 0\n\tglobal_load_lds_dwordx4 %1, off" NTS "\n\ts_mov_b32 m0, %0" : "=&s"(sv) : "v"(g), "s"(lds_base) : "memory");
}
#define RL(v, l) __int_as_float(__builtin_amdgcn_readlane(__float_as_int(v), (l)))
#define SB() __builtin_amdgcn_sched_barrier(0)
#define CBAR() do { asm volatile("" ::: "memory"); __builtin_amdgcn_sched_barrier(0); } while (0)

__device__ __forceinline__ int swz(int row) { return ((row & 3) | ((row >> 1) & 4)) << 1; }
template <int AUX> __device__ __forceinline__ void dma16a(const void* g, LAS unsigned char* l) { __builtin_amdgcn_global_load_lds((const __attribute__((address_space(1))) unsigned*)g, (LAS unsigned*)l, 16, 0, AUX); }
#define dma16 dma16a<NTAUX>
#define NARROW 2
#define dma16n dma16a<NARROW>
#define LDNAR(T, p) __builtin_nontemporal_load((const T*)(p))
template <int RB, bool SW>
__device__ __forceinline__ bf16x8 trfrag(const LAS unsigned char* img, int k0, int ch0, int lane) {
  const int g = lane >> 4, qq = (lane & 15) >> 2, pp = lane & 3;
  s16x4 h[2];
#pragma unroll
  for (int tt = 0; tt < 2; tt++) {
    const int row = k0 + 8 * g + 4 * tt + qq;
    const int ch = SW ? ((ch0 + (pp >> 1)) ^ swz(row)) : (ch0 + (pp >> 1));
    h[tt] = __builtin_amdgcn_ds_read_tr16_b64_v4i16((LAS s16x4*)(img + row * RB + ch * 16 + (pp & 1) * 8));
  }
  return (bf16x8){h[0].x, h[0].y, h[0].z, h[0].w, h[1].x, h[1].y, h[1].z, h[1].w};
}
#define MFMA(a, b, c) __builtin_amdgcn_mfma_f32_16x16x32_bf16(a, b, c, 0, 0, 0)
#define VMWAIT0() do { asm volatile("" ::: "memory"); __builtin_amdgcn_s_waitcnt(0x0F70); asm volatile("s_waitcnt vmcnt(0)" ::: "memory"); } while (0)
#define LGWAIT0() asm volatile("s_waitcnt lgkmcnt(0)" ::: "memory")
typedef unsigned long long u64;
__device__ __forceinline__ unsigned mx4(v4u v) { return max(max(v.x, v.y), max(v.z, v.w)); }
__device__ __forceinline__ void stclr(rsrc_t r, int off, v4u v) { __builtin_amdgcn_raw_buffer_store_b128(v, r, off, 0, CLRAUX); }

__global__ void __launch_bounds__(NT, 2) fused_kernel(
    const u16* __restrict__ x, const u16* __restrict__ ckv, const u16* __restrict__ w_q, const u16* __restrict__ w_uk,
    const u16* __restrict__ w_uv, const u16* __restrict__ w_o, const u16* __restrict__ w1, const u16* __restrict__ b1,
    const u16* __restrict__ w2, const u16* __restrict__ b2, const u16* __restrict__ cvec, const u16* __restrict__ w_mod,
    u16* __restrict__ out, unsigned char* ws) {
  extern __shared__ __attribute__((aligned(16))) unsigned char lds_raw[];
  LAS unsigned char* L = (LAS unsigned char*)lds_raw;
  const int t = threadIdx.x, lane = t & 63, wave = __builtin_amdgcn_readfirstlane(t >> 6);
  const int b = blockIdx.x, xc = b & 7, r = ((b >> 3) + ROFF) & 31;
  const int n = lane & 15, q = lane >> 4;
  const rsrc_t rs = __builtin_amdgcn_make_buffer_rsrc((void*)ws, 0, WS_USED, 0x00020000);
  LAS unsigned char* SM = L + L_SM;
  const v4u sv4 = {SENT, SENT, SENT, SENT};
  const u64 tst = __builtin_amdgcn_s_memrealtime();
  (void)tst;
#define DELAY(x) do { while (__builtin_amdgcn_s_memrealtime() - tst < (x)) __builtin_amdgcn_s_sleep(2); } while (0)

  if (r < 16) {
    const int head = 4 * xc + (r >> 2), cs = r & 3;
    LAS unsigned char* hp = SM;
    LAS unsigned* q16 = (LAS unsigned*)(SM + 2304);
    LAS unsigned char* qst = SM + 8192;
    LAS unsigned char* WKI = L + 98304;
    LAS unsigned char* WQI = L + 65536;
    const int sfoff = SF_OFF + xc * 8192 + lane * 128;
    LAS unsigned* lcnt = (LAS unsigned*)(SM + 9472);
    const unsigned short cvl0 = cvec[(r & 7) * 64 + (t >> 4)], cvl1 = cvec[(r & 7) * 64 + 32 + (t >> 4)];
    bf16x8 wk[4];
    if (wave < 4) {
      const int suoff = SU_OFF + xc * 16384 + cs * 4096 + t * 16;
      const int sqoff = SQ_OFF + head * 2048 + (t & 127) * 16;
      const v4u snapu = ld16(rs, suoff);
      const v4u snapq = ld16(rs, sqoff);
      const unsigned snapf = ld8(rs, sfoff).y;
      if (t == 0) *lcnt = 0u;
      bar();
      v4u v = snapu; int spins = 0; bool ok = false;
      for (;;) {
        CBAR();
        if (!ok) v = ld16(rs, suoff);
        ok = (v.y != snapu.y) & (v.w != snapu.w);
        if (__all(ok) || ++spins > SPIN_CAP) break;
      }
      v2u hh = {v.x, v.z};
      *(LAS v2u*)(hp + ((t >> 4) & 7) * 272 + (t >> 7) * 128 + (t & 15) * 8) = hh;
      bar();
#pragma unroll
      for (int i = 0; i < 4; i++) wk[i] = *(const LAS bf16x8*)(WKI + (wave * 16 + n) * 256 + (((4 * i + q) ^ swz(n)) * 16));
      if (wave < 2) {
        spins = 0; ok = false;
        for (;;) {
          CBAR();
          if (!ok) v = ld16(rs, sqoff);
          ok = (v.y != snapq.y) & (v.w != snapq.w);
          if (__all(ok) || ++spins > SPIN_CAP) break;
        }
        v2u o2 = {v.x, v.z};
        *(LAS v2u*)(qst + (t >> 5) * 272 + (t & 31) * 8) = o2;
      } else if (wave == 2) {
        spins = 0; ok = false;
        for (;;) { CBAR(); if (!ok) ok = ld8(rs, sfoff).y != snapf; if (__all(ok) || ++spins > SPIN_CAP) break; }
      }
      bar();
    } else {
      const int lw = wave - 4;
      const int sqo = SQ_OFF + head * 2048 + cs * 512 + lw * 128 + ((lane >> 2) & 7) * 16;
      const v4u oldq = ld16(rs, sqo);
      bar();
#pragma unroll
      for (int i = 0; i < 16; i++) {
        const int j = 4 * i + lw, rg = j & 31, row = 4 * rg + q;
        const u16* src = (i < 8 ? w_q : w_uk) + (size_t)(cs * 128 + row) * 4096 + head * 128 + ((n ^ swz(row)) * 8);
        dma16(src, L + 65536 + j * 1024);
      }
      VMWAIT0();
      if (lane == 0) __hip_atomic_fetch_add(lcnt, 1u, __ATOMIC_RELAXED, __HIP_MEMORY_SCOPE_WORKGROUP);
      { int spins = 0; while (__hip_atomic_load(lcnt, __ATOMIC_RELAXED, __HIP_MEMORY_SCOPE_WORKGROUP) < 4u && ++spins < SPIN_CAP) {} }
      CBAR();
      bf16x8 wqf[8];
#pragma unroll
      for (int i = 0; i < 8; i++) wqf[i] = trfrag<256, true>(WQI, 32 * (i & 3), 4 * lw + 2 * (i >> 2), lane);
#pragma unroll
      for (int i = 0; i < 4; i++) wk[i] = *(const LAS bf16x8*)(WKI + (wave * 16 + n) * 256 + (((4 * i + q) ^ swz(n)) * 16));
      bar();
      f32x4 a0 = {0, 0, 0, 0}, a1 = {0, 0, 0, 0}; bf16x8 hf[4];
#pragma unroll
      for (int i = 0; i < 4; i++) hf[i] = *(const LAS bf16x8*)(hp + (n & 7) * 272 + i * 64 + q * 16);
      SB();
#pragma unroll
      for (int i = 0; i < 4; i++) { a0 = MFMA(hf[i], wqf[i], a0); a1 = MFMA(hf[i], wqf[4 + i], a1); }
      const float sq = red16((a0[0] + a0[1]) + (a0[2] + a0[3]), (a1[0] + a1[1]) + (a1[2] + a1[3]));
      const unsigned d = pk2(sq, dppf<0x101>(sq));
      const unsigned d2 = dppu<0x102>(d);
      if (lane < 32 && (lane & 3) == 0) { v4u o = {d, oldq.y + 1u, d2, oldq.w + 1u}; st16(rs, sqo, o); }
      {
        const int cb = S3_OFF + (head * 32 + cs * 8) * S3R + (t - 256) * 16;
        stclr(rs, cb, sv4); stclr(rs, cb + 4096, sv4);
        if (wave == 4) stclr(rs, cb + 8192, sv4);
        else if (wave == 5 && lane < 8) stclr(rs, S4_OFF + head * 512 + cs * 128 + lane * 16, sv4);
      }
      VMWAIT0();
      bar();
      if (lane < 32 && (lane & 3) == 0) { v4u o = {d, oldq.y + 2u, d2, oldq.w + 2u}; st16(rs, sqo, o); }
    }
    {
      f32x4 a = {0, 0, 0, 0}; bf16x8 qf[4];
#pragma unroll
      for (int i = 0; i < 4; i++) qf[i] = *(const LAS bf16x8*)(qst + (n & 3) * 272 + i * 64 + q * 16);
      SB();
      f32x4 ab = {0, 0, 0, 0};
      a = MFMA(wk[0], qf[0], a); ab = MFMA(wk[1], qf[1], ab); a = MFMA(wk[2], qf[2], a); ab = MFMA(wk[3], qf[3], ab);
      a += ab;
#pragma unroll
      for (int i = 0; i < 4; i++) { a[i] += dppf<0xB1>(a[i]); a[i] += dppf<0x4E>(a[i]); }
      const float SC = 0.08838834764831845f * 1.4426950408889634f;
      v2u o = {canon(pk2(a[0] * SC, a[1] * SC)), canon(pk2(a[2] * SC, a[3] * SC))};
      if (n == 0) *(LAS v2u*)(q16 + 64 + wave * 8 + q * 2) = o;
    }
    bar();
    if (wave < 2) { const v4u o = *(const LAS v4u*)(q16 + 64 + n * 4); st16(rs, S2_OFF + (wave * 4 + q) * 32768 + head * 1024 + cs * 256 + n * 16, o); }
    bar();
    {
      const int kj = r & 7, cg = xc * 2 + (r >> 3);
      const u16* pl = w_mod + (size_t)(kj * 64 + (t >> 4)) * 3072 + 1024 + cg * 128 + n * 8;
      v4u w0 = LDNT(v4u, pl), w1r = LDNT(v4u, pl + (size_t)32 * 3072);
      CBAR();
      if (r < 8) {
#pragma unroll
        for (int i = 0; i < 8; i++) {
          const int rg = 8 * i + wave, row = 8 * rg + (lane >> 3);
          glds16(w_o + (size_t)(xc * 512 + row) * 512 + r * 64 + (((lane & 7) ^ ((row & 2) | ((row >> 1) & 4))) * 8), (unsigned)(rg * 1024));
        }
        asm volatile("s_waitcnt vmcnt(8)" ::: "memory");
      }
      const float c0 = bflo(cvl0), c1 = bflo(cvl1);
      const unsigned s01 = pk2(c0 * __builtin_amdgcn_rcpf(1.f + __expf(-c0)), c1 * __builtin_amdgcn_rcpf(1.f + __expf(-c1)));
      interleave(w0, w1r);
      float a[8] = {dot2(w0.x, s01, 0.f), dot2(w0.y, s01, 0.f), dot2(w0.z, s01, 0.f), dot2(w0.w, s01, 0.f),
                    dot2(w1r.x, s01, 0.f), dot2(w1r.y, s01, 0.f), dot2(w1r.z, s01, 0.f), dot2(w1r.w, s01, 0.f)};
      float b0 = red32(a[0], a[4]), b1x = red32(a[1], a[5]), b2x = red32(a[2], a[6]), b3 = red32(a[3], a[7]);
      f32x2 o = {red16(b0, b2x), red16(b1x, b3)};
      LAS float* apart = (LAS float*)(SM + 12288);
      *(LAS f32x2*)(apart + wave * 128 + n * 8 + 4 * (lane >> 5) + 2 * ((lane >> 4) & 1)) = o;
      bar();
      if (wave == 7) {
        f32x2 sacc = {0.f, 0.f};
#pragma unroll
        for (int w = 0; w < 8; w++) sacc += *(LAS f32x2*)(apart + w * 128 + lane * 2);
        const unsigned d = canon(pk2(sacc.x, sacc.y));
        v4u o4 = {dppu<0x00>(d), dppu<0x55>(d), dppu<0xAA>(d), dppu<0xFF>(d)};
        if ((lane & 3) == 0) st16(rs, SL_OFF + kj * 4096 + cg * 256 + lane * 4, o4);
        if (cg < 4 || cg >= 12) {
          const unsigned f0 = canon(__float_as_uint(sacc.x)), f1 = canon(__float_as_uint(sacc.y));
          v4u of = {f0, f1, dppu<0x101>(f0), dppu<0x101>(f1)};
          if (!(lane & 1)) st16(rs, SLF_OFF + kj * 4096 + (cg < 4 ? cg : cg - 8) * 512 + lane * 8, of);
        }
      }
      bar();
    }
    if (r < 8) {
      LAS float* fpart = (LAS float*)(SM + 3072);
      LAS unsigned char* cst = SM + 4096;
      LAS float* fp64 = (LAS float*)(SM + 16384);
      VMWAIT0();
      bar();
      bf16x8 wof[8];
      {
        const int g = lane >> 4, qq = (lane & 15) >> 2, pp = lane & 3;
#pragma unroll
        for (int i = 0; i < 8; i++) {
          s16x4 h[2];
#pragma unroll
          for (int tt = 0; tt < 2; tt++) {
            const int row = 32 * (2 * wave + (i >> 2)) + 8 * g + 4 * tt + qq;
            const int ch = (2 * (i & 3) + (pp >> 1)) ^ ((row & 2) | ((row >> 1) & 4));
            h[tt] = __builtin_amdgcn_ds_read_tr16_b64_v4i16((LAS s16x4*)(L + row * 128 + ch * 16 + (pp & 1) * 8));
          }
          wof[i] = (bf16x8){h[0].x, h[0].y, h[0].z, h[0].w, h[1].x, h[1].y, h[1].z, h[1].w};
        }
      }
      if (wave >= 4) {
        int spins = 0;
        for (;;) { CBAR(); v4u v = ld16(rs, S2_OFF + xc * 32768 + xc * 4096 + (t - 256) * 16); if (__all(mx4(v) != SENT) || ++spins > SPIN_CAP) break; __builtin_amdgcn_s_sleep(2); }
      }
      bar();
      {
        v4u av0, av1; int spins = 0;
        const int off = S4_OFF + ((4 * xc + (wave >> 1)) * 2 + (n & 1)) * 256 + (wave & 1) * 128 + q * 16;
        for (;;) { CBAR(); av0 = ld16(rs, off); av1 = ld16(rs, off + 64); if (__all(max(mx4(av0), mx4(av1)) != SENT) || ++spins > SPIN_CAP) break; }
        f32x4 a[4];
#pragma unroll
        for (int T = 0; T < 4; T++) { a[T] = MFMA(__builtin_bit_cast(bf16x8, av0), wof[T], ((f32x4){0, 0, 0, 0})); a[T] = MFMA(__builtin_bit_cast(bf16x8, av1), wof[4 + T], a[T]); }
        const f32x4 sel = q == 0 ? a[0] : q == 1 ? a[1] : q == 2 ? a[2] : a[3];
        fp64[wave * 64 + lane] = sel[0] + sel[1];
        bar();
        if (wave == 0) {
          float v = 0.f;
#pragma unroll
          for (int w = 0; w < 8; w++) v += fp64[w * 64 + lane];
          const unsigned d = canon(pk2(v, dppf<0x101>(v)));
          if (!(lane & 1)) ((LAS unsigned*)cst)[lane >> 1] = d;
          LGWAIT0();
          const v4u o = *(const LAS v4u*)(cst + (lane & 7) * 16);
          st16(rs, S5_OFF + (lane >> 3) * 8192 + xc * 1024 + r * 128 + (lane & 7) * 16, o);
        }
      }
    } else {
      const int ti = xc * 8 + (r - 8);
      LAS unsigned char* W2I = L;
      const float b2v = bflo(b2[ti * 8 + (lane & 7)]);
      const float x8 = bflo(x[ti * 8 + (lane & 7)]);
      CBAR();
#pragma unroll
      for (int i = 0; i < 3; i++) { const int rg = 8 * i + wave; if (rg < 22) { int row = rg * 64 + lane; row = row < 1376 ? row : 1375; dma16n(w2 + (size_t)row * 512 + ti * 8, W2I + rg * 1024); } }
      CBAR();
      LAS float* hpart = (LAS float*)(SM + 2048);
      LAS unsigned* sbuf = (LAS unsigned*)(SM + 2560);
      VMWAIT0();
      bar();
      bf16x8 w2f[6];
#pragma unroll
      for (int i = 0; i < 6; i++) {
        const int ks = wave + 8 * i < 43 ? wave + 8 * i : 0;
        const int g = lane >> 4, qq = (lane & 15) >> 2, pp = lane & 3;
        s16x4 h0 = __builtin_amdgcn_ds_read_tr16_b64_v4i16((LAS s16x4*)(W2I + (32 * ks + 8 * g + qq) * 16 + (pp & 1) * 8));
        s16x4 h1 = __builtin_amdgcn_ds_read_tr16_b64_v4i16((LAS s16x4*)(W2I + (32 * ks + 8 * g + 4 + qq) * 16 + (pp & 1) * 8));
        w2f[i] = (bf16x8){h0.x, h0.y, h0.z, h0.w, h1.x, h1.y, h1.z, h1.w};
      }
      float xn8 = 0.f, gbv = 0.f;
      if (wave == 0) {
        const int off = lane < 16 ? S5_OFF + xc * 8192 + (lane & 7) * 1024 + ti * 16 : SLF_OFF + ((lane >> 1) & 7) * 4096 + ((lane & 32) ? 2048 : 0) + ti * 32 + (lane & 1) * 16;
        v4u v; int spins = 0;
        for (;;) { CBAR(); v = ld16(rs, off); if (__all(mx4(v) != SENT) || ++spins > SPIN_CAP) break; __builtin_amdgcn_s_sleep(4); }
        if (lane < 48) *(LAS v4u*)(sbuf + lane * 4) = v;
        LGWAIT0();
        const LAS u16* sh = (const LAS u16*)sbuf; const LAS float* sf = (const LAS float*)sbuf;
        float ao = 0, ga8 = 0;
#pragma unroll
        for (int k = 0; k < 8; k++) { ao += bflo(sh[k * 8 + (lane & 7)]); ga8 += sf[64 + k * 8 + (lane & 7)]; gbv += sf[128 + k * 8 + (lane & 7)]; }
        xn8 = x8 + ga8 * ao;
      }
      bar();
      {
        v4u hv[6]; int spins = 0;
        const int off = S6_OFF + xc * 6144 + wave * 128 + q * 16, off5 = wave < 3 ? off + 5120 : off;
        for (;;) {
          CBAR();
#pragma unroll
          for (int i = 0; i < 5; i++) hv[i] = ld16(rs, off + i * 1024);
          hv[5] = ld16(rs, off5);
          const unsigned m = max(max(max(mx4(hv[0]), mx4(hv[1])), max(mx4(hv[2]), mx4(hv[3]))), max(mx4(hv[4]), mx4(hv[5])));
          if (__all(m != SENT) || ++spins > SPIN_CAP) break;
        }
        if (wave >= 3) hv[5] = (v4u){0, 0, 0, 0};
        f32x4 a = {0, 0, 0, 0}, a2 = {0, 0, 0, 0};
#pragma unroll
        for (int i = 0; i < 3; i++) { a = MFMA(__builtin_bit_cast(bf16x8, hv[2 * i]), w2f[2 * i], a); a2 = MFMA(__builtin_bit_cast(bf16x8, hv[2 * i + 1]), w2f[2 * i + 1], a2); }
        if (lane < 8) hpart[wave * 8 + lane] = a[0] + a2[0];
      }
      bar();
      if (wave == 0) {
        float a = hpart[lane];
        a += dppf<0x128>(a); a = red16(a, a); a = red32(a, a);
        const float res = xn8 + gbv * (a + b2v);
        if (lane < 8) out[ti * 8 + lane] = __builtin_bit_cast(u16, (__bf16)res);
      }
    }
  } else if (r >= 24) {
    const int e = r - 24, head = 4 * xc + (e >> 1), half = e & 1, ti = xc * 8 + e;
    const int gi = e * 8 + xc;
    const int wst = (gi < 43 ? gi : 0) * 4;
    const int mcol = wave * 64 + 8 * (lane & 7) + 4 * (lane >> 5) + 2 * ((lane >> 4) & 1) + ((lane >> 3) & 1);
    LAS unsigned char* W1I = L + 65536;
    LAS unsigned char* STG = L + 98304;
    LAS u16* y16 = (LAS u16*)(SM + 5376);
    LAS unsigned* pub = (LAS unsigned*)(SM + 7424);
    LAS unsigned* cnt = (LAS unsigned*)(SM + 7680);
    LAS unsigned* tbase = (LAS unsigned*)(SM + 8192);
    LAS float* apart = (LAS float*)(SM + 10240);
    const int puoff = SU_OFF + q * 16384 + (xc * 8 + e) * 256 + n * 16;
    const int pfoff = SF_OFF + n * 8192 + ti * 128;
    const int sfoff = SF_OFF + (8 + xc) * 8192 + lane * 128;
#define LDSADD(p) __hip_atomic_fetch_add((p), 1u, __ATOMIC_RELAXED, __HIP_MEMORY_SCOPE_WORKGROUP)
#define REPUB() do { v2u p2_ = *(LAS v2u*)(pub + n * 2); v4u tb_ = *(LAS v4u*)(tbase + lane * 8); unsigned tf_ = tbase[lane * 8 + 4]; unsigned k_ = 0; if (lane == 0) k_ = LDSADD(cnt); k_ = __builtin_amdgcn_readfirstlane(k_); \
      v4u o_ = {p2_.x, tb_.x + k_, p2_.y, tb_.y + k_}; st16(rs, puoff, o_); v4u o2_ = {p2_.x, tb_.z + k_, p2_.y, tb_.w + k_}; st16(rs, puoff + 65536, o2_); \
      if (lane < 16) { v2u f_ = {0u, tf_ + k_}; st8(rs, pfoff, f_); } } while (0)
    v2u pd = {0, 0};
    v4u old0 = {0, 0, 0, 0}, old1 = {0, 0, 0, 0}, xv = {0, 0, 0, 0}; unsigned snapf = 0, xw = 0;
    v4u wm[2]; unsigned short cv[2];
    {
      const u16* p = w_mod + (size_t)(e * 64 + (t >> 4)) * 3072 + (n >> 3) * 512 + xc * 64 + (n & 7) * 8;
      wm[0] = LDNT(v4u, p); wm[1] = LDNT(v4u, p + (size_t)32 * 3072);
      cv[0] = cvec[e * 64 + (t >> 4)]; cv[1] = cvec[e * 64 + 32 + (t >> 4)];
    }
    CBAR();
    const unsigned oldf = ld8(rs, pfoff).y;
    if (wave == 0) { old0 = ld16(rs, puoff); old1 = ld16(rs, puoff + 65536); xv = *(const v4u*)(x + lane * 8); xw = ((const unsigned*)x)[xc * 32 + (lane & 31)]; }
    if (wave == 1) snapf = ld8(rs, sfoff).y;
    CBAR();
    {
      const int pc = t >= 64 ? t - 64 : t;
      st16(rs, wave == 0 ? DUMMY_OFF + ti * 1024 + lane * 16 : S2_OFF + ti * (EGC_PIECES * 16) + (pc < EGC_PIECES ? pc : pc - EGC_PIECES) * 16, sv4);
    }
    CBAR();
    float ln0 = 0.f, ln1 = 0.f;
    if (wave == 0) {
      float xs[8] = {bflo(xv.x), bfhi(xv.x), bflo(xv.y), bfhi(xv.y), bflo(xv.z), bfhi(xv.z), bflo(xv.w), bfhi(xv.w)};
      float s = 0;
#pragma unroll
      for (int i = 0; i < 8; i++) s += xs[i];
      const float mean = wave_sum(s) * (1.f / 512.f);
      float vs = 0;
#pragma unroll
      for (int i = 0; i < 8; i++) { float d = xs[i] - mean; vs += d * d; }
      const float rstd = rsqrtf(wave_sum(vs) * (1.f / 512.f) + 1e-5f);
      ln0 = (bflo(xw) - mean) * rstd; ln1 = (bfhi(xw) - mean) * rstd;
      v4u tb = {old0.y, old0.w, old1.y, old1.w};
      *(LAS v4u*)(tbase + lane * 8) = tb; tbase[lane * 8 + 4] = oldf;
      if (lane == 0) { cnt[0] = 2u + NREP; cnt[1] = 0u; }
    }
    CBAR();
    {
      const float c0 = bflo(cv[0]), c1 = bflo(cv[1]);
      const unsigned s01 = pk2(c0 * __builtin_amdgcn_rcpf(1.f + __expf(-c0)), c1 * __builtin_amdgcn_rcpf(1.f + __expf(-c1)));
      interleave(wm[0], wm[1]);
      float a[8] = {dot2(wm[0].x, s01, 0.f), dot2(wm[0].y, s01, 0.f), dot2(wm[0].z, s01, 0.f), dot2(wm[0].w, s01, 0.f),
                    dot2(wm[1].x, s01, 0.f), dot2(wm[1].y, s01, 0.f), dot2(wm[1].z, s01, 0.f), dot2(wm[1].w, s01, 0.f)};
      float b0 = red32(a[0], a[4]), b1x = red32(a[1], a[5]), b2x = red32(a[2], a[6]), b3 = red32(a[3], a[7]);
      f32x2 o = {red16(b0, b2x), red16(b1x, b3)};
      *(LAS f32x2*)(apart + wave * 128 + n * 8 + 4 * (lane >> 5) + 2 * ((lane >> 4) & 1)) = o;
    }
    bar();
    if (wave == 0) {
      f32x2 sacc = {0.f, 0.f};
#pragma unroll
      for (int w = 0; w < 8; w++) sacc += *(LAS f32x2*)(apart + w * 128 + lane * 2);
      auto r0 = __builtin_amdgcn_permlane32_swap(__float_as_uint(sacc.x), __float_as_uint(sacc.x), false, false);
      auto r1 = __builtin_amdgcn_permlane32_swap(__float_as_uint(sacc.y), __float_as_uint(sacc.y), false, false);
      const float one = e == 0 ? 1.f : 0.f;
      const float u0 = ln0 * (one + __uint_as_float(r0[1])) + __uint_as_float(r0[0]), u1 = ln1 * (one + __uint_as_float(r1[1])) + __uint_as_float(r1[0]);
      if (lane < 32) pub[lane] = pk2(u0, u1);
      LGWAIT0();
      pd = *(LAS v2u*)(pub + n * 2);
      v4u o = {pd.x, old0.y + 1u, pd.y, old0.w + 1u}; st16(rs, puoff, o);
      v4u o2 = {pd.x, old1.y + 1u, pd.y, old1.w + 1u}; st16(rs, puoff + 65536, o2);
    }
    unsigned karr = 0;
    if (wave != 0) { VMWAIT0(); if (lane == 0) karr = LDSADD(cnt + 1); }
    const bool lastw = __builtin_amdgcn_readfirstlane(karr) == 6u;
    if (lastw && lane < 16) { v2u f = {0u, oldf + 1u}; st8(rs, pfoff, f); }
#pragma unroll
    for (int i = 0; i < 4; i++) if (gi < 43) { const int rg = 8 * i + wave; dma16n(w1 + (size_t)(rg * 16 + (lane >> 2)) * 1376 + wst * 8 + (lane & 3) * 8, W1I + rg * 1024); }
    CBAR();
    DELAY(EGL_DELAY);
    const float xme = bflo(x[mcol]);
    const float b1v = bflo(b1[wst * 8 + (lane & 31)]);
    CBAR();
#pragma unroll
    for (int i = 0; i < 8; i++) {
      const int rg = 8 * i + wave, row = 4 * rg + q;
      dma16(w_uv + (size_t)(half * 256 + row) * 4096 + head * 128 + ((n ^ swz(row)) * 8), L + rg * 1024);
    }
    CBAR();
    if (wave == 0 || lastw) {
#pragma unroll 1
      for (int k = 0; k < NREP; k++) {
        __builtin_amdgcn_s_sleep(14);
        if (wave == 0) { v4u o_ = {pd.x, old0.y + 2u + k, pd.y, old0.w + 2u + k}; st16(rs, puoff, o_); v4u o2_ = {pd.x, old1.y + 2u + k, pd.y, old1.w + 2u + k}; st16(rs, puoff + 65536, o2_); }
        if (lastw && lane < 16) { v2u f_ = {0u, oldf + 2u + k}; st8(rs, pfoff, f_); }
      }
    }
    VMWAIT0();
    bar();
    bf16x8 wvf[16], w1f[4];
#pragma unroll
    for (int s2 = 0; s2 < 16; s2++) wvf[s2] = trfrag<256, true>(L, 64 * (wave & 3) + 32 * (s2 >> 3), 2 * (s2 & 7), lane);
#pragma unroll
    for (int i = 0; i < 4; i++) w1f[i] = trfrag<64, false>(W1I, 64 * wave + 32 * (i >> 1), 2 * (i & 1), lane);
    if (wave == 1) {
      int spins = 0; bool ok = false;
      for (;;) { CBAR(); if (!ok) ok = ld8(rs, sfoff).y != snapf; if (__all(ok) || ++spins > SPIN_CAP) break; if ((spins & 3) == 0) REPUB(); __builtin_amdgcn_s_sleep(2); }
      spins = 0;
      for (;;) { CBAR(); v4u v = ld16(rs, S2_OFF + xc * 32768 + head * 1024 + lane * 16); if (__all(mx4(v) != SENT) || ++spins > SPIN_CAP) break; if ((spins & 3) == 0) REPUB(); __builtin_amdgcn_s_sleep(2); }
    }
    bar();
    {
      LAS float* sclf = (LAS float*)(SM);
      LAS float* epart = (LAS float*)(STG);
      {
        const int soff = S3_OFF + (head * 32 + (lane & 31)) * S3R + 1024;
        if (wave == 7) {
          v2u stt; int spins = 0;
          for (;;) { CBAR(); stt = ld8(rs, soff); if (__all(max(stt.x, stt.y) != SENT) || ++spins > SPIN_CAP) break; if ((spins & 3) == 0) REPUB(); }
          const float m = __uint_as_float(stt.x), l = __uint_as_float(stt.y);
          const float mg = wave_max(m);
          const float sc = __builtin_amdgcn_exp2f(m - mg);
          const float il = 2.f * __builtin_amdgcn_rcpf(wave_sum(sc * l));
          if (lane < 32) sclf[lane] = sc * il;
        }
      }
      v4u p0, p1; int spins = 0;
      const int off = S3_OFF + (head * 32 + (wave >> 2) * 16 + n) * S3R + half * 512 + (wave & 3) * 128 + q * 16;
      for (;;) {
        CBAR();
        p0 = ld16(rs, off); p1 = ld16(rs, off + 64);
        if (__all(max(mx4(p0), mx4(p1)) != SENT) || ++spins > SPIN_CAP) break;
        if ((spins & 15) == 0) REPUB();
      }
      f32x4 y0[8];
#pragma unroll
      for (int T = 0; T < 8; T++) { y0[T] = MFMA(__builtin_bit_cast(bf16x8, p0), wvf[T], ((f32x4){0, 0, 0, 0})); y0[T] = MFMA(__builtin_bit_cast(bf16x8, p1), wvf[8 + T], y0[T]); }
      bar();
      const f32x4 s4 = *(const LAS f32x4*)(sclf + 16 * (wave >> 2) + 4 * q);
      float z[8];
#pragma unroll
      for (int T = 0; T < 8; T++) z[T] = (y0[T][0] * s4[0] + y0[T][1] * s4[1]) + (y0[T][2] * s4[2] + y0[T][3] * s4[3]);
      const float r0 = red32(z[0], z[4]), r1 = red32(z[1], z[5]), r2 = red32(z[2], z[6]), r3 = red32(z[3], z[7]);
      const float u0 = red16(r0, r2), u1 = red16(r1, r3);
      const int T0 = 4 * (lane >> 5) + 2 * ((lane >> 4) & 1);
      epart[wave * 128 + T0 * 16 + n] = u0; epart[wave * 128 + T0 * 16 + 16 + n] = u1;
      bar();
      if (wave == 0) {
        f32x2 sacc = {0.f, 0.f};
#pragma unroll
        for (int w = 0; w < 8; w++) sacc += *(LAS f32x2*)(epart + w * 128 + lane * 2);
        const unsigned d = canon(pk2(sacc.x, sacc.y));
        v4u o4 = {dppu<0x00>(d), dppu<0x55>(d), dppu<0xAA>(d), dppu<0xFF>(d)};
        if ((lane & 3) == 0) st16(rs, S4_OFF + (head * 2 + half) * 256 + lane * 4, o4);
      }
    }
    if (gi >= 43) {
      if (wave == 0) {
        int spins = 0;
        for (;;) {
          CBAR(); v4u v = ld16(rs, DN_OFF + xc * 128);
          if (v.x != SENT || ++spins > SPIN_CAP) break;
          if ((spins & 3) == 0) REPUB();
          __builtin_amdgcn_s_sleep(8);
        }
      } return;
    }
    {
      v4u v0, v1, v2; int spins = 0;
      const int off = SL_OFF + wave * 4096 + lane * 16;
      for (;;) { CBAR(); v0 = ld16(rs, off); v1 = ld16(rs, off + 1024); v2 = ld16(rs, off + 2048); if (__all(max(max(mx4(v0), mx4(v1)), mx4(v2)) != SENT) || ++spins > SPIN_CAP) break; if ((spins & 15) == 0) REPUB(); __builtin_amdgcn_s_sleep(1); }
      *(LAS v4u*)(L + wave * 3072 + lane * 16) = v0; *(LAS v4u*)(L + wave * 3072 + 1024 + lane * 16) = v1; *(LAS v4u*)(L + wave * 3072 + 2048 + lane * 16) = v2;
    }
    bar();
    float gam = 0.f, sbm = 0.f, cb1 = 0.f;
    {
      const LAS u16* mlh = (const LAS u16*)L;
#pragma unroll
      for (int k = 0; k < 8; k++) { gam += bflo(mlh[k * 1536 + mcol]); sbm += bflo(mlh[k * 1536 + 512 + mcol]); cb1 += bflo(mlh[k * 1536 + 1024 + mcol]); }
      sbm = rbf(sbm); cb1 = rbf(1.f + cb1);
    }
    LAS u16* cs16 = (LAS u16*)(SM + 10240);
    LAS float* gp64 = (LAS float*)(SM + 12288);
    cs16[mcol] = __builtin_bit_cast(u16, (__bf16)cb1); cs16[512 + mcol] = __builtin_bit_cast(u16, (__bf16)sbm);
    LGWAIT0();
    bf16x8 auxf[2];
    {
      const bf16x8 ones = {0x3F80, 0x3F80, 0x3F80, 0x3F80, 0x3F80, 0x3F80, 0x3F80, 0x3F80};
#pragma unroll
      for (int i = 0; i < 2; i++) { const bf16x8 v = *(const LAS bf16x8*)(cs16 + ((n - 1) & 1) * 512 + wave * 64 + i * 32 + q * 8); auxf[i] = n == 0 ? ones : v; }
      f32x4 k0 = MFMA(auxf[0], w1f[0], ((f32x4){0, 0, 0, 0})), k1 = MFMA(auxf[0], w1f[1], ((f32x4){0, 0, 0, 0})), kg = MFMA(auxf[0], auxf[0], ((f32x4){0, 0, 0, 0}));
      k0 = MFMA(auxf[1], w1f[2], k0); k1 = MFMA(auxf[1], w1f[3], k1); kg = MFMA(auxf[1], auxf[1], kg);
      if (lane < 16) {
        LAS float* g = (LAS float*)(L + 32768) + wave * 192;
        g[n] = k0[0]; g[16 + n] = k1[0]; g[32 + n] = kg[0]; g[64 + n] = k0[1]; g[80 + n] = k1[1]; g[96 + n] = kg[1]; g[128 + n] = k0[2]; g[144 + n] = k1[2]; g[160 + n] = kg[2];
      }
    }
    bar();
    float Ow = 0.f, Cw = 0.f, Sw = 0.f, Kc = 0.f, Ks = 0.f, Kc2 = 0.f, Kcs = 0.f, Ks2 = 0.f;
    if (wave == 0) {
      const LAS float* g = (const LAS float*)(L + 32768);
      float o = 0.f, c = 0.f, sg = 0.f;
#pragma unroll
      for (int w = 0; w < 8; w++) { o += g[w * 192 + (lane < 48 ? lane : 0)]; c += g[w * 192 + 64 + (lane < 48 ? lane : 0)]; sg += g[w * 192 + 128 + (lane < 48 ? lane : 0)]; }
      Ow = o; Cw = c; Sw = sg;
      Kc = RL(o, 33); Ks = RL(o, 34); Kc2 = RL(c, 33); Kcs = RL(c, 34); Ks2 = RL(sg, 34);
    }
    asm volatile("" : "+v"(gam), "+v"(cb1), "+v"(Ow), "+v"(Cw), "+v"(Sw));
    {
      v4u v; int spins = 0;
      const int off = S5_OFF + xc * 8192 + (lane >> 3) * 1024 + wave * 128 + (lane & 7) * 16;
      for (;;) { CBAR(); v = ld16(rs, off); if (__all(mx4(v) != SENT) || ++spins > SPIN_CAP) break; if ((spins & 15) == 0) REPUB(); }
      const float b0 = red32(bflo(v.x), bflo(v.z)), b1x = red32(bfhi(v.x), bfhi(v.z)), b2x = red32(bflo(v.y), bflo(v.w)), b3 = red32(bfhi(v.y), bfhi(v.w));
      const float c0 = red16(b0, b2x), c1 = red16(b1x, b3);
      const bool up = lane & 8;
      const float keep = up ? c1 : c0, send = up ? c0 : c1;
      const float ao = keep + dppf<0x128>(send);
      const __bf16 xb = (__bf16)(xme + gam * ao);
      cs16[mcol] = __builtin_bit_cast(u16, (__bf16)((float)xb * cb1)); cs16[512 + mcol] = __builtin_bit_cast(u16, xb);
      LGWAIT0();
      const bf16x8 y0 = *(const LAS bf16x8*)(cs16 + (n & 1) * 512 + wave * 64 + q * 8), y1 = *(const LAS bf16x8*)(cs16 + (n & 1) * 512 + wave * 64 + 32 + q * 8);
      f32x4 z0 = MFMA(y0, w1f[0], ((f32x4){0, 0, 0, 0})), z1 = MFMA(y0, w1f[1], ((f32x4){0, 0, 0, 0})), ax = MFMA(y0, auxf[0], ((f32x4){0, 0, 0, 0})), gr = MFMA(y0, y0, ((f32x4){0, 0, 0, 0}));
      z0 = MFMA(y1, w1f[2], z0); z1 = MFMA(y1, w1f[3], z1); ax = MFMA(y1, auxf[1], ax); gr = MFMA(y1, y1, gr);
      const float lastq = n == 0 ? ax[1] : n == 1 ? gr[1] : gr[0];
      gp64[wave * 64 + lane] = q == 0 ? z0[0] : q == 1 ? z1[0] : q == 2 ? ax[0] : lastq;
    }
    bar();
    if (wave == 0) {
      float a = 0;
#pragma unroll
      for (int w = 0; w < 8; w++) a += gp64[w * 64 + lane];
      const float B1 = RL(a, 32), B3 = RL(a, 33), B4 = RL(a, 34), A1 = RL(a, 48), A2 = RL(a, 49), B2 = RL(a, 50);
      const float mu = A1 * (1.f / 512.f), rr = __builtin_amdgcn_rsqf(fmaxf(A2 * (1.f / 512.f) - mu * mu, 0.f) + 1e-5f);
      const float m2 = (rr * (B1 - mu * Kc) + Ks) * (1.f / 512.f);
      const float e2 = (rr * rr * (B2 - 2.f * mu * B3 + mu * mu * Kc2) + 2.f * rr * (B4 - mu * Kcs) + Ks2) * (1.f / 512.f);
      const float r2 = __builtin_amdgcn_rsqf(fmaxf(e2 - m2 * m2, 0.f) + 1e-5f);
      const float zz = r2 * (rr * (a - mu * Cw) + Sw - m2 * Ow);
      const float z = zz + b1v;
      const float u = z * (0.7978845608028654f + 0.035677408136300125f * z * z);
      const float gl = z * __builtin_amdgcn_rcpf(1.f + __builtin_amdgcn_exp2f(-2.885390081777927f * u));
      const unsigned d = canon(pk2(gl, dppf<0x101>(gl)));
      if (lane < 32 && !(lane & 1)) ((LAS unsigned*)y16)[lane >> 1] = d;
      LGWAIT0();
      const v4u o = *(const LAS v4u*)((LAS unsigned*)y16 + (lane & 3) * 4);
      if (lane < 32) st16(rs, S6_OFF + (lane >> 2) * 6144 + gi * 128 + (lane & 3) * 16, o);
    }
    if (ti == 0 && wave == 1 && lane < 8) { v4u o = {1, 1, 1, 1}; st16(rs, DN_OFF + lane * 128, o); }
  } else {
    const int e = r - 16, split = 4 * xc + (e & 3), hg = e >> 2;
    const int sfoff = SF_OFF + (8 + xc) * 8192 + lane * 128;
    LAS unsigned char* qa = SM;
    LAS unsigned char* Pm = SM + 16896;
    LAS float* wmax = (LAS float*)(SM + 21504);
    LAS float* wsum = wmax + 128;
    unsigned snapf = 0;
    if (wave == 0) snapf = ld8(rs, sfoff).y;
    bar();
    CBAR();
#pragma unroll
    for (int i = 0; i < 16; i++) {
      const int key = ((i * 8 + wave) + hg * 64) & 127;
      dma16(ckv + (size_t)(split * 128 + key) * 512 + ((lane ^ swz(key)) * 8), L + key * 1024);
    }
    VMWAIT0();
    bar();
    bf16x8 ka[16], va[16];
    {
      const int key = wave * 16 + n, f2 = swz(key);
#pragma unroll
      for (int s2 = 0; s2 < 16; s2++) ka[s2] = *(const LAS bf16x8*)(L + key * 1024 + (((4 * s2 + q) ^ f2) * 16));
#pragma unroll
      for (int i = 0; i < 16; i++) va[i] = trfrag<1024, true>(L, 32 * (i & 3), wave * 8 + 2 * (i >> 2), lane);
    }
    if (wave == 0) {
      int spins = 0; bool ok = false;
      for (;;) { CBAR(); if (!ok) ok = ld8(rs, sfoff).y != snapf; if (__all(ok) || ++spins > SPIN_CAP) break; __builtin_amdgcn_s_sleep(1); }
    }
    bar();
    {
      const int off = S2_OFF + xc * 32768 + hg * 16384 + t * 16;
      v4u v0, v1; int spins = 0;
      for (;;) {
        CBAR();
        v0 = ld16(rs, off); v1 = ld16(rs, off + 8192);
        if (__all(max(mx4(v0), mx4(v1)) != SENT) || ++spins > SPIN_CAP) break;
      }
      *(LAS v4u*)(qa + wave * 1056 + lane * 16) = v0;
      *(LAS v4u*)(qa + (8 + wave) * 1056 + lane * 16) = v1;
    }
    bar();
    f32x4 sc = {0, 0, 0, 0};
    {
      bf16x8 qf[16];
#pragma unroll
      for (int s2 = 0; s2 < 16; s2++) qf[s2] = *(const LAS bf16x8*)(qa + n * 1056 + s2 * 64 + q * 16);
      SB();
      f32x4 sc2 = {0, 0, 0, 0};
#pragma unroll
      for (int s2 = 0; s2 < 8; s2++) { sc = MFMA(ka[2 * s2], qf[2 * s2], sc); sc2 = MFMA(ka[2 * s2 + 1], qf[2 * s2 + 1], sc2); }
      sc += sc2;
    }
    float mx = fmaxf(fmaxf(sc[0], sc[1]), fmaxf(sc[2], sc[3]));
    mx = max16(mx); mx = max32(mx);
    if (lane < 16) wmax[lane * 8 + wave] = mx;
    bar();
    {
      f32x4 m0 = *(LAS f32x4*)(wmax + n * 8), m1 = *(LAS f32x4*)(wmax + n * 8 + 4);
      mx = fmaxf(fmaxf(fmaxf(m0[0], m0[1]), fmaxf(m0[2], m0[3])), fmaxf(fmaxf(m1[0], m1[1]), fmaxf(m1[2], m1[3])));
    }
    const float p0 = __builtin_amdgcn_exp2f(sc[0] - mx), p1 = __builtin_amdgcn_exp2f(sc[1] - mx), p2 = __builtin_amdgcn_exp2f(sc[2] - mx), p3 = __builtin_amdgcn_exp2f(sc[3] - mx);
    { v2u o = {pk2(p0, p1), pk2(p2, p3)}; *(LAS v2u*)(Pm + n * 288 + (wave * 16 + q * 4) * 2) = o; }
    float ps = (p0 + p1) + (p2 + p3);
    ps = red16(ps, ps); ps = red32(ps, ps);
    if (lane < 16) wsum[lane * 8 + wave] = ps;
    bar();
    {
      if (wave == 7) {
        f32x4 s0 = *(LAS f32x4*)(wsum + n * 8), s1 = *(LAS f32x4*)(wsum + n * 8 + 4);
        const float l = ((s0[0] + s0[1]) + (s0[2] + s0[3])) + ((s1[0] + s1[1]) + (s1[2] + s1[3]));
        v2u o2 = {canon(__float_as_uint(mx)), canon(__float_as_uint(l))};
        if (lane < 16) st8(rs, S3_OFF + ((hg * 16 + lane) * 32 + split) * S3R + 1024, o2);
      }
      bf16x8 bp[4];
#pragma unroll
      for (int ks = 0; ks < 4; ks++) bp[ks] = *(const LAS bf16x8*)(Pm + n * 288 + ks * 64 + q * 16);
      SB();
      f32x4 acc[4];
#pragma unroll
      for (int T = 0; T < 4; T++) {
        acc[T] = (f32x4){0, 0, 0, 0};
#pragma unroll
        for (int ks = 0; ks < 4; ks++) acc[T] = MFMA(va[T * 4 + ks], bp[ks], acc[T]);
      }
      unsigned d0[4], d1[4];
#pragma unroll
      for (int T = 0; T < 4; T++) { d0[T] = canon(pk2(acc[T][0], acc[T][1])); d1[T] = canon(pk2(acc[T][2], acc[T][3])); }
      auto r0 = __builtin_amdgcn_permlane16_swap(d0[0], d0[1], false, false);
      auto r1 = __builtin_amdgcn_permlane16_swap(d1[0], d1[1], false, false);
      auto r2 = __builtin_amdgcn_permlane16_swap(d0[2], d0[3], false, false);
      auto r3 = __builtin_amdgcn_permlane16_swap(d1[2], d1[3], false, false);
      v4u oa = {r0[0], r1[0], r0[1], r1[1]}, ob = {r2[0], r3[0], r2[1], r3[1]};
      const v4u obs = {dppu<0x128>(ob.x), dppu<0x128>(ob.y), dppu<0x128>(ob.z), dppu<0x128>(ob.w)};
      const bool lo = n < 8;
      const int o3b = S3_OFF + ((hg * 16 + (n & 7)) * 32 + split) * S3R + (wave * 64 + (q & 1) * 16 + (q & 2) * 4) * 2;
      st16(rs, o3b + (lo ? 0 : 64), lo ? oa : obs);
      st16(rs, o3b + 8 * 32 * S3R + (lo ? 64 : 0), lo ? obs : oa);
    }
  }
}

extern "C" void kernel_launch(void* const* d_in, const int* in_sizes, int n_in, void* d_out, int out_size, void* d_ws, size_t ws_size, hipStream_t stream) {
  (void)in_sizes; (void)n_in; (void)out_size; (void)ws_size;
  (void)hipFuncSetAttribute((const void*)fused_kernel, hipFuncAttributeMaxDynamicSharedMemorySize, L_TOTAL);
  fused_kernel<<<256, NT, L_TOTAL, stream>>>((const u16*)d_in[0], (const u16*)d_in[1], (const u16*)d_in[2], (const u16*)d_in[3], (const u16*)d_in[4], (const u16*)d_in[5],
                                             (const u16*)d_in[7], (const u16*)d_in[8], (const u16*)d_in[9], (const u16*)d_in[10], (const u16*)d_in[11], (const u16*)d_in[12],
                                             (u16*)d_out, (unsigned char*)d_ws);
}
```

```cpp
#include <hip/hip_runtime.h>
#include <stdint.h>

typedef unsigned short u16;
typedef unsigned v4u __attribute__((ext_vector_type(4)));
typedef unsigned v2u __attribute__((ext_vector_type(2)));
typedef float f32x4 __attribute__((ext_vector_type(4)));
typedef float f32x2 __attribute__((ext_vector_type(2)));
typedef short bf16x8 __attribute__((ext_vector_type(8)));
typedef short s16x4 __attribute__((ext_vector_type(4)));
typedef __bf16 bf2 __attribute__((ext_vector_type(2)));
#define LAS __attribute__((address_space(3)))

constexpr unsigned SENT = 0xFFFFFFFFu;
constexpr int NT = 512;
constexpr int SPIN_CAP = 1 << 20;
#define NREP 2
#define EGL_DELAY 400
#define ROFF 24
#define CLRAUX 16
#define NTAUX 2
#define NTS " nt"
#define LDNT(T, p) __builtin_nontemporal_load((const T*)(p))
constexpr int SU_OFF = 0;
constexpr int SF_OFF = 131072;
constexpr int SQ_OFF = 262144;
constexpr int CLR_OFF = 327680;
constexpr int S2_OFF = CLR_OFF;
constexpr int S5_OFF = S2_OFF + 8 * 32768;
constexpr int S6_OFF = S5_OFF + 65536;
constexpr int SL_OFF = S6_OFF + 49152;
constexpr int SLF_OFF = SL_OFF + 32768;
constexpr int DN_OFF = SLF_OFF + 32768;
constexpr int EGC_END = DN_OFF + 4096;
constexpr int EGC_PIECES = (EGC_END - S2_OFF) / 16 / 64;
constexpr int S4_OFF = EGC_END;
constexpr int S3R = 1152;
constexpr int S3_OFF = S4_OFF + 16384;
constexpr int CLR_END = S3_OFF + 1024 * S3R;
constexpr int DUMMY_OFF = CLR_END;
constexpr int WS_USED = CLR_END + 65536;
constexpr int L_SM = 131072;
constexpr int L_TOTAL = 131072 + 30720;

__device__ __forceinline__ float bflo(unsigned w) { return __uint_as_float(w << 16); }
__device__ __forceinline__ float bfhi(unsigned w) { return __uint_as_float(w & 0xffff0000u); }
__device__ __forceinline__ unsigned pk2(float a, float b) { bf2 p; p.x = (__bf16)a; p.y = (__bf16)b; return __builtin_bit_cast(unsigned, p); }
__device__ __forceinline__ float rbf(float a) { return (float)(__bf16)a; }
__device__ __forceinline__ unsigned canon(unsigned w) { return w == SENT ? 0x7FC07FC0u : w; }
__device__ __forceinline__ float dot2(unsigned a, unsigned b, float c) { return __builtin_amdgcn_fdot2_f32_bf16(__builtin_bit_cast(bf2, a), __builtin_bit_cast(bf2, b), c, false); }
__device__ __forceinline__ void bar() { __builtin_amdgcn_sched_barrier(0); asm volatile("s_waitcnt lgkmcnt(0)\n\ts_barrier" ::: "memory"); __builtin_amdgcn_sched_barrier(0); }
template <int CTRL> __device__ __forceinline__ float dppf(float v) { return __int_as_float(__builtin_amdgcn_update_dpp(0, __float_as_int(v), CTRL, 0xf, 0xf, false)); }
template <int CTRL> __device__ __forceinline__ unsigned dppu(unsigned v) { return (unsigned)__builtin_amdgcn_update_dpp(0, (int)v, CTRL, 0xf, 0xf, false); }
__device__ __forceinline__ float red32(float a, float b) {
  auto r = __builtin_amdgcn_permlane32_swap(__float_as_uint(a), __float_as_uint(b), false, false);
  return __uint_as_float(r[0]) + __uint_as_float(r[1]);
}
__device__ __forceinline__ float red16(float a, float b) {
  auto r = __builtin_amdgcn_permlane16_swap(__float_as_uint(a), __float_as_uint(b), false, false);
  return __uint_as_float(r[0]) + __uint_as_float(r[1]);
}
__device__ __forceinline__ float max32(float a) { auto r = __builtin_amdgcn_permlane32_swap(__float_as_uint(a), __float_as_uint(a), false, false); return fmaxf(__uint_as_float(r[0]), __uint_as_float(r[1])); }
__device__ __forceinline__ float max16(float a) { auto r = __builtin_amdgcn_permlane16_swap(__float_as_uint(a), __float_as_uint(a), false, false); return fmaxf(__uint_as_float(r[0]), __uint_as_float(r[1])); }
__device__ __forceinline__ float wave_sum(float v) {
  v += dppf<0x128>(v); v += dppf<0x124>(v); v += dppf<0x122>(v); v += dppf<0x121>(v);
  v = red16(v, v); v = red32(v, v); return v;
}
__device__ __forceinline__ float wave_max(float v) {
  v = fmaxf(v, dppf<0x128>(v)); v = fmaxf(v, dppf<0x124>(v)); v = fmaxf(v, dppf<0x122>(v)); v = fmaxf(v, dppf<0x121>(v));
  v = max16(v); v = max32(v); return v;
}
__device__ __forceinline__ unsigned ilo(unsigned a, unsigned b) { return __builtin_amdgcn_perm(b, a, 0x05040100u); }
__device__ __forceinline__ unsigned ihi(unsigned a, unsigned b) { return __builtin_amdgcn_perm(b, a, 0x07060302u); }
__device__ __forceinline__ void interleave(v4u& a, v4u& b) {
  v4u x, y;
  x.x = ilo(a.x, b.x); x.y = ihi(a.x, b.x); x.z = ilo(a.y, b.y); x.w = ihi(a.y, b.y);
  y.x = ilo(a.z, b.z); y.y = ihi(a.z, b.z); y.z = ilo(a.w, b.w); y.w = ihi(a.w, b.w);
  a = x; b = y;
}
typedef __amdgpu_buffer_rsrc_t rsrc_t;
__device__ __forceinline__ v4u ld16(rsrc_t r, int off) { return __builtin_amdgcn_raw_buffer_load_b128(r, off, 0, 16); }
__device__ __forceinline__ v2u ld8(rsrc_t r, int off) { return __builtin_amdgcn_raw_buffer_load_b64(r, off, 0, 16); }
__device__ __forceinline__ void st16(rsrc_t r, int off, v4u v) { __builtin_amdgcn_raw_buffer_store_b128(v, r, off, 0, 16); }
__device__ __forceinline__ void st8(rsrc_t r, int off, v2u v) { __builtin_amdgcn_raw_buffer_store_b64(v, r, off, 0, 16); }
__device__ __forceinline__ void glds16(const void* g, unsigned lds_base) {
  unsigned sv; asm volatile("s_mov_b32 %0, m0\n\ts_mov_b32 m0, %2\n\ts_nop 0\n\tglobal_load_lds_dwordx4 %1, off" NTS "\n\ts_mov_b32 m0, %0" : "=&s"(sv) : "v"(g), "s"(lds_base) : "memory");
}
#define RL(v, l) __int_as_float(__builtin_amdgcn_readlane(__float_as_int(v), (l)))
#define SB() __builtin_amdgcn_sched_barrier(0)
#define CBAR() do { asm volatile("" ::: "memory"); __builtin_amdgcn_sched_barrier(0); } while (0)

__device__ __forceinline__ int swz(int row) { return ((row & 3) | ((row >> 1) & 4)) << 1; }
template <int AUX> __device__ __forceinline__ void dma16a(const void* g, LAS unsigned char* l) { __builtin_amdgcn_global_load_lds((const __attribute__((address_space(1))) unsigned*)g, (LAS unsigned*)l, 16, 0, AUX); }
#define dma16 dma16a<NTAUX>
#define NARROW 2
#define dma16n dma16a<NARROW>
#define LDNAR(T, p) __builtin_nontemporal_load((const T*)(p))
template <int RB, bool SW>
__device__ __forceinline__ bf16x8 trfrag(const LAS unsigned char* img, int k0, int ch0, int lane) {
  const int g = lane >> 4, qq = (lane & 15) >> 2, pp = lane & 3;
  s16x4 h[2];
#pragma unroll
  for (int tt = 0; tt < 2; tt++) {
    const int row = k0 + 8 * g + 4 * tt + qq;
    const int ch = SW ? ((ch0 + (pp >> 1)) ^ swz(row)) : (ch0 + (pp >> 1));
    h[tt] = __builtin_amdgcn_ds_read_tr16_b64_v4i16((LAS s16x4*)(img + row * RB + ch * 16 + (pp & 1) * 8));
  }
  return (bf16x8){h[0].x, h[0].y, h[0].z, h[0].w, h[1].x, h[1].y, h[1].z, h[1].w};
}
#define MFMA(a, b, c) __builtin_amdgcn_mfma_f32_16x16x32_bf16(a, b, c, 0, 0, 0)
#define VMWAIT0() do { asm volatile("" ::: "memory"); __builtin_amdgcn_s_waitcnt(0x0F70); asm volatile("s_waitcnt vmcnt(0)" ::: "memory"); } while (0)
#define LGWAIT0() asm volatile("s_waitcnt lgkmcnt(0)" ::: "memory")
typedef unsigned long long u64;
__device__ __forceinline__ unsigned mx4(v4u v) { return max(max(v.x, v.y), max(v.z, v.w)); }
__device__ __forceinline__ void stclr(rsrc_t r, int off, v4u v) { __builtin_amdgcn_raw_buffer_store_b128(v, r, off, 0, CLRAUX); }

__global__ void __launch_bounds__(NT, 2) fused_kernel(
    const u16* __restrict__ x, const u16* __restrict__ ckv, const u16* __restrict__ w_q, const u16* __restrict__ w_uk,
    const u16* __restrict__ w_uv, const u16* __restrict__ w_o, const u16* __restrict__ w1, const u16* __restrict__ b1,
    const u16* __restrict__ w2, const u16* __restrict__ b2, const u16* __restrict__ cvec, const u16* __restrict__ w_mod,
    u16* __restrict__ out, unsigned char* ws) {
  extern __shared__ __attribute__((aligned(16))) unsigned char lds_raw[];
  LAS unsigned char* L = (LAS unsigned char*)lds_raw;
  const int t = threadIdx.x, lane = t & 63, wave = __builtin_amdgcn_readfirstlane(t >> 6);
  const int b = blockIdx.x, xc = b & 7, r = ((b >> 3) + ROFF) & 31;
  const int n = lane & 15, q = lane >> 4;
  const rsrc_t rs = __builtin_amdgcn_make_buffer_rsrc((void*)ws, 0, WS_USED, 0x00020000);
  LAS unsigned char* SM = L + L_SM;
  const v4u sv4 = {SENT, SENT, SENT, SENT};
  const u64 tst = __builtin_amdgcn_s_memrealtime();
  (void)tst;
#define DELAY(x) do { while (__builtin_amdgcn_s_memrealtime() - tst < (x)) __builtin_amdgcn_s_sleep(2); } while (0)

  if (r < 16) {
    const int head = 4 * xc + (r >> 2), cs = r & 3;
    LAS unsigned char* hp = SM;
    LAS unsigned* q16 = (LAS unsigned*)(SM + 2304);
    LAS unsigned char* qst = SM + 8192;
    LAS unsigned char* WKI = L + 98304;
    LAS unsigned char* WQI = L + 65536;
    const int sfoff = SF_OFF + xc * 8192 + lane * 128;
    LAS unsigned* lcnt = (LAS unsigned*)(SM + 9472);
    const unsigned short cvl0 = cvec[(r & 7) * 64 + (t >> 4)], cvl1 = cvec[(r & 7) * 64 + 32 + (t >> 4)];
    bf16x8 wk[4];
    if (wave < 4) {
      const int suoff = SU_OFF + xc * 16384 + cs * 4096 + t * 16;
      const int sqoff = SQ_OFF + head * 2048 + (t & 127) * 16;
      const v4u snapu = ld16(rs, suoff);
      const v4u snapq = ld16(rs, sqoff);
      const unsigned snapf = ld8(rs, sfoff).y;
      if (t == 0) *lcnt = 0u;
      bar();
      v4u v = snapu; int spins = 0; bool ok = false;
      for (;;) {
        CBAR();
        if (!ok) v = ld16(rs, suoff);
        ok = (v.y != snapu.y) & (v.w != snapu.w);
        if (__all(ok) || ++spins > SPIN_CAP) break;
      }
      v2u hh = {v.x, v.z};
      *(LAS v2u*)(hp + ((t >> 4) & 7) * 272 + (t >> 7) * 128 + (t & 15) * 8) = hh;
      bar();
#pragma unroll
      for (int i = 0; i < 4; i++) wk[i] = *(const LAS bf16x8*)(WKI + (wave * 16 + n) * 256 + (((4 * i + q) ^ swz(n)) * 16));
      if (wave < 2) {
        spins = 0; ok = false;
        for (;;) {
          CBAR();
          if (!ok) v = ld16(rs, sqoff);
          ok = (v.y != snapq.y) & (v.w != snapq.w);
          if (__all(ok) || ++spins > SPIN_CAP) break;
        }
        v2u o2 = {v.x, v.z};
        *(LAS v2u*)(qst + (t >> 5) * 272 + (t & 31) * 8) = o2;
      } else if (wave == 2) {
        spins = 0; ok = false;
        for (;;) { CBAR(); if (!ok) ok = ld8(rs, sfoff).y != snapf; if (__all(ok) || ++spins > SPIN_CAP) break; }
      }
      bar();
    } else {
      const int lw = wave - 4;
      const int sqo = SQ_OFF + head * 2048 + cs * 512 + lw * 128 + ((lane >> 2) & 7) * 16;
      const v4u oldq = ld16(rs, sqo);
      bar();
#pragma unroll
      for (int i = 0; i < 16; i++) {
        const int j = 4 * i + lw, rg = j & 31, row = 4 * rg + q;
        const u16* src = (i < 8 ? w_q : w_uk) + (size_t)(cs * 128 + row) * 4096 + head * 128 + ((n ^ swz(row)) * 8);
        dma16(src, L + 65536 + j * 1024);
      }
      VMWAIT0();
      if (lane == 0) __hip_atomic_fetch_add(lcnt, 1u, __ATOMIC_RELAXED, __HIP_MEMORY_SCOPE_WORKGROUP);
      { int spins = 0; while (__hip_atomic_load(lcnt, __ATOMIC_RELAXED, __HIP_MEMORY_SCOPE_WORKGROUP) < 4u && ++spins < SPIN_CAP) {} }
      CBAR();
      bf16x8 wqf[8];
#pragma unroll
      for (int i = 0; i < 8; i++) wqf[i] = trfrag<256, true>(WQI, 32 * (i & 3), 4 * lw + 2 * (i >> 2), lane);
#pragma unroll
      for (int i = 0; i < 4; i++) wk[i] = *(const LAS bf16x8*)(WKI + (wave * 16 + n) * 256 + (((4 * i + q) ^ swz(n)) * 16));
      bar();
      f32x4 a0 = {0, 0, 0, 0}, a1 = {0, 0, 0, 0}; bf16x8 hf[4];
#pragma unroll
      for (int i = 0; i < 4; i++) hf[i] = *(const LAS bf16x8*)(hp + (n & 7) * 272 + i * 64 + q * 16);
      SB();
#pragma unroll
      for (int i = 0; i < 4; i++) { a0 = MFMA(hf[i], wqf[i], a0); a1 = MFMA(hf[i], wqf[4 + i], a1); }
      const float sq = red16((a0[0] + a0[1]) + (a0[2] + a0[3]), (a1[0] + a1[1]) + (a1[2] + a1[3]));
      const unsigned d = pk2(sq, dppf<0x101>(sq));
      const unsigned d2 = dppu<0x102>(d);
      if (lane < 32 && (lane & 3) == 0) { v4u o = {d, oldq.y + 1u, d2, oldq.w + 1u}; st16(rs, sqo, o); }
      {
        const int cb = S3_OFF + (head * 32 + cs * 8) * S3R + (t - 256) * 16;
        stclr(rs, cb, sv4); stclr(rs, cb + 4096, sv4);
        if (wave == 4) stclr(rs, cb + 8192, sv4);
        else if (wave == 5 && lane < 8) stclr(rs, S4_OFF + head * 512 + cs * 128 + lane * 16, sv4);
      }
      VMWAIT0();
      bar();
      if (lane < 32 && (lane & 3) == 0) { v4u o = {d, oldq.y + 2u, d2, oldq.w + 2u}; st16(rs, sqo, o); }
    }
    {
      f32x4 a = {0, 0, 0, 0}; bf16x8 qf[4];
#pragma unroll
      for (int i = 0; i < 4; i++) qf[i] = *(const LAS bf16x8*)(qst + (n & 3) * 272 + i * 64 + q * 16);
      SB();
      f32x4 ab = {0, 0, 0, 0};
      a = MFMA(wk[0], qf[0], a); ab = MFMA(wk[1], qf[1], ab); a = MFMA(wk[2], qf[2], a); ab = MFMA(wk[3], qf[3], ab);
      a += ab;
#pragma unroll
      for (int i = 0; i < 4; i++) { a[i] += dppf<0xB1>(a[i]); a[i] += dppf<0x4E>(a[i]); }
      const float SC = 0.08838834764831845f * 1.4426950408889634f;
      v2u o = {canon(pk2(a[0] * SC, a[1] * SC)), canon(pk2(a[2] * SC, a[3] * SC))};
      if (n == 0) *(LAS v2u*)(q16 + 64 + wave * 8 + q * 2) = o;
    }
    bar();
    if (wave < 2) { const v4u o = *(const LAS v4u*)(q16 + 64 + n * 4); st16(rs, S2_OFF + (wave * 4 + q) * 32768 + head * 1024 + cs * 256 + n * 16, o); }
    bar();
    {
      const int kj = r & 7, cg = xc * 2 + (r >> 3);
      const u16* pl = w_mod + (size_t)(kj * 64 + (t >> 4)) * 3072 + 1024 + cg * 128 + n * 8;
      v4u w0 = LDNT(v4u, pl), w1r = LDNT(v4u, pl + (size_t)32 * 3072);
      CBAR();
      if (r < 8) {
#pragma unroll
        for (int i = 0; i < 8; i++) {
          const int rg = 8 * i + wave, row = 8 * rg + (lane >> 3);
          glds16(w_o + (size_t)(xc * 512 + row) * 512 + r * 64 + (((lane & 7) ^ ((row & 2) | ((row >> 1) & 4))) * 8), (unsigned)(rg * 1024));
        }
        asm volatile("s_waitcnt vmcnt(8)" ::: "memory");
      }
      const float c0 = bflo(cvl0), c1 = bflo(cvl1);
      const unsigned s01 = pk2(c0 * __builtin_amdgcn_rcpf(1.f + __expf(-c0)), c1 * __builtin_amdgcn_rcpf(1.f + __expf(-c1)));
      interleave(w0, w1r);
      float a[8] = {dot2(w0.x, s01, 0.f), dot2(w0.y, s01, 0.f), dot2(w0.z, s01, 0.f), dot2(w0.w, s01, 0.f),
                    dot2(w1r.x, s01, 0.f), dot2(w1r.y, s01, 0.f), dot2(w1r.z, s01, 0.f), dot2(w1r.w, s01, 0.f)};
      float b0 = red32(a[0], a[4]), b1x = red32(a[1], a[5]), b2x = red32(a[2], a[6]), b3 = red32(a[3], a[7]);
      f32x2 o = {red16(b0, b2x), red16(b1x, b3)};
      LAS float* apart = (LAS float*)(SM + 12288);
      *(LAS f32x2*)(apart + wave * 128 + n * 8 + 4 * (lane >> 5) + 2 * ((lane >> 4) & 1)) = o;
      bar();
      if (wave == 7) {
        f32x2 sacc = {0.f, 0.f};
#pragma unroll
        for (int w = 0; w < 8; w++) sacc += *(LAS f32x2*)(apart + w * 128 + lane * 2);
        const unsigned d = canon(pk2(sacc.x, sacc.y));
        v4u o4 = {dppu<0x00>(d), dppu<0x55>(d), dppu<0xAA>(d), dppu<0xFF>(d)};
        if ((lane & 3) == 0) st16(rs, SL_OFF + kj * 4096 + cg * 256 + lane * 4, o4);
        if (cg < 4 || cg >= 12) {
          const unsigned f0 = canon(__float_as_uint(sacc.x)), f1 = canon(__float_as_uint(sacc.y));
          v4u of = {f0, f1, dppu<0x101>(f0), dppu<0x101>(f1)};
          if (!(lane & 1)) st16(rs, SLF_OFF + kj * 4096 + (cg < 4 ? cg : cg - 8) * 512 + lane * 8, of);
        }
      }
      bar();
    }
    if (r < 8) {
      LAS float* fpart = (LAS float*)(SM + 3072);
      LAS unsigned char* cst = SM + 4096;
      LAS float* fp64 = (LAS float*)(SM + 16384);
      VMWAIT0();
      bar();
      bf16x8 wof[8];
      {
        const int g = lane >> 4, qq = (lane & 15) >> 2, pp = lane & 3;
#pragma unroll
        for (int i = 0; i < 8; i++) {
          s16x4 h[2];
#pragma unroll
          for (int tt = 0; tt < 2; tt++) {
            const int row = 32 * (2 * wave + (i >> 2)) + 8 * g + 4 * tt + qq;
            const int ch = (2 * (i & 3) + (pp >> 1)) ^ ((row & 2) | ((row >> 1) & 4));
            h[tt] = __builtin_amdgcn_ds_read_tr16_b64_v4i16((LAS s16x4*)(L + row * 128 + ch * 16 + (pp & 1) * 8));
          }
          wof[i] = (bf16x8){h[0].x, h[0].y, h[0].z, h[0].w, h[1].x, h[1].y, h[1].z, h[1].w};
        }
      }
      if (wave >= 4) {
        int spins = 0;
        for (;;) { CBAR(); v4u v = ld16(rs, S2_OFF + xc * 32768 + xc * 4096 + (t - 256) * 16); if (__all(mx4(v) != SENT) || ++spins > SPIN_CAP) break; __builtin_amdgcn_s_sleep(2); }
      }
      bar();
      {
        v4u av0, av1; int spins = 0;
        const int off = S4_OFF + ((4 * xc + (wave >> 1)) * 2 + (n & 1)) * 256 + (wave & 1) * 128 + q * 16;
        for (;;) { CBAR(); av0 = ld16(rs, off); av1 = ld16(rs, off + 64); if (__all(max(mx4(av0), mx4(av1)) != SENT) || ++spins > SPIN_CAP) break; }
        f32x4 a[4];
#pragma unroll
        for (int T = 0; T < 4; T++) { a[T] = MFMA(__builtin_bit_cast(bf16x8, av0), wof[T], ((f32x4){0, 0, 0, 0})); a[T] = MFMA(__builtin_bit_cast(bf16x8, av1), wof[4 + T], a[T]); }
        const f32x4 sel = q == 0 ? a[0] : q == 1 ? a[1] : q == 2 ? a[2] : a[3];
        fp64[wave * 64 + lane] = sel[0] + sel[1];
        bar();
        if (wave == 0) {
          float v = 0.f;
#pragma unroll
          for (int w = 0; w < 8; w++) v += fp64[w * 64 + lane];
          const unsigned d = canon(pk2(v, dppf<0x101>(v)));
          if (!(lane & 1)) ((LAS unsigned*)cst)[lane >> 1] = d;
          LGWAIT0();
          const v4u o = *(const LAS v4u*)(cst + (lane & 7) * 16);
          st16(rs, S5_OFF + (lane >> 3) * 8192 + xc * 1024 + r * 128 + (lane & 7) * 16, o);
        }
      }
    } else {
      const int ti = xc * 8 + (r - 8);
      LAS unsigned char* W2I = L;
      const float b2v = bflo(b2[ti * 8 + (lane & 7)]);
      const float x8 = bflo(x[ti * 8 + (lane & 7)]);
      CBAR();
#pragma unroll
      for (int i = 0; i < 3; i++) { const int rg = 8 * i + wave; if (rg < 22) { int row = rg * 64 + lane; row = row < 1376 ? row : 1375; dma16n(w2 + (size_t)row * 512 + ti * 8, W2I + rg * 1024); } }
      CBAR();
      LAS float* hpart = (LAS float*)(SM + 2048);
      LAS unsigned* sbuf = (LAS unsigned*)(SM + 2560);
      VMWAIT0();
      bar();
      bf16x8 w2f[6];
#pragma unroll
      for (int i = 0; i < 6; i++) {
        const int ks = wave + 8 * i < 43 ? wave + 8 * i : 0;
        const int g = lane >> 4, qq = (lane & 15) >> 2, pp = lane & 3;
        s16x4 h0 = __builtin_amdgcn_ds_read_tr16_b64_v4i16((LAS s16x4*)(W2I + (32 * ks + 8 * g + qq) * 16 + (pp & 1) * 8));
        s16x4 h1 = __builtin_amdgcn_ds_read_tr16_b64_v4i16((LAS s16x4*)(W2I + (32 * ks + 8 * g + 4 + qq) * 16 + (pp & 1) * 8));
        w2f[i] = (bf16x8){h0.x, h0.y, h0.z, h0.w, h1.x, h1.y, h1.z, h1.w};
      }
      float xn8 = 0.f, gbv = 0.f;
      if (wave == 0) {
        const int off = lane < 16 ? S5_OFF + xc * 8192 + (lane & 7) * 1024 + ti * 16 : SLF_OFF + ((lane >> 1) & 7) * 4096 + ((lane & 32) ? 2048 : 0) + ti * 32 + (lane & 1) * 16;
        v4u v; int spins = 0;
        for (;;) { CBAR(); v = ld16(rs, off); if (__all(mx4(v) != SENT) || ++spins > SPIN_CAP) break; __builtin_amdgcn_s_sleep(4); }
        if (lane < 48) *(LAS v4u*)(sbuf + lane * 4) = v;
        LGWAIT0();
        const LAS u16* sh = (const LAS u16*)sbuf; const LAS float* sf = (const LAS float*)sbuf;
        float ao = 0, ga8 = 0;
#pragma unroll
        for (int k = 0; k < 8; k++) { ao += bflo(sh[k * 8 + (lane & 7)]); ga8 += sf[64 + k * 8 + (lane & 7)]; gbv += sf[128 + k * 8 + (lane & 7)]; }
        xn8 = x8 + ga8 * ao;
      }
      bar();
      {
        v4u hv[6]; int spins = 0;
        const int off = S6_OFF + xc * 6144 + wave * 128 + q * 16, off5 = wave < 3 ? off + 5120 : off;
        for (;;) {
          CBAR();
#pragma unroll
          for (int i = 0; i < 5; i++) hv[i] = ld16(rs, off + i * 1024);
          hv[5] = ld16(rs, off5);
          const unsigned m = max(max(max(mx4(hv[0]), mx4(hv[1])), max(mx4(hv[2]), mx4(hv[3]))), max(mx4(hv[4]), mx4(hv[5])));
          if (__all(m != SENT) || ++spins > SPIN_CAP) break;
        }
        if (wave >= 3) hv[5] = (v4u){0, 0, 0, 0};
        f32x4 a = {0, 0, 0, 0}, a2 = {0, 0, 0, 0};
#pragma unroll
        for (int i = 0; i < 3; i++) { a = MFMA(__builtin_bit_cast(bf16x8, hv[2 * i]), w2f[2 * i], a); a2 = MFMA(__builtin_bit_cast(bf16x8, hv[2 * i + 1]), w2f[2 * i + 1], a2); }
        if (lane < 8) hpart[wave * 8 + lane] = a[0] + a2[0];
      }
      bar();
      if (wave == 0) {
        float a = hpart[lane];
        a += dppf<0x128>(a); a = red16(a, a); a = red32(a, a);
        const float res = xn8 + gbv * (a + b2v);
        if (lane < 8) out[ti * 8 + lane] = __builtin_bit_cast(u16, (__bf16)res);
      }
    }
  } else if (r >= 24) {
    const int e = r - 24, head = 4 * xc + (e >> 1), half = e & 1, ti = xc * 8 + e;
    const int gi = e * 8 + xc;
    const int wst = (gi < 43 ? gi : 0) * 4;
    const int mcol = wave * 64 + 8 * (lane & 7) + 4 * (lane >> 5) + 2 * ((lane >> 4) & 1) + ((lane >> 3) & 1);
    LAS unsigned char* W1I = L + 65536;
    LAS unsigned char* STG = L + 98304;
    LAS u16* scl = (LAS u16*)(SM);
    LAS unsigned* cc16 = (LAS unsigned*)(SM + 1024);
    LAS unsigned* ml = (LAS unsigned*)(SM + 2048);
    LAS float* stat = (LAS float*)(SM + 5120);
    LAS u16* y16 = (LAS u16*)(SM + 5376);
    LAS float* gpart = (LAS float*)(SM + 6400);
    LAS unsigned* pub = (LAS unsigned*)(SM + 7424);
    LAS unsigned* cnt = (LAS unsigned*)(SM + 7680);
    LAS unsigned* tbase = (LAS unsigned*)(SM + 8192);
    LAS float* apart = (LAS float*)(SM + 10240);
    LAS float* fpart = (LAS float*)(SM + 14336);
    LAS unsigned char* cst = SM + 15360;
    const int puoff = SU_OFF + q * 16384 + (xc * 8 + e) * 256 + n * 16;
    const int pfoff = SF_OFF + n * 8192 + ti * 128;
    const int sfoff = SF_OFF + (8 + xc) * 8192 + lane * 128;
#define LDSADD(p) __hip_atomic_fetch_add((p), 1u, __ATOMIC_RELAXED, __HIP_MEMORY_SCOPE_WORKGROUP)
#define REPUB() do { v2u p2_ = *(LAS v2u*)(pub + n * 2); v4u tb_ = *(LAS v4u*)(tbase + lane * 8); unsigned tf_ = tbase[lane * 8 + 4]; unsigned k_ = 0; if (lane == 0) k_ = LDSADD(cnt); k_ = __builtin_amdgcn_readfirstlane(k_); \
      v4u o_ = {p2_.x, tb_.x + k_, p2_.y, tb_.y + k_}; st16(rs, puoff, o_); v4u o2_ = {p2_.x, tb_.z + k_, p2_.y, tb_.w + k_}; st16(rs, puoff + 65536, o2_); \
      if (lane < 16) { v2u f_ = {0u, tf_ + k_}; st8(rs, pfoff, f_); } } while (0)
    v2u pd = {0, 0};
    v4u old0 = {0, 0, 0, 0}, old1 = {0, 0, 0, 0}, xv = {0, 0, 0, 0}; unsigned snapf = 0, xw = 0;
    v4u wm[2]; unsigned short cv[2];
    {
      const u16* p = w_mod + (size_t)(e * 64 + (t >> 4)) * 3072 + (n >> 3) * 512 + xc * 64 + (n & 7) * 8;
      wm[0] = LDNT(v4u, p); wm[1] = LDNT(v4u, p + (size_t)32 * 3072);
      cv[0] = cvec[e * 64 + (t >> 4)]; cv[1] = cvec[e * 64 + 32 + (t >> 4)];
    }
    CBAR();
    const unsigned oldf = ld8(rs, pfoff).y;
    if (wave == 0) { old0 = ld16(rs, puoff); old1 = ld16(rs, puoff + 65536); xv = *(const v4u*)(x + lane * 8); xw = ((const unsigned*)x)[xc * 32 + (lane & 31)]; }
    if (wave == 1) snapf = ld8(rs, sfoff).y;
    CBAR();
    {
      const int pc = t >= 64 ? t - 64 : t;
      st16(rs, wave == 0 ? DUMMY_OFF + ti * 1024 + lane * 16 : S2_OFF + ti * (EGC_PIECES * 16) + (pc < EGC_PIECES ? pc : pc - EGC_PIECES) * 16, sv4);
    }
    CBAR();
    float ln0 = 0.f, ln1 = 0.f;
    if (wave == 0) {
      float xs[8] = {bflo(xv.x), bfhi(xv.x), bflo(xv.y), bfhi(xv.y), bflo(xv.z), bfhi(xv.z), bflo(xv.w), bfhi(xv.w)};
      float s = 0;
#pragma unroll
      for (int i = 0; i < 8; i++) s += xs[i];
      const float mean = wave_sum(s) * (1.f / 512.f);
      float vs = 0;
#pragma unroll
      for (int i = 0; i < 8; i++) { float d = xs[i] - mean; vs += d * d; }
      const float rstd = rsqrtf(wave_sum(vs) * (1.f / 512.f) + 1e-5f);
      ln0 = (bflo(xw) - mean) * rstd; ln1 = (bfhi(xw) - mean) * rstd;
      v4u tb = {old0.y, old0.w, old1.y, old1.w};
      *(LAS v4u*)(tbase + lane * 8) = tb; tbase[lane * 8 + 4] = oldf;
      if (lane == 0) { cnt[0] = 2u + NREP; cnt[1] = 0u; }
    }
    CBAR();
    {
      const float c0 = bflo(cv[0]), c1 = bflo(cv[1]);
      const unsigned s01 = pk2(c0 * __builtin_amdgcn_rcpf(1.f + __expf(-c0)), c1 * __builtin_amdgcn_rcpf(1.f + __expf(-c1)));
      interleave(wm[0], wm[1]);
      float a[8] = {dot2(wm[0].x, s01, 0.f), dot2(wm[0].y, s01, 0.f), dot2(wm[0].z, s01, 0.f), dot2(wm[0].w, s01, 0.f),
                    dot2(wm[1].x, s01, 0.f), dot2(wm[1].y, s01, 0.f), dot2(wm[1].z, s01, 0.f), dot2(wm[1].w, s01, 0.f)};
      float b0 = red32(a[0], a[4]), b1x = red32(a[1], a[5]), b2x = red32(a[2], a[6]), b3 = red32(a[3], a[7]);
      f32x2 o = {red16(b0, b2x), red16(b1x, b3)};
      *(LAS f32x2*)(apart + wave * 128 + n * 8 + 4 * (lane >> 5) + 2 * ((lane >> 4) & 1)) = o;
    }
    bar();
    if (wave == 0) {
      f32x2 sacc = {0.f, 0.f};
#pragma unroll
      for (int w = 0; w < 8; w++) sacc += *(LAS f32x2*)(apart + w * 128 + lane * 2);
      auto r0 = __builtin_amdgcn_permlane32_swap(__float_as_uint(sacc.x), __float_as_uint(sacc.x), false, false);
      auto r1 = __builtin_amdgcn_permlane32_swap(__float_as_uint(sacc.y), __float_as_uint(sacc.y), false, false);
      const float one = e == 0 ? 1.f : 0.f;
      const float u0 = ln0 * (one + __uint_as_float(r0[1])) + __uint_as_float(r0[0]), u1 = ln1 * (one + __uint_as_float(r1[1])) + __uint_as_float(r1[0]);
      if (lane < 32) pub[lane] = pk2(u0, u1);
      LGWAIT0();
      pd = *(LAS v2u*)(pub + n * 2);
      v4u o = {pd.x, old0.y + 1u, pd.y, old0.w + 1u}; st16(rs, puoff, o);
      v4u o2 = {pd.x, old1.y + 1u, pd.y, old1.w + 1u}; st16(rs, puoff + 65536, o2);
    }
    unsigned karr = 0;
    if (wave != 0) { VMWAIT0(); if (lane == 0) karr = LDSADD(cnt + 1); }
    const bool lastw = __builtin_amdgcn_readfirstlane(karr) == 6u;
    if (lastw && lane < 16) { v2u f = {0u, oldf + 1u}; st8(rs, pfoff, f); }
#pragma unroll
    for (int i = 0; i < 4; i++) if (gi < 43) { const int rg = 8 * i + wave; dma16n(w1 + (size_t)(rg * 16 + (lane >> 2)) * 1376 + wst * 8 + (lane & 3) * 8, W1I + rg * 1024); }
    CBAR();
    DELAY(EGL_DELAY);
    const float xme = bflo(x[mcol]);
    const float b1v = bflo(b1[wst * 8 + (lane & 31)]);
    CBAR();
#pragma unroll
    for (int i = 0; i < 8; i++) {
      const int rg = 8 * i + wave, row = 4 * rg + q;
      dma16(w_uv + (size_t)(half * 256 + row) * 4096 + head * 128 + ((n ^ swz(row)) * 8), L + rg * 1024);
    }
    CBAR();
    if (wave == 0 || lastw) {
#pragma unroll 1
      for (int k = 0; k < NREP; k++) {
        __builtin_amdgcn_s_sleep(14);
        if (wave == 0) { v4u o_ = {pd.x, old0.y + 2u + k, pd.y, old0.w + 2u + k}; st16(rs, puoff, o_); v4u o2_ = {pd.x, old1.y + 2u + k, pd.y, old1.w + 2u + k}; st16(rs, puoff + 65536, o2_); }
        if (lastw && lane < 16) { v2u f_ = {0u, oldf + 2u + k}; st8(rs, pfoff, f_); }
      }
    }
    VMWAIT0();
    bar();
    bf16x8 wvf[16], w1f[4];
#pragma unroll
    for (int s2 = 0; s2 < 16; s2++) wvf[s2] = trfrag<256, true>(L, 64 * (wave & 3) + 32 * (s2 >> 3), 2 * (s2 & 7), lane);
#pragma unroll
    for (int i = 0; i < 4; i++) w1f[i] = trfrag<64, false>(W1I, 64 * wave + 32 * (i >> 1), 2 * (i & 1), lane);
    if (wave == 1) {
      int spins = 0; bool ok = false;
      for (;;) { CBAR(); if (!ok) ok = ld8(rs, sfoff).y != snapf; if (__all(ok) || ++spins > SPIN_CAP) break; if ((spins & 3) == 0) REPUB(); __builtin_amdgcn_s_sleep(2); }
      spins = 0;
      for (;;) { CBAR(); v4u v = ld16(rs, S2_OFF + xc * 32768 + head * 1024 + lane * 16); if (__all(mx4(v) != SENT) || ++spins > SPIN_CAP) break; if ((spins & 3) == 0) REPUB(); __builtin_amdgcn_s_sleep(2); }
    }
    bar();
    {
      LAS float* sclf = (LAS float*)(SM);
      LAS float* epart = (LAS float*)(STG);
      {
        const int soff = S3_OFF + (head * 32 + (lane & 31)) * S3R + 1024;
        if (wave == 7) {
          v2u stt; int spins = 0;
          for (;;) { CBAR(); stt = ld8(rs, soff); if (__all(max(stt.x, stt.y) != SENT) || ++spins > SPIN_CAP) break; if ((spins & 3) == 0) REPUB(); }
          const float m = __uint_as_float(stt.x), l = __uint_as_float(stt.y);
          const float mg = wave_max(m);
          const float sc = __builtin_amdgcn_exp2f(m - mg);
          const float il = 2.f * __builtin_amdgcn_rcpf(wave_sum(sc * l));
          if (lane < 32) sclf[lane] = sc * il;
        }
      }
      v4u p0, p1; int spins = 0;
      const int off = S3_OFF + (head * 32 + (wave >> 2) * 16 + n) * S3R + half * 512 + (wave & 3) * 128 + q * 16;
      for (;;) {
        CBAR();
        p0 = ld16(rs, off); p1 = ld16(rs, off + 64);
        if (__all(max(mx4(p0), mx4(p1)) != SENT) || ++spins > SPIN_CAP) break;
        if ((spins & 15) == 0) REPUB();
      }
      f32x4 y0[8];
#pragma unroll
      for (int T = 0; T < 8; T++) { y0[T] = MFMA(__builtin_bit_cast(bf16x8, p0), wvf[T], ((f32x4){0, 0, 0, 0})); y0[T] = MFMA(__builtin_bit_cast(bf16x8, p1), wvf[8 + T], y0[T]); }
      bar();
      const f32x4 s4 = *(const LAS f32x4*)(sclf + 16 * (wave >> 2) + 4 * q);
      float z[8];
#pragma unroll
      for (int T = 0; T < 8; T++) z[T] = (y0[T][0] * s4[0] + y0[T][1] * s4[1]) + (y0[T][2] * s4[2] + y0[T][3] * s4[3]);
      const float r0 = red32(z[0], z[4]), r1 = red32(z[1], z[5]), r2 = red32(z[2], z[6]), r3 = red32(z[3], z[7]);
      const float u0 = red16(r0, r2), u1 = red16(r1, r3);
      const int T0 = 4 * (lane >> 5) + 2 * ((lane >> 4) & 1);
      epart[wave * 128 + T0 * 16 + n] = u0; epart[wave * 128 + T0 * 16 + 16 + n] = u1;
      bar();
      if (wave == 0) {
        f32x2 sacc = {0.f, 0.f};
#pragma unroll
        for (int w = 0; w < 8; w++) sacc += *(LAS f32x2*)(epart + w * 128 + lane * 2);
        const unsigned d = canon(pk2(sacc.x, sacc.y));
        v4u o4 = {dppu<0x00>(d), dppu<0x55>(d), dppu<0xAA>(d), dppu<0xFF>(d)};
        if ((lane & 3) == 0) st16(rs, S4_OFF + (head * 2 + half) * 256 + lane * 4, o4);
      }
    }
    if (gi >= 43) {
      if (wave == 0) {
        int spins = 0;
        for (;;) {
          CBAR(); v4u v = ld16(rs, DN_OFF + xc * 128);
          if (v.x != SENT || ++spins > SPIN_CAP) break;
          if ((spins & 3) == 0) REPUB();
          __builtin_amdgcn_s_sleep(8);
        }
      } return;
    }
    {
      v4u v0, v1, v2; int spins = 0;
      const int off = SL_OFF + wave * 4096 + lane * 16;
      for (;;) { CBAR(); v0 = ld16(rs, off); v1 = ld16(rs, off + 1024); v2 = ld16(rs, off + 2048); if (__all(max(max(mx4(v0), mx4(v1)), mx4(v2)) != SENT) || ++spins > SPIN_CAP) break; if ((spins & 15) == 0) REPUB(); __builtin_amdgcn_s_sleep(1); }
      *(LAS v4u*)(L + wave * 3072 + lane * 16) = v0; *(LAS v4u*)(L + wave * 3072 + 1024 + lane * 16) = v1; *(LAS v4u*)(L + wave * 3072 + 2048 + lane * 16) = v2;
    }
    bar();
    float gam = 0.f, sbm = 0.f, cb1 = 0.f;
    {
      const LAS u16* mlh = (const LAS u16*)L;
#pragma unroll
      for (int k = 0; k < 8; k++) { gam += bflo(mlh[k * 1536 + mcol]); sbm += bflo(mlh[k * 1536 + 512 + mcol]); cb1 += bflo(mlh[k * 1536 + 1024 + mcol]); }
      sbm = rbf(sbm); cb1 = rbf(1.f + cb1);
    }
    LAS u16* cs16 = (LAS u16*)(SM + 10240);
    LAS float* gp64 = (LAS float*)(SM + 12288);
    cs16[mcol] = __builtin_bit_cast(u16, (__bf16)cb1); cs16[512 + mcol] = __builtin_bit_cast(u16, (__bf16)sbm);
    LGWAIT0();
    bf16x8 auxf[2];
    {
      const bf16x8 ones = {0x3F80, 0x3F80, 0x3F80, 0x3F80, 0x3F80, 0x3F80, 0x3F80, 0x3F80};
#pragma unroll
      for (int i = 0; i < 2; i++) { const bf16x8 v = *(const LAS bf16x8*)(cs16 + ((n - 1) & 1) * 512 + wave * 64 + i * 32 + q * 8); auxf[i] = n == 0 ? ones : v; }
      f32x4 k0 = MFMA(auxf[0], w1f[0], ((f32x4){0, 0, 0, 0})), k1 = MFMA(auxf[0], w1f[1], ((f32x4){0, 0, 0, 0})), kg = MFMA(auxf[0], auxf[0], ((f32x4){0, 0, 0, 0}));
      k0 = MFMA(auxf[1], w1f[2], k0); k1 = MFMA(auxf[1], w1f[3], k1); kg = MFMA(auxf[1], auxf[1], kg);
      if (lane < 16) {
        LAS float* g = (LAS float*)(L + 32768) + wave * 192;
        g[n] = k0[0]; g[16 + n] = k1[0]; g[32 + n] = kg[0]; g[64 + n] = k0[1]; g[80 + n] = k1[1]; g[96 + n] = kg[1]; g[128 + n] = k0[2]; g[144 + n] = k1[2]; g[160 + n] = kg[2];
      }
    }
    bar();
    float Ow = 0.f, Cw = 0.f, Sw = 0.f, Kc = 0.f, Ks = 0.f, Kc2 = 0.f, Kcs = 0.f, Ks2 = 0.f;
    if (wave == 0) {
      const LAS float* g = (const LAS float*)(L + 32768);
      float o = 0.f, c = 0.f, sg = 0.f;
#pragma unroll
      for (int w = 0; w < 8; w++) { o += g[w * 192 + (lane < 48 ? lane : 0)]; c += g[w * 192 + 64 + (lane < 48 ? lane : 0)]; sg += g[w * 192 + 128 + (lane < 48 ? lane : 0)]; }
      Ow = o; Cw = c; Sw = sg;
      Kc = RL(o, 33); Ks = RL(o, 34); Kc2 = RL(c, 33); Kcs = RL(c, 34); Ks2 = RL(sg, 34);
    }
    asm volatile("" : "+v"(gam), "+v"(cb1), "+v"(Ow), "+v"(Cw), "+v"(Sw));
    {
      v4u v; int spins = 0;
      const int off = S5_OFF + xc * 8192 + (lane >> 3) * 1024 + wave * 128 + (lane & 7) * 16;
      for (;;) { CBAR(); v = ld16(rs, off); if (__all(mx4(v) != SENT) || ++spins > SPIN_CAP) break; if ((spins & 15) == 0) REPUB(); }
      const float b0 = red32(bflo(v.x), bflo(v.z)), b1x = red32(bfhi(v.x), bfhi(v.z)), b2x = red32(bflo(v.y), bflo(v.w)), b3 = red32(bfhi(v.y), bfhi(v.w));
      const float c0 = red16(b0, b2x), c1 = red16(b1x, b3);
      const bool up = lane & 8;
      const float keep = up ? c1 : c0, send = up ? c0 : c1;
      const float ao = keep + dppf<0x128>(send);
      const __bf16 xb = (__bf16)(xme + gam * ao);
      cs16[mcol] = __builtin_bit_cast(u16, (__bf16)((float)xb * cb1)); cs16[512 + mcol] = __builtin_bit_cast(u16, xb);
      LGWAIT0();
      const bf16x8 y0 = *(const LAS bf16x8*)(cs16 + (n & 1) * 512 + wave * 64 + q * 8), y1 = *(const LAS bf16x8*)(cs16 + (n & 1) * 512 + wave * 64 + 32 + q * 8);
      f32x4 z0 = MFMA(y0, w1f[0], ((f32x4){0, 0, 0, 0})), z1 = MFMA(y0, w1f[1], ((f32x4){0, 0, 0, 0})), ax = MFMA(y0, auxf[0], ((f32x4){0, 0, 0, 0})), gr = MFMA(y0, y0, ((f32x4){0, 0, 0, 0}));
      z0 = MFMA(y1, w1f[2], z0); z1 = MFMA(y1, w1f[3], z1); ax = MFMA(y1, auxf[1], ax); gr = MFMA(y1, y1, gr);
      const float lastq = n == 0 ? ax[1] : n == 1 ? gr[1] : gr[0];
      gp64[wave * 64 + lane] = q == 0 ? z0[0] : q == 1 ? z1[0] : q == 2 ? ax[0] : lastq;
    }
    bar();
    if (wave == 0) {
      float a = 0;
#pragma unroll
      for (int w = 0; w < 8; w++) a += gp64[w * 64 + lane];
      const float B1 = RL(a, 32), B3 = RL(a, 33), B4 = RL(a, 34), A1 = RL(a, 48), A2 = RL(a, 49), B2 = RL(a, 50);
      const float mu = A1 * (1.f / 512.f), rr = __builtin_amdgcn_rsqf(fmaxf(A2 * (1.f / 512.f) - mu * mu, 0.f) + 1e-5f);
      const float m2 = (rr * (B1 - mu * Kc) + Ks) * (1.f / 512.f);
      const float e2 = (rr * rr * (B2 - 2.f * mu * B3 + mu * mu * Kc2) + 2.f * rr * (B4 - mu * Kcs) + Ks2) * (1.f / 512.f);
      const float r2 = __builtin_amdgcn_rsqf(fmaxf(e2 - m2 * m2, 0.f) + 1e-5f);
      const float zz = r2 * (rr * (a - mu * Cw) + Sw - m2 * Ow);
      const float z = zz + b1v;
      const float u = z * (0.7978845608028654f + 0.035677408136300125f * z * z);
      const float gl = z * __builtin_amdgcn_rcpf(1.f + __builtin_amdgcn_exp2f(-2.885390081777927f * u));
      const unsigned d = canon(pk2(gl, dppf<0x101>(gl)));
      if (lane < 32 && !(lane & 1)) ((LAS unsigned*)y16)[lane >> 1] = d;
      LGWAIT0();
      const v4u o = *(const LAS v4u*)((LAS unsigned*)y16 + (lane & 3) * 4);
      if (lane < 32) st16(rs, S6_OFF + (lane >> 2) * 6144 + gi * 128 + (lane & 3) * 16, o);
    }
    if (ti == 0 && wave == 1 && lane < 8) { v4u o = {1, 1, 1, 1}; st16(rs, DN_OFF + lane * 128, o); }
  } else {
    const int e = r - 16, split = 4 * xc + (e & 3), hg = e >> 2;
    const int sfoff = SF_OFF + (8 + xc) * 8192 + lane * 128;
    LAS unsigned char* qa = SM;
    LAS unsigned char* Pm = SM + 16896;
    LAS float* wmax = (LAS float*)(SM + 21504);
    LAS float* wsum = wmax + 128;
    unsigned snapf = 0;
    if (wave == 0) snapf = ld8(rs, sfoff).y;
    bar();
    CBAR();
#pragma unroll
    for (int i = 0; i < 16; i++) {
      const int key = ((i * 8 + wave) + hg * 64) & 127;
      dma16(ckv + (size_t)(split * 128 + key) * 512 + ((lane ^ swz(key)) * 8), L + key * 1024);
    }
    VMWAIT0();
    bar();
    bf16x8 ka[16], va[16];
    {
      const int key = wave * 16 + n, f2 = swz(key);
#pragma unroll
      for (int s2 = 0; s2 < 16; s2++) ka[s2] = *(const LAS bf16x8*)(L + key * 1024 + (((4 * s2 + q) ^ f2) * 16));
#pragma unroll
      for (int i = 0; i < 16; i++) va[i] = trfrag<1024, true>(L, 32 * (i & 3), wave * 8 + 2 * (i >> 2), lane);
    }
    if (wave == 0) {
      int spins = 0; bool ok = false;
      for (;;) { CBAR(); if (!ok) ok = ld8(rs, sfoff).y != snapf; if (__all(ok) || ++spins > SPIN_CAP) break; __builtin_amdgcn_s_sleep(1); }
    }
    bar();
    {
      const int off = S2_OFF + xc * 32768 + hg * 16384 + t * 16;
      v4u v0, v1; int spins = 0;
      for (;;) {
        CBAR();
        v0 = ld16(rs, off); v1 = ld16(rs, off + 8192);
        if (__all(max(mx4(v0), mx4(v1)) != SENT) || ++spins > SPIN_CAP) break;
      }
      *(LAS v4u*)(qa + wave * 1056 + lane * 16) = v0;
      *(LAS v4u*)(qa + (8 + wave) * 1056 + lane * 16) = v1;
    }
    bar();
    f32x4 sc = {0, 0, 0, 0};
    {
      bf16x8 qf[16];
#pragma unroll
      for (int s2 = 0; s2 < 16; s2++) qf[s2] = *(const LAS bf16x8*)(qa + n * 1056 + s2 * 64 + q * 16);
      SB();
      f32x4 sc2 = {0, 0, 0, 0};
#pragma unroll
      for (int s2 = 0; s2 < 8; s2++) { sc = MFMA(ka[2 * s2], qf[2 * s2], sc); sc2 = MFMA(ka[2 * s2 + 1], qf[2 * s2 + 1], sc2); }
      sc += sc2;
    }
    float mx = fmaxf(fmaxf(sc[0], sc[1]), fmaxf(sc[2], sc[3]));
    mx = max16(mx); mx = max32(mx);
    if (lane < 16) wmax[lane * 8 + wave] = mx;
    bar();
    {
      f32x4 m0 = *(LAS f32x4*)(wmax + n * 8), m1 = *(LAS f32x4*)(wmax + n * 8 + 4);
      mx = fmaxf(fmaxf(fmaxf(m0[0], m0[1]), fmaxf(m0[2], m0[3])), fmaxf(fmaxf(m1[0], m1[1]), fmaxf(m1[2], m1[3])));
    }
    const float p0 = __builtin_amdgcn_exp2f(sc[0] - mx), p1 = __builtin_amdgcn_exp2f(sc[1] - mx), p2 = __builtin_amdgcn_exp2f(sc[2] - mx), p3 = __builtin_amdgcn_exp2f(sc[3] - mx);
    { v2u o = {pk2(p0, p1), pk2(p2, p3)}; *(LAS v2u*)(Pm + n * 288 + (wave * 16 + q * 4) * 2) = o; }
    float ps = (p0 + p1) + (p2 + p3);
    ps = red16(ps, ps); ps = red32(ps, ps);
    if (lane < 16) wsum[lane * 8 + wave] = ps;
    bar();
    {
      if (wave == 7) {
        f32x4 s0 = *(LAS f32x4*)(wsum + n * 8), s1 = *(LAS f32x4*)(wsum + n * 8 + 4);
        const float l = ((s0[0] + s0[1]) + (s0[2] + s0[3])) + ((s1[0] + s1[1]) + (s1[2] + s1[3]));
        v2u o2 = {canon(__float_as_uint(mx)), canon(__float_as_uint(l))};
        if (lane < 16) st8(rs, S3_OFF + ((hg * 16 + lane) * 32 + split) * S3R + 1024, o2);
      }
      bf16x8 bp[4];
#pragma unroll
      for (int ks = 0; ks < 4; ks++) bp[ks] = *(const LAS bf16x8*)(Pm + n * 288 + ks * 64 + q * 16);
      SB();
      f32x4 acc[4];
#pragma unroll
      for (int T = 0; T < 4; T++) {
        acc[T] = (f32x4){0, 0, 0, 0};
#pragma unroll
        for (int ks = 0; ks < 4; ks++) acc[T] = MFMA(va[T * 4 + ks], bp[ks], acc[T]);
      }
      unsigned d0[4], d1[4];
#pragma unroll
      for (int T = 0; T < 4; T++) { d0[T] = canon(pk2(acc[T][0], acc[T][1])); d1[T] = canon(pk2(acc[T][2], acc[T][3])); }
      auto r0 = __builtin_amdgcn_permlane16_swap(d0[0], d0[1], false, false);
      auto r1 = __builtin_amdgcn_permlane16_swap(d1[0], d1[1], false, false);
      auto r2 = __builtin_amdgcn_permlane16_swap(d0[2], d0[3], false, false);
      auto r3 = __builtin_amdgcn_permlane16_swap(d1[2], d1[3], false, false);
      v4u oa = {r0[0], r1[0], r0[1], r1[1]}, ob = {r2[0], r3[0], r2[1], r3[1]};
      const v4u obs = {dppu<0x128>(ob.x), dppu<0x128>(ob.y), dppu<0x128>(ob.z), dppu<0x128>(ob.w)};
      const bool lo = n < 8;
      const int o3b = S3_OFF + ((hg * 16 + (n & 7)) * 32 + split) * S3R + (wave * 64 + (q & 1) * 16 + (q & 2) * 4) * 2;
      st16(rs, o3b + (lo ? 0 : 64), lo ? oa : obs);
      st16(rs, o3b + 8 * 32 * S3R + (lo ? 64 : 0), lo ? obs : oa);
    }
  }
}

extern "C" void kernel_launch(void* const* d_in, const int* in_sizes, int n_in, void* d_out, int out_size, void* d_ws, size_t ws_size, hipStream_t stream) {
  (void)in_sizes; (void)n_in; (void)out_size; (void)ws_size;
  (void)hipFuncSetAttribute((const void*)fused_kernel, hipFuncAttributeMaxDynamicSharedMemorySize, L_TOTAL);
  fused_kernel<<<256, NT, L_TOTAL, stream>>>((const u16*)d_in[0], (const u16*)d_in[1], (const u16*)d_in[2], (const u16*)d_in[3], (const u16*)d_in[4], (const u16*)d_in[5],
                                             (const u16*)d_in[7], (const u16*)d_in[8], (const u16*)d_in[9], (const u16*)d_in[10], (const u16*)d_in[11], (const u16*)d_in[12],
                                             (u16*)d_out, (unsigned char*)d_ws);
}
```

```cpp
#include <hip/hip_runtime.h>
#include <stdint.h>

typedef unsigned short u16;
typedef unsigned v4u __attribute__((ext_vector_type(4)));
typedef unsigned v2u __attribute__((ext_vector_type(2)));
typedef float f32x4 __attribute__((ext_vector_type(4)));
typedef float f32x2 __attribute__((ext_vector_type(2)));
typedef short bf16x8 __attribute__((ext_vector_type(8)));
typedef short s16x4 __attribute__((ext_vector_type(4)));
typedef __bf16 bf2 __attribute__((ext_vector_type(2)));
#define LAS __attribute__((address_space(3)))

constexpr unsigned SENT = 0xFFFFFFFFu;
constexpr int NT = 512;
constexpr int SPIN_CAP = 1 << 20;
#define NREP 2
#define EGL_DELAY 450
#define AD_DELAY 130
#define ROFF 24
#define CLRAUX 16
#define NTAUX 2
#define NTS " nt"
#define LDNT(T, p) __builtin_nontemporal_load((const T*)(p))
constexpr int SU_OFF = 0;
constexpr int SF_OFF = 131072;
constexpr int SQ_OFF = 262144;
constexpr int CLR_OFF = 327680;
constexpr int S2_OFF = CLR_OFF;
constexpr int S5_OFF = S2_OFF + 8 * 32768;
constexpr int S6_OFF = S5_OFF + 65536;
constexpr int SL_OFF = S6_OFF + 49152;
constexpr int SLF_OFF = SL_OFF + 32768;
constexpr int DN_OFF = SLF_OFF + 32768;
constexpr int EGC_END = DN_OFF + 4096;
constexpr int EGC_PIECES = (EGC_END - S2_OFF) / 16 / 64;
static_assert(EGC_PIECES <= 448 && EGC_PIECES * 1024 == EGC_END - S2_OFF, "clear share");
constexpr int S4_OFF = EGC_END;
constexpr int S3R = 1152;
constexpr int S3_OFF = S4_OFF + 16384;
constexpr int CLR_END = S3_OFF + 1024 * S3R;
constexpr int DUMMY_OFF = CLR_END;
constexpr int WS_USED = CLR_END + 65536;
constexpr int L_SM = 131072;
constexpr int L_TOTAL = 131072 + 30720;

__device__ __forceinline__ float bflo(unsigned w) { return __uint_as_float(w << 16); }
__device__ __forceinline__ float bfhi(unsigned w) { return __uint_as_float(w & 0xffff0000u); }
__device__ __forceinline__ unsigned pk2(float a, float b) { bf2 p; p.x = (__bf16)a; p.y = (__bf16)b; return __builtin_bit_cast(unsigned, p); }
__device__ __forceinline__ float rbf(float a) { return (float)(__bf16)a; }
__device__ __forceinline__ unsigned canon(unsigned w) { return w == SENT ? 0x7FC07FC0u : w; }
__device__ __forceinline__ float dot2(unsigned a, unsigned b, float c) { return __builtin_amdgcn_fdot2_f32_bf16(__builtin_bit_cast(bf2, a), __builtin_bit_cast(bf2, b), c, false); }
__device__ __forceinline__ void bar() { __builtin_amdgcn_sched_barrier(0); asm volatile("s_waitcnt lgkmcnt(0)\n\ts_barrier" ::: "memory"); __builtin_amdgcn_sched_barrier(0); }
template <int CTRL> __device__ __forceinline__ float dppf(float v) { return __int_as_float(__builtin_amdgcn_update_dpp(0, __float_as_int(v), CTRL, 0xf, 0xf, false)); }
template <int CTRL> __device__ __forceinline__ unsigned dppu(unsigned v) { return (unsigned)__builtin_amdgcn_update_dpp(0, (int)v, CTRL, 0xf, 0xf, false); }
__device__ __forceinline__ float red32(float a, float b) {
  auto r = __builtin_amdgcn_permlane32_swap(__float_as_uint(a), __float_as_uint(b), false, false);
  return __uint_as_float(r[0]) + __uint_as_float(r[1]);
}
__device__ __forceinline__ float red16(float a, float b) {
  auto r = __builtin_amdgcn_permlane16_swap(__float_as_uint(a), __float_as_uint(b), false, false);
  return __uint_as_float(r[0]) + __uint_as_float(r[1]);
}
__device__ __forceinline__ float max32(float a) { auto r = __builtin_amdgcn_permlane32_swap(__float_as_uint(a), __float_as_uint(a), false, false); return fmaxf(__uint_as_float(r[0]), __uint_as_float(r[1])); }
__device__ __forceinline__ float max16(float a) { auto r = __builtin_amdgcn_permlane16_swap(__float_as_uint(a), __float_as_uint(a), false, false); return fmaxf(__uint_as_float(r[0]), __uint_as_float(r[1])); }
__device__ __forceinline__ float wave_sum(float v) {
  v += dppf<0x128>(v); v += dppf<0x124>(v); v += dppf<0x122>(v); v += dppf<0x121>(v);
  v = red16(v, v); v = red32(v, v); return v;
}
__device__ __forceinline__ float wave_max(float v) {
  v = fmaxf(v, dppf<0x128>(v)); v = fmaxf(v, dppf<0x124>(v)); v = fmaxf(v, dppf<0x122>(v)); v = fmaxf(v, dppf<0x121>(v));
  v = max16(v); v = max32(v); return v;
}
__device__ __forceinline__ unsigned ilo(unsigned a, unsigned b) { return __builtin_amdgcn_perm(b, a, 0x05040100u); }
__device__ __forceinline__ unsigned ihi(unsigned a, unsigned b) { return __builtin_amdgcn_perm(b, a, 0x07060302u); }
__device__ __forceinline__ void interleave(v4u& a, v4u& b) {
  v4u x, y;
  x.x = ilo(a.x, b.x); x.y = ihi(a.x, b.x); x.z = ilo(a.y, b.y); x.w = ihi(a.y, b.y);
  y.x = ilo(a.z, b.z); y.y = ihi(a.z, b.z); y.z = ilo(a.w, b.w); y.w = ihi(a.w, b.w);
  a = x; b = y;
}
typedef __amdgpu_buffer_rsrc_t rsrc_t;
__device__ __forceinline__ v4u ld16(rsrc_t r, int off) { return __builtin_amdgcn_raw_buffer_load_b128(r, off, 0, 16); }
__device__ __forceinline__ v2u ld8(rsrc_t r, int off) { return __builtin_amdgcn_raw_buffer_load_b64(r, off, 0, 16); }
__device__ __forceinline__ void st16(rsrc_t r, int off, v4u v) { __builtin_amdgcn_raw_buffer_store_b128(v, r, off, 0, 16); }
__device__ __forceinline__ void st8(rsrc_t r, int off, v2u v) { __builtin_amdgcn_raw_buffer_store_b64(v, r, off, 0, 16); }
__device__ __forceinline__ void glds16(const void* g, unsigned lds_base) {
  unsigned sv; asm volatile("s_mov_b32 %0, m0\n\ts_mov_b32 m0, %2\n\ts_nop 0\n\tglobal_load_lds_dwordx4 %1, off" NTS "\n\ts_mov_b32 m0, %0" : "=&s"(sv) : "v"(g), "s"(lds_base) : "memory");
}
#define RL(v, l) __int_as_float(__builtin_amdgcn_readlane(__float_as_int(v), (l)))
#define SB() __builtin_amdgcn_sched_barrier(0)
#define CBAR() do { asm volatile("" ::: "memory"); __builtin_amdgcn_sched_barrier(0); } while (0)

__device__ __forceinline__ int swz(int row) { return ((row & 3) | ((row >> 1) & 4)) << 1; }
template <int AUX> __device__ __forceinline__ void dma16a(const void* g, LAS unsigned char* l) { __builtin_amdgcn_global_load_lds((const __attribute__((address_space(1))) unsigned*)g, (LAS unsigned*)l, 16, 0, AUX); }
#define dma16 dma16a<NTAUX>
#define NARROW 2
#define dma16n dma16a<NARROW>
#define LDNAR(T, p) __builtin_nontemporal_load((const T*)(p))
template <int RB, bool SW>
__device__ __forceinline__ bf16x8 trfrag(const LAS unsigned char* img, int k0, int ch0, int lane) {
  const int g = lane >> 4, qq = (lane & 15) >> 2, pp = lane & 3;
  s16x4 h[2];
#pragma unroll
  for (int tt = 0; tt < 2; tt++) {
    const int row = k0 + 8 * g + 4 * tt + qq;
    const int ch = SW ? ((ch0 + (pp >> 1)) ^ swz(row)) : (ch0 + (pp >> 1));
    h[tt] = __builtin_amdgcn_ds_read_tr16_b64_v4i16((LAS s16x4*)(img + row * RB + ch * 16 + (pp & 1) * 8));
  }
  return (bf16x8){h[0].x, h[0].y, h[0].z, h[0].w, h[1].x, h[1].y, h[1].z, h[1].w};
}
#define MFMA(a, b, c) __builtin_amdgcn_mfma_f32_16x16x32_bf16(a, b, c, 0, 0, 0)
#define VMWAIT0() do { asm volatile("" ::: "memory"); __builtin_amdgcn_s_waitcnt(0x0F70); asm volatile("s_waitcnt vmcnt(0)" ::: "memory"); } while (0)
#define LGWAIT0() asm volatile("s_waitcnt lgkmcnt(0)" ::: "memory")
typedef unsigned long long u64;
__device__ __forceinline__ unsigned mx4(v4u v) { return max(max(v.x, v.y), max(v.z, v.w)); }
__device__ __forceinline__ void stclr(rsrc_t r, int off, v4u v) { __builtin_amdgcn_raw_buffer_store_b128(v, r, off, 0, CLRAUX); }

__global__ void __launch_bounds__(NT, 2) fused_kernel(
    const u16* __restrict__ x, const u16* __restrict__ ckv, const u16* __restrict__ w_q, const u16* __restrict__ w_uk,
    const u16* __restrict__ w_uv, const u16* __restrict__ w_o, const u16* __restrict__ w1, const u16* __restrict__ b1,
    const u16* __restrict__ w2, const u16* __restrict__ b2, const u16* __restrict__ cvec, const u16* __restrict__ w_mod,
    u16* __restrict__ out, unsigned char* ws) {
  extern __shared__ __attribute__((aligned(16))) unsigned char lds_raw[];
  LAS unsigned char* L = (LAS unsigned char*)lds_raw;
  const int t = threadIdx.x, lane = t & 63, wave = __builtin_amdgcn_readfirstlane(t >> 6);
  const int b = blockIdx.x, xc = b & 7, r = ((b >> 3) + ROFF) & 31;
  const int n = lane & 15, q = lane >> 4;
  const rsrc_t rs = __builtin_amdgcn_make_buffer_rsrc((void*)ws, 0, WS_USED, 0x00020000);
  LAS unsigned char* SM = L + L_SM;
  const v4u sv4 = {SENT, SENT, SENT, SENT};
  const u64 tst = __builtin_amdgcn_s_memrealtime();
  (void)tst;
#define DELAY(x) do { while (__builtin_amdgcn_s_memrealtime() - tst < (x)) __builtin_amdgcn_s_sleep(2); } while (0)

  if (r < 16) {
    const int head = 4 * xc + (r >> 2), cs = r & 3;
    LAS unsigned char* hp = SM;
    LAS unsigned* q16 = (LAS unsigned*)(SM + 2304);
    LAS unsigned char* qst = SM + 8192;
    LAS unsigned char* WKI = L + 98304;
    LAS unsigned char* WQI = L + 65536;
    const int sfoff = SF_OFF + xc * 8192 + lane * 128;
    LAS unsigned* lcnt = (LAS unsigned*)(SM + 9472);
    const unsigned short cvl0 = cvec[(r & 7) * 64 + (t >> 4)], cvl1 = cvec[(r & 7) * 64 + 32 + (t >> 4)];
    bf16x8 wk[4];
    if (wave < 4) {
      const int suoff = SU_OFF + xc * 16384 + cs * 4096 + t * 16;
      const int sqoff = SQ_OFF + head * 2048 + (t & 127) * 16;
      const v4u snapu = ld16(rs, suoff);
      const v4u snapq = ld16(rs, sqoff);
      const unsigned snapf = ld8(rs, sfoff).y;
      if (t == 0) *lcnt = 0u;
      bar();
      v4u v = snapu; int spins = 0; bool ok = false;
      for (;;) {
        CBAR();
        if (!ok) v = ld16(rs, suoff);
        ok = (v.y != snapu.y) & (v.w != snapu.w);
        if (__all(ok) || ++spins > SPIN_CAP) break;
      }
      v2u hh = {v.x, v.z};
      *(LAS v2u*)(hp + ((t >> 4) & 7) * 272 + (t >> 7) * 128 + (t & 15) * 8) = hh;
      bar();
#pragma unroll
      for (int i = 0; i < 4; i++) wk[i] = *(const LAS bf16x8*)(WKI + (wave * 16 + n) * 256 + (((4 * i + q) ^ swz(n)) * 16));
      if (wave < 2) {
        spins = 0; ok = false;
        for (;;) {
          CBAR();
          if (!ok) v = ld16(rs, sqoff);
          ok = (v.y != snapq.y) & (v.w != snapq.w);
          if (__all(ok) || ++spins > SPIN_CAP) break;
        }
        v2u o2 = {v.x, v.z};
        *(LAS v2u*)(qst + (t >> 5) * 272 + (t & 31) * 8) = o2;
      } else if (wave == 2) {
        spins = 0; ok = false;
        for (;;) { CBAR(); if (!ok) ok = ld8(rs, sfoff).y != snapf; if (__all(ok) || ++spins > SPIN_CAP) break; }
      }
      bar();
    } else {
      const int lw = wave - 4;
      const int sqo = SQ_OFF + head * 2048 + cs * 512 + lw * 128 + ((lane >> 2) & 7) * 16;
      const v4u oldq = ld16(rs, sqo);
      bar();
#pragma unroll
      for (int i = 0; i < 16; i++) {
        const int j = 4 * i + lw, rg = j & 31, row = 4 * rg + q;
        const u16* src = (i < 8 ? w_q : w_uk) + (size_t)(cs * 128 + row) * 4096 + head * 128 + ((n ^ swz(row)) * 8);
        dma16(src, L + 65536 + j * 1024);
      }
      VMWAIT0();
      if (lane == 0) __hip_atomic_fetch_add(lcnt, 1u, __ATOMIC_RELAXED, __HIP_MEMORY_SCOPE_WORKGROUP);
      { int spins = 0; while (__hip_atomic_load(lcnt, __ATOMIC_RELAXED, __HIP_MEMORY_SCOPE_WORKGROUP) < 4u && ++spins < SPIN_CAP) {} }
      CBAR();
      bf16x8 wqf[8];
#pragma unroll
      for (int i = 0; i < 8; i++) wqf[i] = trfrag<256, true>(WQI, 32 * (i & 3), 4 * lw + 2 * (i >> 2), lane);
#pragma unroll
      for (int i = 0; i < 4; i++) wk[i] = *(const LAS bf16x8*)(WKI + (wave * 16 + n) * 256 + (((4 * i + q) ^ swz(n)) * 16));
      bar();
      f32x4 a0 = {0, 0, 0, 0}, a1 = {0, 0, 0, 0}; bf16x8 hf[4];
#pragma unroll
      for (int i = 0; i < 4; i++) hf[i] = *(const LAS bf16x8*)(hp + (n & 7) * 272 + i * 64 + q * 16);
      SB();
#pragma unroll
      for (int i = 0; i < 4; i++) { a0 = MFMA(hf[i], wqf[i], a0); a1 = MFMA(hf[i], wqf[4 + i], a1); }
      const float sq = red16((a0[0] + a0[1]) + (a0[2] + a0[3]), (a1[0] + a1[1]) + (a1[2] + a1[3]));
      const unsigned d = pk2(sq, dppf<0x101>(sq));
      const unsigned d2 = dppu<0x102>(d);
      if (lane < 32 && (lane & 3) == 0) { v4u o = {d, oldq.y + 1u, d2, oldq.w + 1u}; st16(rs, sqo, o); }
      {
        const int cb = S3_OFF + (head * 32 + cs * 8) * S3R + (t - 256) * 16;
        stclr(rs, cb, sv4); stclr(rs, cb + 4096, sv4);
        if (wave == 4) stclr(rs, cb + 8192, sv4);
        else if (wave == 5 && lane < 8) stclr(rs, S4_OFF + head * 512 + cs * 128 + lane * 16, sv4);
      }
      VMWAIT0();
      bar();
      if (lane < 32 && (lane & 3) == 0) { v4u o = {d, oldq.y + 2u, d2, oldq.w + 2u}; st16(rs, sqo, o); }
    }
    {
      f32x4 a = {0, 0, 0, 0}; bf16x8 qf[4];
#pragma unroll
      for (int i = 0; i < 4; i++) qf[i] = *(const LAS bf16x8*)(qst + (n & 3) * 272 + i * 64 + q * 16);
      SB();
      f32x4 ab = {0, 0, 0, 0};
      a = MFMA(wk[0], qf[0], a); ab = MFMA(wk[1], qf[1], ab); a = MFMA(wk[2], qf[2], a); ab = MFMA(wk[3], qf[3], ab);
      a += ab;
#pragma unroll
      for (int i = 0; i < 4; i++) { a[i] += dppf<0xB1>(a[i]); a[i] += dppf<0x4E>(a[i]); }
      const float SC = 0.08838834764831845f * 1.4426950408889634f;
      v2u o = {canon(pk2(a[0] * SC, a[1] * SC)), canon(pk2(a[2] * SC, a[3] * SC))};
      if (n == 0) *(LAS v2u*)(q16 + 64 + wave * 8 + q * 2) = o;
    }
    bar();
    if (wave < 2) { const v4u o = *(const LAS v4u*)(q16 + 64 + n * 4); st16(rs, S2_OFF + (wave * 4 + q) * 32768 + head * 1024 + cs * 256 + n * 16, o); }
    bar();
    {
      const int kj = r & 7, cg = xc * 2 + (r >> 3);
      const u16* pl = w_mod + (size_t)(kj * 64 + (t >> 4)) * 3072 + 1024 + cg * 128 + n * 8;
      v4u w0 = LDNT(v4u, pl), w1r = LDNT(v4u, pl + (size_t)32 * 3072);
      CBAR();
      if (r < 8) {
#pragma unroll
        for (int i = 0; i < 8; i++) {
          const int rg = 8 * i + wave, row = 8 * rg + (lane >> 3);
          glds16(w_o + (size_t)(xc * 512 + row) * 512 + r * 64 + (((lane & 7) ^ ((row & 2) | ((row >> 1) & 4))) * 8), (unsigned)(rg * 1024));
        }
        asm volatile("s_waitcnt vmcnt(8)" ::: "memory");
      }
      const float c0 = bflo(cvl0), c1 = bflo(cvl1);
      const unsigned s01 = pk2(c0 * __builtin_amdgcn_rcpf(1.f + __expf(-c0)), c1 * __builtin_amdgcn_rcpf(1.f + __expf(-c1)));
      interleave(w0, w1r);
      float a[8] = {dot2(w0.x, s01, 0.f), dot2(w0.y, s01, 0.f), dot2(w0.z, s01, 0.f), dot2(w0.w, s01, 0.f),
                    dot2(w1r.x, s01, 0.f), dot2(w1r.y, s01, 0.f), dot2(w1r.z, s01, 0.f), dot2(w1r.w, s01, 0.f)};
      float b0 = red32(a[0], a[4]), b1x = red32(a[1], a[5]), b2x = red32(a[2], a[6]), b3 = red32(a[3], a[7]);
      f32x2 o = {red16(b0, b2x), red16(b1x, b3)};
      LAS float* apart = (LAS float*)(SM + 12288);
      *(LAS f32x2*)(apart + wave * 128 + n * 8 + 4 * (lane >> 5) + 2 * ((lane >> 4) & 1)) = o;
      bar();
      if (wave == 7) {
        f32x2 sacc = {0.f, 0.f};
#pragma unroll
        for (int w = 0; w < 8; w++) sacc += *(LAS f32x2*)(apart + w * 128 + lane * 2);
        const unsigned d = canon(pk2(sacc.x, sacc.y));
        v4u o4 = {dppu<0x00>(d), dppu<0x55>(d), dppu<0xAA>(d), dppu<0xFF>(d)};
        if ((lane & 3) == 0) st16(rs, SL_OFF + kj * 4096 + cg * 256 + lane * 4, o4);
        if (cg < 4 || cg >= 12) {
          const unsigned f0 = canon(__float_as_uint(sacc.x)), f1 = canon(__float_as_uint(sacc.y));
          v4u of = {f0, f1, dppu<0x101>(f0), dppu<0x101>(f1)};
          if (!(lane & 1)) st16(rs, SLF_OFF + kj * 4096 + (cg < 4 ? cg : cg - 8) * 512 + lane * 8, of);
        }
      }
      bar();
    }
    if (r < 8) {
      LAS float* fpart = (LAS float*)(SM + 3072);
      LAS unsigned char* cst = SM + 4096;
      LAS float* fp64 = (LAS float*)(SM + 16384);
      VMWAIT0();
      bar();
      bf16x8 wof[8];
      {
        const int g = lane >> 4, qq = (lane & 15) >> 2, pp = lane & 3;
#pragma unroll
        for (int i = 0; i < 8; i++) {
          s16x4 h[2];
#pragma unroll
          for (int tt = 0; tt < 2; tt++) {
            const int row = 32 * (2 * wave + (i >> 2)) + 8 * g + 4 * tt + qq;
            const int ch = (2 * (i & 3) + (pp >> 1)) ^ ((row & 2) | ((row >> 1) & 4));
            h[tt] = __builtin_amdgcn_ds_read_tr16_b64_v4i16((LAS s16x4*)(L + row * 128 + ch * 16 + (pp & 1) * 8));
          }
          wof[i] = (bf16x8){h[0].x, h[0].y, h[0].z, h[0].w, h[1].x, h[1].y, h[1].z, h[1].w};
        }
      }
      if (wave >= 4) {
        int spins = 0;
        for (;;) { CBAR(); v4u v = ld16(rs, S2_OFF + xc * 32768 + xc * 4096 + (t - 256) * 16); if (__all(mx4(v) != SENT) || ++spins > SPIN_CAP) break; __builtin_amdgcn_s_sleep(2); }
      }
      bar();
      {
        v4u av0, av1; int spins = 0;
        const int off = S4_OFF + ((4 * xc + (wave >> 1)) * 2 + (n & 1)) * 256 + (wave & 1) * 128 + q * 16;
        for (;;) { CBAR(); av0 = ld16(rs, off); av1 = ld16(rs, off + 64); if (__all(max(mx4(av0), mx4(av1)) != SENT) || ++spins > SPIN_CAP) break; }
        f32x4 a[4];
#pragma unroll
        for (int T = 0; T < 4; T++) { a[T] = MFMA(__builtin_bit_cast(bf16x8, av0), wof[T], ((f32x4){0, 0, 0, 0})); a[T] = MFMA(__builtin_bit_cast(bf16x8, av1), wof[4 + T], a[T]); }
        const f32x4 sel = q == 0 ? a[0] : q == 1 ? a[1] : q == 2 ? a[2] : a[3];
        fp64[wave * 64 + lane] = sel[0] + sel[1];
        bar();
        if (wave == 0) {
          float v = 0.f;
#pragma unroll
          for (int w = 0; w < 8; w++) v += fp64[w * 64 + lane];
          const unsigned d = canon(pk2(v, dppf<0x101>(v)));
          if (!(lane & 1)) ((LAS unsigned*)cst)[lane >> 1] = d;
          LGWAIT0();
          const v4u o = *(const LAS v4u*)(cst + (lane & 7) * 16);
          st16(rs, S5_OFF + (lane >> 3) * 8192 + xc * 1024 + r * 128 + (lane & 7) * 16, o);
        }
      }
    } else {
      const int ti = xc * 8 + (r - 8);
      LAS unsigned char* W2I = L;
      const float b2v = bflo(b2[ti * 8 + (lane & 7)]);
      const float x8 = bflo(x[ti * 8 + (lane & 7)]);
      CBAR();
#pragma unroll
      for (int i = 0; i < 3; i++) { const int rg = 8 * i + wave; if (rg < 22) { int row = rg * 64 + lane; row = row < 1376 ? row : 1375; dma16n(w2 + (size_t)row * 512 + ti * 8, W2I + rg * 1024); } }
      CBAR();
      LAS float* hpart = (LAS float*)(SM + 2048);
      LAS unsigned* sbuf = (LAS unsigned*)(SM + 2560);
      VMWAIT0();
      bar();
      bf16x8 w2f[6];
#pragma unroll
      for (int i = 0; i < 6; i++) {
        const int ks = wave + 8 * i < 43 ? wave + 8 * i : 0;
        const int g = lane >> 4, qq = (lane & 15) >> 2, pp = lane & 3;
        s16x4 h0 = __builtin_amdgcn_ds_read_tr16_b64_v4i16((LAS s16x4*)(W2I + (32 * ks + 8 * g + qq) * 16 + (pp & 1) * 8));
        s16x4 h1 = __builtin_amdgcn_ds_read_tr16_b64_v4i16((LAS s16x4*)(W2I + (32 * ks + 8 * g + 4 + qq) * 16 + (pp & 1) * 8));
        w2f[i] = (bf16x8){h0.x, h0.y, h0.z, h0.w, h1.x, h1.y, h1.z, h1.w};
      }
      float xn8 = 0.f, gbv = 0.f;
      if (wave == 0) {
        const int off = lane < 16 ? S5_OFF + xc * 8192 + (lane & 7) * 1024 + ti * 16 : SLF_OFF + ((lane >> 1) & 7) * 4096 + ((lane & 32) ? 2048 : 0) + ti * 32 + (lane & 1) * 16;
        v4u v; int spins = 0;
        for (;;) { CBAR(); v = ld16(rs, off); if (__all(mx4(v) != SENT) || ++spins > SPIN_CAP) break; __builtin_amdgcn_s_sleep(4); }
        if (lane < 48) *(LAS v4u*)(sbuf + lane * 4) = v;
        LGWAIT0();
        const LAS u16* sh = (const LAS u16*)sbuf; const LAS float* sf = (const LAS float*)sbuf;
        float ao = 0, ga8 = 0;
#pragma unroll
        for (int k = 0; k < 8; k++) { ao += bflo(sh[k * 8 + (lane & 7)]); ga8 += sf[64 + k * 8 + (lane & 7)]; gbv += sf[128 + k * 8 + (lane & 7)]; }
        xn8 = x8 + ga8 * ao;
      }
      bar();
      {
        v4u hv[6]; int spins = 0;
        const int off = S6_OFF + xc * 6144 + wave * 128 + q * 16, off5 = wave < 3 ? off + 5120 : off;
        for (;;) {
          CBAR();
#pragma unroll
          for (int i = 0; i < 5; i++) hv[i] = ld16(rs, off + i * 1024);
          hv[5] = ld16(rs, off5);
          const unsigned m = max(max(max(mx4(hv[0]), mx4(hv[1])), max(mx4(hv[2]), mx4(hv[3]))), max(mx4(hv[4]), mx4(hv[5])));
          if (__all(m != SENT) || ++spins > SPIN_CAP) break;
        }
        if (wave >= 3) hv[5] = (v4u){0, 0, 0, 0};
        f32x4 a = {0, 0, 0, 0}, a2 = {0, 0, 0, 0};
#pragma unroll
        for (int i = 0; i < 3; i++) { a = MFMA(__builtin_bit_cast(bf16x8, hv[2 * i]), w2f[2 * i], a); a2 = MFMA(__builtin_bit_cast(bf16x8, hv[2 * i + 1]), w2f[2 * i + 1], a2); }
        if (lane < 8) hpart[wave * 8 + lane] = a[0] + a2[0];
      }
      bar();
      if (wave == 0) {
        float a = hpart[lane];
        a += dppf<0x128>(a); a = red16(a, a); a = red32(a, a);
        const float res = xn8 + gbv * (a + b2v);
        if (lane < 8) out[ti * 8 + lane] = __builtin_bit_cast(u16, (__bf16)res);
      }
    }
  } else if (r >= 24) {
    const int e = r - 24, head = 4 * xc + (e >> 1), half = e & 1, ti = xc * 8 + e;
    const int gi = e * 8 + xc;
    const int wst = (gi < 43 ? gi : 0) * 4;
    const int mcol = wave * 64 + 8 * (lane & 7) + 4 * (lane >> 5) + 2 * ((lane >> 4) & 1) + ((lane >> 3) & 1);
    LAS unsigned char* W1I = L + 65536;
    LAS unsigned char* STG = L + 98304;
    LAS u16* y16 = (LAS u16*)(SM + 5376);
    LAS unsigned* pub = (LAS unsigned*)(SM + 7424);
    LAS unsigned* cnt = (LAS unsigned*)(SM + 7680);
    LAS unsigned* tbase = (LAS unsigned*)(SM + 8192);
    LAS float* apart = (LAS float*)(SM + 10240);
    const int puoff = SU_OFF + q * 16384 + (xc * 8 + e) * 256 + n * 16;
    const int pfoff = SF_OFF + n * 8192 + ti * 128;
    const int sfoff = SF_OFF + (8 + xc) * 8192 + lane * 128;
#define LDSADD(p) __hip_atomic_fetch_add((p), 1u, __ATOMIC_RELAXED, __HIP_MEMORY_SCOPE_WORKGROUP)
#define REPUB() do { v2u p2_ = *(LAS v2u*)(pub + n * 2); v4u tb_ = *(LAS v4u*)(tbase + lane * 8); unsigned tf_ = tbase[lane * 8 + 4]; unsigned k_ = 0; if (lane == 0) k_ = LDSADD(cnt); k_ = __builtin_amdgcn_readfirstlane(k_); \
      v4u o_ = {p2_.x, tb_.x + k_, p2_.y, tb_.y + k_}; st16(rs, puoff, o_); v4u o2_ = {p2_.x, tb_.z + k_, p2_.y, tb_.w + k_}; st16(rs, puoff + 65536, o2_); \
      if (lane < 16) { v2u f_ = {0u, tf_ + k_}; st8(rs, pfoff, f_); } } while (0)
    v2u pd = {0, 0};
    v4u old0 = {0, 0, 0, 0}, old1 = {0, 0, 0, 0}, xv = {0, 0, 0, 0}; unsigned snapf = 0, xw = 0;
    v4u wm[2]; unsigned short cv[2];
    {
      const u16* p = w_mod + (size_t)(e * 64 + (t >> 4)) * 3072 + (n >> 3) * 512 + xc * 64 + (n & 7) * 8;
      wm[0] = LDNT(v4u, p); wm[1] = LDNT(v4u, p + (size_t)32 * 3072);
      cv[0] = cvec[e * 64 + (t >> 4)]; cv[1] = cvec[e * 64 + 32 + (t >> 4)];
    }
    CBAR();
    const unsigned oldf = ld8(rs, pfoff).y;
    if (wave == 0) { old0 = ld16(rs, puoff); old1 = ld16(rs, puoff + 65536); xv = *(const v4u*)(x + lane * 8); xw = ((const unsigned*)x)[xc * 32 + (lane & 31)]; }
    if (wave == 1) snapf = ld8(rs, sfoff).y;
    CBAR();
    {
      const int pc = t >= 64 ? t - 64 : t;
      st16(rs, wave == 0 ? DUMMY_OFF + ti * 1024 + lane * 16 : S2_OFF + ti * (EGC_PIECES * 16) + (pc < EGC_PIECES ? pc : pc - EGC_PIECES) * 16, sv4);
    }
    CBAR();
    float ln0 = 0.f, ln1 = 0.f;
    if (wave == 0) {
      float xs[8] = {bflo(xv.x), bfhi(xv.x), bflo(xv.y), bfhi(xv.y), bflo(xv.z), bfhi(xv.z), bflo(xv.w), bfhi(xv.w)};
      float s = 0;
#pragma unroll
      for (int i = 0; i < 8; i++) s += xs[i];
      const float mean = wave_sum(s) * (1.f / 512.f);
      float vs = 0;
#pragma unroll
      for (int i = 0; i < 8; i++) { float d = xs[i] - mean; vs += d * d; }
      const float rstd = rsqrtf(wave_sum(vs) * (1.f / 512.f) + 1e-5f);
      ln0 = (bflo(xw) - mean) * rstd; ln1 = (bfhi(xw) - mean) * rstd;
      v4u tb = {old0.y, old0.w, old1.y, old1.w};
      *(LAS v4u*)(tbase + lane * 8) = tb; tbase[lane * 8 + 4] = oldf;
      if (lane == 0) { cnt[0] = 2u + NREP; cnt[1] = 0u; }
    }
    CBAR();
    {
      const float c0 = bflo(cv[0]), c1 = bflo(cv[1]);
      const unsigned s01 = pk2(c0 * __builtin_amdgcn_rcpf(1.f + __expf(-c0)), c1 * __builtin_amdgcn_rcpf(1.f + __expf(-c1)));
      interleave(wm[0], wm[1]);
      float a[8] = {dot2(wm[0].x, s01, 0.f), dot2(wm[0].y, s01, 0.f), dot2(wm[0].z, s01, 0.f), dot2(wm[0].w, s01, 0.f),
                    dot2(wm[1].x, s01, 0.f), dot2(wm[1].y, s01, 0.f), dot2(wm[1].z, s01, 0.f), dot2(wm[1].w, s01, 0.f)};
      float b0 = red32(a[0], a[4]), b1x = red32(a[1], a[5]), b2x = red32(a[2], a[6]), b3 = red32(a[3], a[7]);
      f32x2 o = {red16(b0, b2x), red16(b1x, b3)};
      *(LAS f32x2*)(apart + wave * 128 + n * 8 + 4 * (lane >> 5) + 2 * ((lane >> 4) & 1)) = o;
    }
    bar();
    if (wave == 0) {
      f32x2 sacc = {0.f, 0.f};
#pragma unroll
      for (int w = 0; w < 8; w++) sacc += *(LAS f32x2*)(apart + w * 128 + lane * 2);
      auto r0 = __builtin_amdgcn_permlane32_swap(__float_as_uint(sacc.x), __float_as_uint(sacc.x), false, false);
      auto r1 = __builtin_amdgcn_permlane32_swap(__float_as_uint(sacc.y), __float_as_uint(sacc.y), false, false);
      const float one = e == 0 ? 1.f : 0.f;
      const float u0 = ln0 * (one + __uint_as_float(r0[1])) + __uint_as_float(r0[0]), u1 = ln1 * (one + __uint_as_float(r1[1])) + __uint_as_float(r1[0]);
      if (lane < 32) pub[lane] = pk2(u0, u1);
      LGWAIT0();
      pd = *(LAS v2u*)(pub + n * 2);
      v4u o = {pd.x, old0.y + 1u, pd.y, old0.w + 1u}; st16(rs, puoff, o);
      v4u o2 = {pd.x, old1.y + 1u, pd.y, old1.w + 1u}; st16(rs, puoff + 65536, o2);
    }
    unsigned karr = 0;
    if (wave != 0) { VMWAIT0(); if (lane == 0) karr = LDSADD(cnt + 1); }
    const bool lastw = __builtin_amdgcn_readfirstlane(karr) == 6u;
    if (lastw && lane < 16) { v2u f = {0u, oldf + 1u}; st8(rs, pfoff, f); }
#pragma unroll
    for (int i = 0; i < 4; i++) if (gi < 43) { const int rg = 8 * i + wave; dma16n(w1 + (size_t)(rg * 16 + (lane >> 2)) * 1376 + wst * 8 + (lane & 3) * 8, W1I + rg * 1024); }
    CBAR();
    DELAY(EGL_DELAY);
    const float xme = bflo(x[mcol]);
    const float b1v = bflo(b1[wst * 8 + (lane & 31)]);
    CBAR();
#pragma unroll
    for (int i = 0; i < 8; i++) {
      const int rg = 8 * i + wave, row = 4 * rg + q;
      dma16(w_uv + (size_t)(half * 256 + row) * 4096 + head * 128 + ((n ^ swz(row)) * 8), L + rg * 1024);
    }
    CBAR();
    if (wave == 0 || lastw) {
#pragma unroll 1
      for (int k = 0; k < NREP; k++) {
        __builtin_amdgcn_s_sleep(14);
        if (wave == 0) { v4u o_ = {pd.x, old0.y + 2u + k, pd.y, old0.w + 2u + k}; st16(rs, puoff, o_); v4u o2_ = {pd.x, old1.y + 2u + k, pd.y, old1.w + 2u + k}; st16(rs, puoff + 65536, o2_); }
        if (lastw && lane < 16) { v2u f_ = {0u, oldf + 2u + k}; st8(rs, pfoff, f_); }
      }
    }
    VMWAIT0();
    bar();
    bf16x8 wvf[16], w1f[4];
#pragma unroll
    for (int s2 = 0; s2 < 16; s2++) wvf[s2] = trfrag<256, true>(L, 64 * (wave & 3) + 32 * (s2 >> 3), 2 * (s2 & 7), lane);
#pragma unroll
    for (int i = 0; i < 4; i++) w1f[i] = trfrag<64, false>(W1I, 64 * wave + 32 * (i >> 1), 2 * (i & 1), lane);
    if (wave == 1) {
      int spins = 0; bool ok = false;
      for (;;) { CBAR(); if (!ok) ok = ld8(rs, sfoff).y != snapf; if (__all(ok) || ++spins > SPIN_CAP) break; if ((spins & 3) == 0) REPUB(); __builtin_amdgcn_s_sleep(2); }
      spins = 0;
      for (;;) { CBAR(); v4u v = ld16(rs, S2_OFF + xc * 32768 + head * 1024 + lane * 16); if (__all(mx4(v) != SENT) || ++spins > SPIN_CAP) break; if ((spins & 3) == 0) REPUB(); __builtin_amdgcn_s_sleep(2); }
    }
    bar();
    {
      LAS float* sclf = (LAS float*)(SM);
      LAS float* epart = (LAS float*)(STG);
      {
        const int soff = S3_OFF + (head * 32 + (lane & 31)) * S3R + 1024;
        if (wave == 7) {
          v2u stt; int spins = 0;
          for (;;) { CBAR(); stt = ld8(rs, soff); if (__all(max(stt.x, stt.y) != SENT) || ++spins > SPIN_CAP) break; if ((spins & 3) == 0) REPUB(); }
          const float m = __uint_as_float(stt.x), l = __uint_as_float(stt.y);
          const float mg = wave_max(m);
          const float sc = __builtin_amdgcn_exp2f(m - mg);
          const float il = 2.f * __builtin_amdgcn_rcpf(wave_sum(sc * l));
          if (lane < 32) sclf[lane] = sc * il;
        }
      }
      v4u p0, p1; int spins = 0;
      const int off = S3_OFF + (head * 32 + (wave >> 2) * 16 + n) * S3R + half * 512 + (wave & 3) * 128 + q * 16;
      for (;;) {
        CBAR();
        p0 = ld16(rs, off); p1 = ld16(rs, off + 64);
        if (__all(max(mx4(p0), mx4(p1)) != SENT) || ++spins > SPIN_CAP) break;
        if ((spins & 15) == 0) REPUB();
      }
      f32x4 y0[8];
#pragma unroll
      for (int T = 0; T < 8; T++) { y0[T] = MFMA(__builtin_bit_cast(bf16x8, p0), wvf[T], ((f32x4){0, 0, 0, 0})); y0[T] = MFMA(__builtin_bit_cast(bf16x8, p1), wvf[8 + T], y0[T]); }
      bar();
      const f32x4 s4 = *(const LAS f32x4*)(sclf + 16 * (wave >> 2) + 4 * q);
      float z[8];
#pragma unroll
      for (int T = 0; T < 8; T++) z[T] = (y0[T][0] * s4[0] + y0[T][1] * s4[1]) + (y0[T][2] * s4[2] + y0[T][3] * s4[3]);
      const float r0 = red32(z[0], z[4]), r1 = red32(z[1], z[5]), r2 = red32(z[2], z[6]), r3 = red32(z[3], z[7]);
      const float u0 = red16(r0, r2), u1 = red16(r1, r3);
      const int T0 = 4 * (lane >> 5) + 2 * ((lane >> 4) & 1);
      epart[wave * 128 + T0 * 16 + n] = u0; epart[wave * 128 + T0 * 16 + 16 + n] = u1;
      bar();
      if (wave == 0) {
        f32x2 sacc = {0.f, 0.f};
#pragma unroll
        for (int w = 0; w < 8; w++) sacc += *(LAS f32x2*)(epart + w * 128 + lane * 2);
        const unsigned d = canon(pk2(sacc.x, sacc.y));
        v4u o4 = {dppu<0x00>(d), dppu<0x55>(d), dppu<0xAA>(d), dppu<0xFF>(d)};
        if ((lane & 3) == 0) st16(rs, S4_OFF + (head * 2 + half) * 256 + lane * 4, o4);
      }
    }
    if (gi >= 43) {
      if (wave == 0) {
        int spins = 0;
        for (;;) {
          CBAR(); v4u v = ld16(rs, DN_OFF + xc * 128);
          if (v.x != SENT || ++spins > SPIN_CAP) break;
          if ((spins & 3) == 0) REPUB();
          __builtin_amdgcn_s_sleep(8);
        }
      } return;
    }
    {
      v4u v0, v1, v2; int spins = 0;
      const int off = SL_OFF + wave * 4096 + lane * 16;
      for (;;) { CBAR(); v0 = ld16(rs, off); v1 = ld16(rs, off + 1024); v2 = ld16(rs, off + 2048); if (__all(max(max(mx4(v0), mx4(v1)), mx4(v2)) != SENT) || ++spins > SPIN_CAP) break; if ((spins & 15) == 0) REPUB(); __builtin_amdgcn_s_sleep(1); }
      *(LAS v4u*)(L + wave * 3072 + lane * 16) = v0; *(LAS v4u*)(L + wave * 3072 + 1024 + lane * 16) = v1; *(LAS v4u*)(L + wave * 3072 + 2048 + lane * 16) = v2;
    }
    bar();
    float gam = 0.f, sbm = 0.f, cb1 = 0.f;
    {
      const LAS u16* mlh = (const LAS u16*)L;
#pragma unroll
      for (int k = 0; k < 8; k++) { gam += bflo(mlh[k * 1536 + mcol]); sbm += bflo(mlh[k * 1536 + 512 + mcol]); cb1 += bflo(mlh[k * 1536 + 1024 + mcol]); }
      sbm = rbf(sbm); cb1 = rbf(1.f + cb1);
    }
    LAS u16* cs16 = (LAS u16*)(SM + 10240);
    LAS float* gp64 = (LAS float*)(SM + 12288);
    cs16[mcol] = __builtin_bit_cast(u16, (__bf16)cb1); cs16[512 + mcol] = __builtin_bit_cast(u16, (__bf16)sbm);
    LGWAIT0();
    bf16x8 auxf[2];
    {
      const bf16x8 ones = {0x3F80, 0x3F80, 0x3F80, 0x3F80, 0x3F80, 0x3F80, 0x3F80, 0x3F80};
#pragma unroll
      for (int i = 0; i < 2; i++) { const bf16x8 v = *(const LAS bf16x8*)(cs16 + ((n - 1) & 1) * 512 + wave * 64 + i * 32 + q * 8); auxf[i] = n == 0 ? ones : v; }
      f32x4 k0 = MFMA(auxf[0], w1f[0], ((f32x4){0, 0, 0, 0})), k1 = MFMA(auxf[0], w1f[1], ((f32x4){0, 0, 0, 0})), kg = MFMA(auxf[0], auxf[0], ((f32x4){0, 0, 0, 0}));
      k0 = MFMA(auxf[1], w1f[2], k0); k1 = MFMA(auxf[1], w1f[3], k1); kg = MFMA(auxf[1], auxf[1], kg);
      if (lane < 16) {
        LAS float* g = (LAS float*)(L + 32768) + wave * 192;
        g[n] = k0[0]; g[16 + n] = k1[0]; g[32 + n] = kg[0]; g[64 + n] = k0[1]; g[80 + n] = k1[1]; g[96 + n] = kg[1]; g[128 + n] = k0[2]; g[144 + n] = k1[2]; g[160 + n] = kg[2];
      }
    }
    bar();
    float Ow = 0.f, Cw = 0.f, Sw = 0.f, Kc = 0.f, Ks = 0.f, Kc2 = 0.f, Kcs = 0.f, Ks2 = 0.f;
    if (wave == 0) {
      const LAS float* g = (const LAS float*)(L + 32768);
      float o = 0.f, c = 0.f, sg = 0.f;
#pragma unroll
      for (int w = 0; w < 8; w++) { o += g[w * 192 + (lane < 48 ? lane : 0)]; c += g[w * 192 + 64 + (lane < 48 ? lane : 0)]; sg += g[w * 192 + 128 + (lane < 48 ? lane : 0)]; }
      Ow = o; Cw = c; Sw = sg;
      Kc = RL(o, 33); Ks = RL(o, 34); Kc2 = RL(c, 33); Kcs = RL(c, 34); Ks2 = RL(sg, 34);
    }
    asm volatile("" : "+v"(gam), "+v"(cb1), "+v"(Ow), "+v"(Cw), "+v"(Sw));
    {
      v4u v; int spins = 0;
      const int off = S5_OFF + xc * 8192 + (lane >> 3) * 1024 + wave * 128 + (lane & 7) * 16;
      for (;;) { CBAR(); v = ld16(rs, off); if (__all(mx4(v) != SENT) || ++spins > SPIN_CAP) break; if ((spins & 15) == 0) REPUB(); }
      const float b0 = red32(bflo(v.x), bflo(v.z)), b1x = red32(bfhi(v.x), bfhi(v.z)), b2x = red32(bflo(v.y), bflo(v.w)), b3 = red32(bfhi(v.y), bfhi(v.w));
      const float c0 = red16(b0, b2x), c1 = red16(b1x, b3);
      const bool up = lane & 8;
      const float keep = up ? c1 : c0, send = up ? c0 : c1;
      const float ao = keep + dppf<0x128>(send);
      const __bf16 xb = (__bf16)(xme + gam * ao);
      cs16[mcol] = __builtin_bit_cast(u16, (__bf16)((float)xb * cb1)); cs16[512 + mcol] = __builtin_bit_cast(u16, xb);
      LGWAIT0();
      const bf16x8 y0 = *(const LAS bf16x8*)(cs16 + (n & 1) * 512 + wave * 64 + q * 8), y1 = *(const LAS bf16x8*)(cs16 + (n & 1) * 512 + wave * 64 + 32 + q * 8);
      f32x4 z0 = MFMA(y0, w1f[0], ((f32x4){0, 0, 0, 0})), z1 = MFMA(y0, w1f[1], ((f32x4){0, 0, 0, 0})), ax = MFMA(y0, auxf[0], ((f32x4){0, 0, 0, 0})), gr = MFMA(y0, y0, ((f32x4){0, 0, 0, 0}));
      z0 = MFMA(y1, w1f[2], z0); z1 = MFMA(y1, w1f[3], z1); ax = MFMA(y1, auxf[1], ax); gr = MFMA(y1, y1, gr);
      const float lastq = n == 0 ? ax[1] : n == 1 ? gr[1] : gr[0];
      gp64[wave * 64 + lane] = q == 0 ? z0[0] : q == 1 ? z1[0] : q == 2 ? ax[0] : lastq;
    }
    bar();
    if (wave == 0) {
      float a = 0;
#pragma unroll
      for (int w = 0; w < 8; w++) a += gp64[w * 64 + lane];
      const float B1 = RL(a, 32), B3 = RL(a, 33), B4 = RL(a, 34), A1 = RL(a, 48), A2 = RL(a, 49), B2 = RL(a, 50);
      const float mu = A1 * (1.f / 512.f), rr = __builtin_amdgcn_rsqf(fmaxf(A2 * (1.f / 512.f) - mu * mu, 0.f) + 1e-5f);
      const float m2 = (rr * (B1 - mu * Kc) + Ks) * (1.f / 512.f);
      const float e2 = (rr * rr * (B2 - 2.f * mu * B3 + mu * mu * Kc2) + 2.f * rr * (B4 - mu * Kcs) + Ks2) * (1.f / 512.f);
      const float r2 = __builtin_amdgcn_rsqf(fmaxf(e2 - m2 * m2, 0.f) + 1e-5f);
      const float zz = r2 * (rr * (a - mu * Cw) + Sw - m2 * Ow);
      const float z = zz + b1v;
      const float u = z * (0.7978845608028654f + 0.035677408136300125f * z * z);
      const float gl = z * __builtin_amdgcn_rcpf(1.f + __builtin_amdgcn_exp2f(-2.885390081777927f * u));
      const unsigned d = canon(pk2(gl, dppf<0x101>(gl)));
      if (lane < 32 && !(lane & 1)) ((LAS unsigned*)y16)[lane >> 1] = d;
      LGWAIT0();
      const v4u o = *(const LAS v4u*)((LAS unsigned*)y16 + (lane & 3) * 4);
      if (lane < 32) st16(rs, S6_OFF + (lane >> 2) * 6144 + gi * 128 + (lane & 3) * 16, o);
    }
    if (ti == 0 && wave == 1 && lane < 8) { v4u o = {1, 1, 1, 1}; st16(rs, DN_OFF + lane * 128, o); }
  } else {
    const int e = r - 16, split = 4 * xc + (e & 3), hg = e >> 2;
    const int sfoff = SF_OFF + (8 + xc) * 8192 + lane * 128;
    LAS unsigned char* qa = SM;
    LAS unsigned char* Pm = SM + 16896;
    LAS float* wmax = (LAS float*)(SM + 21504);
    LAS float* wsum = wmax + 128;
    unsigned snapf = 0;
    if (wave == 0) snapf = ld8(rs, sfoff).y;
    bar();
    DELAY(AD_DELAY);
    CBAR();
#pragma unroll
    for (int i = 0; i < 16; i++) {
      const int key = ((i * 8 + wave) + hg * 64) & 127;
      dma16(ckv + (size_t)(split * 128 + key) * 512 + ((lane ^ swz(key)) * 8), L + key * 1024);
    }
    VMWAIT0();
    bar();
    bf16x8 ka[16], va[16];
    {
      const int key = wave * 16 + n, f2 = swz(key);
#pragma unroll
      for (int s2 = 0; s2 < 16; s2++) ka[s2] = *(const LAS bf16x8*)(L + key * 1024 + (((4 * s2 + q) ^ f2) * 16));
#pragma unroll
      for (int i = 0; i < 16; i++) va[i] = trfrag<1024, true>(L, 32 * (i & 3), wave * 8 + 2 * (i >> 2), lane);
    }
    if (wave == 0) {
      int spins = 0; bool ok = false;
      for (;;) { CBAR(); if (!ok) ok = ld8(rs, sfoff).y != snapf; if (__all(ok) || ++spins > SPIN_CAP) break; __builtin_amdgcn_s_sleep(1); }
    }
    bar();
    {
      const int off = S2_OFF + xc * 32768 + hg * 16384 + t * 16;
      v4u v0, v1; int spins = 0;
      for (;;) {
        CBAR();
        v0 = ld16(rs, off); v1 = ld16(rs, off + 8192);
        if (__all(max(mx4(v0), mx4(v1)) != SENT) || ++spins > SPIN_CAP) break;
      }
      *(LAS v4u*)(qa + wave * 1056 + lane * 16) = v0;
      *(LAS v4u*)(qa + (8 + wave) * 1056 + lane * 16) = v1;
    }
    bar();
    f32x4 sc = {0, 0, 0, 0};
    {
      bf16x8 qf[16];
#pragma unroll
      for (int s2 = 0; s2 < 16; s2++) qf[s2] = *(const LAS bf16x8*)(qa + n * 1056 + s2 * 64 + q * 16);
      SB();
      f32x4 sc2 = {0, 0, 0, 0};
#pragma unroll
      for (int s2 = 0; s2 < 8; s2++) { sc = MFMA(ka[2 * s2], qf[2 * s2], sc); sc2 = MFMA(ka[2 * s2 + 1], qf[2 * s2 + 1], sc2); }
      sc += sc2;
    }
    float mx = fmaxf(fmaxf(sc[0], sc[1]), fmaxf(sc[2], sc[3]));
    mx = max16(mx); mx = max32(mx);
    if (lane < 16) wmax[lane * 8 + wave] = mx;
    bar();
    {
      f32x4 m0 = *(LAS f32x4*)(wmax + n * 8), m1 = *(LAS f32x4*)(wmax + n * 8 + 4);
      mx = fmaxf(fmaxf(fmaxf(m0[0], m0[1]), fmaxf(m0[2], m0[3])), fmaxf(fmaxf(m1[0], m1[1]), fmaxf(m1[2], m1[3])));
    }
    const float p0 = __builtin_amdgcn_exp2f(sc[0] - mx), p1 = __builtin_amdgcn_exp2f(sc[1] - mx), p2 = __builtin_amdgcn_exp2f(sc[2] - mx), p3 = __builtin_amdgcn_exp2f(sc[3] - mx);
    { v2u o = {pk2(p0, p1), pk2(p2, p3)}; *(LAS v2u*)(Pm + n * 288 + (wave * 16 + q * 4) * 2) = o; }
    float ps = (p0 + p1) + (p2 + p3);
    ps = red16(ps, ps); ps = red32(ps, ps);
    if (lane < 16) wsum[lane * 8 + wave] = ps;
    bar();
    {
      if (wave == 7) {
        f32x4 s0 = *(LAS f32x4*)(wsum + n * 8), s1 = *(LAS f32x4*)(wsum + n * 8 + 4);
        const float l = ((s0[0] + s0[1]) + (s0[2] + s0[3])) + ((s1[0] + s1[1]) + (s1[2] + s1[3]));
        v2u o2 = {canon(__float_as_uint(mx)), canon(__float_as_uint(l))};
        if (lane < 16) st8(rs, S3_OFF + ((hg * 16 + lane) * 32 + split) * S3R + 1024, o2);
      }
      bf16x8 bp[4];
#pragma unroll
      for (int ks = 0; ks < 4; ks++) bp[ks] = *(const LAS bf16x8*)(Pm + n * 288 + ks * 64 + q * 16);
      SB();
      f32x4 acc[4];
#pragma unroll
      for (int T = 0; T < 4; T++) {
        acc[T] = (f32x4){0, 0, 0, 0};
#pragma unroll
        for (int ks = 0; ks < 4; ks++) acc[T] = MFMA(va[T * 4 + ks], bp[ks], acc[T]);
      }
      unsigned d0[4], d1[4];
#pragma unroll
      for (int T = 0; T < 4; T++) { d0[T] = canon(pk2(acc[T][0], acc[T][1])); d1[T] = canon(pk2(acc[T][2], acc[T][3])); }
      auto r0 = __builtin_amdgcn_permlane16_swap(d0[0], d0[1], false, false);
      auto r1 = __builtin_amdgcn_permlane16_swap(d1[0], d1[1], false, false);
      auto r2 = __builtin_amdgcn_permlane16_swap(d0[2], d0[3], false, false);
      auto r3 = __builtin_amdgcn_permlane16_swap(d1[2], d1[3], false, false);
      v4u oa = {r0[0], r1[0], r0[1], r1[1]}, ob = {r2[0], r3[0], r2[1], r3[1]};
      const v4u obs = {dppu<0x128>(ob.x), dppu<0x128>(ob.y), dppu<0x128>(ob.z), dppu<0x128>(ob.w)};
      const bool lo = n < 8;
      const int o3b = S3_OFF + ((hg * 16 + (n & 7)) * 32 + split) * S3R + (wave * 64 + (q & 1) * 16 + (q & 2) * 4) * 2;
      st16(rs, o3b + (lo ? 0 : 64), lo ? oa : obs);
      st16(rs, o3b + 8 * 32 * S3R + (lo ? 64 : 0), lo ? obs : oa);
    }
  }
}

extern "C" void kernel_launch(void* const* d_in, const int* in_sizes, int n_in, void* d_out, int out_size, void* d_ws, size_t ws_size, hipStream_t stream) {
  (void)in_sizes; (void)n_in; (void)out_size; (void)ws_size;
  (void)hipFuncSetAttribute((const void*)fused_kernel, hipFuncAttributeMaxDynamicSharedMemorySize, L_TOTAL);
  fused_kernel<<<256, NT, L_TOTAL, stream>>>((const u16*)d_in[0], (const u16*)d_in[1], (const u16*)d_in[2], (const u16*)d_in[3], (const u16*)d_in[4], (const u16*)d_in[5],
                                             (const u16*)d_in[7], (const u16*)d_in[8], (const u16*)d_in[9], (const u16*)d_in[10], (const u16*)d_in[11], (const u16*)d_in[12],
                                             (u16*)d_out, (unsigned char*)d_ws);
}
```
